```python
import torch

def _softmax(x, axis=-1):
    return torch.softmax(x, dim=axis)
def _gelu(x):
    return torch.nn.functional.gelu(x, approximate="tanh")
def _silu(x):
    return torch.nn.functional.silu(x)
def _sigmoid(x):
    return torch.sigmoid(x)
def _layernorm(x, axis=-1, eps=1e-5):
    m = x.mean(dim=axis, keepdim=True)
    v = x.var(dim=axis, keepdim=True, unbiased=False)
    return (x - m) / torch.sqrt(v + eps)
def _scatter_add(tgt, idx, src, axis=0):
    return tgt.clone().index_add_(axis, idx, src)
def _segment_sum(src, seg, n):
    out = torch.zeros((n,) + src.shape[1:], dtype=src.dtype)
    return out.index_add_(0, seg, src)
def _pad(x, pw):
    flat = tuple(v for pair in reversed(pw) for v in pair)
    return torch.nn.functional.pad(x, flat)

kernel_name = 'standalone__mha_block_cached__near_ridge'

def setup_inputs():

    _w = (lambda a: (lambda t: t.to(torch.bfloat16) if t.is_floating_point() else t)(torch.as_tensor(a)))
    B, Sq, D = 32, 144, 512
    H, Sk, Dh = 32, 512, 128
    return {
        "x": _w((lambda *s: torch.randn(*s, dtype=torch.bfloat16) * 0.8)(B, Sq, D)),
        "k_cache": _w((lambda *s: torch.randn(*s, dtype=torch.bfloat16) * 0.8)(B, H, Sk, Dh)),
        "v_cache": _w((lambda *s: torch.randn(*s, dtype=torch.bfloat16) * 0.05)(B, H, Sk, Dh)),
        "wq": _w((lambda *s: torch.randn(*s, dtype=torch.bfloat16) * 0.8)(D, H * Dh) * (D ** -0.5)),
        "wo": _w((lambda *s: torch.randn(*s, dtype=torch.bfloat16) * 0.05)(H * Dh, D) * ((H * Dh) ** -0.5)),
    }

def reference(x, k_cache, v_cache, wq, wo):
    B, Sq, D = x.shape
    H, Dh = 32, 128
    q = (lambda x, *d: x.permute(*d))((x @ wq).reshape(B, Sq, H, Dh), 0, 2, 1, 3)
    scores = torch.einsum("bhqd,bhkd->bhqk", q, k_cache) * (Dh ** -0.5)
    p = _softmax(scores, axis=-1)
    o = torch.einsum("bhqk,bhkd->bhqd", p, v_cache)
    return (lambda x, *d: x.permute(*d))(o, 0, 2, 1, 3).reshape(B, Sq, H * Dh) @ wo
```

```cpp
#include <hip/hip_runtime.h>
#include <hip/hip_bf16.h>

typedef __attribute__((ext_vector_type(8))) short bf16x8;
typedef __attribute__((ext_vector_type(4))) short s16x4;
typedef __attribute__((ext_vector_type(16))) float f32x16;
typedef __attribute__((ext_vector_type(4))) float f32x4;
typedef __attribute__((ext_vector_type(4))) unsigned u32x4;
typedef __attribute__((ext_vector_type(2))) unsigned u32x2;
typedef __attribute__((address_space(3))) s16x4 lds_s16x4;
typedef __attribute__((address_space(3))) char lds_char;

constexpr int NB = 32, SQ = 144, DM = 512, NH = 32, SK = 512, DH = 128;
constexpr int HD = NH * DH;

#define MFMA32(a, b, c) __builtin_amdgcn_mfma_f32_32x32x16_bf16((a), (b), (c), 0, 0, 0)
#define MFMA16(a, b, c) __builtin_amdgcn_mfma_f32_16x16x32_bf16((a), (b), (c), 0, 0, 0)
#define MFMA16K16(a, b, c) __builtin_amdgcn_mfma_f32_16x16x16bf16_1k((a), (b), (c), 0, 0, 0)

__device__ __forceinline__ unsigned cvtpk(float lo, float hi) {
  unsigned r;
  asm volatile("v_cvt_pk_bf16_f32 %0, %1, %2" : "=v"(r) : "v"(lo), "v"(hi));
  return r;
}
__device__ __forceinline__ s16x4 tr_read(const char* smem_base, int off) {
  return __builtin_amdgcn_ds_read_tr16_b64_v4i16((lds_s16x4*)((lds_char*)smem_base + off));
}
__device__ __forceinline__ bf16x8 cat8(s16x4 lo, s16x4 hi) {
  return __builtin_shufflevector(lo, hi, 0, 1, 2, 3, 4, 5, 6, 7);
}
__device__ __forceinline__ void glds16(const void* g, char* l) {
  __builtin_amdgcn_global_load_lds((const unsigned*)g, (unsigned*)l, 16, 0, 0);
}
__device__ __forceinline__ float xmax32(float v) {
  auto rr = __builtin_amdgcn_permlane32_swap(__float_as_uint(v), __float_as_uint(v), false, false);
  return fmaxf(__uint_as_float(rr[0]), __uint_as_float(rr[1]));
}
__device__ __forceinline__ float xsum32(float v) {
  auto rr = __builtin_amdgcn_permlane32_swap(__float_as_uint(v), __float_as_uint(v), false, false);
  return __uint_as_float(rr[0]) + __uint_as_float(rr[1]);
}
__device__ __forceinline__ float xmax16(float v) {
  auto rr = __builtin_amdgcn_permlane16_swap(__float_as_uint(v), __float_as_uint(v), false, false);
  return fmaxf(__uint_as_float(rr[0]), __uint_as_float(rr[1]));
}
__device__ __forceinline__ float xsum16(float v) {
  auto rr = __builtin_amdgcn_permlane16_swap(__float_as_uint(v), __float_as_uint(v), false, false);
  return __uint_as_float(rr[0]) + __uint_as_float(rr[1]);
}
#define WAIT_VM0() asm volatile("s_waitcnt vmcnt(0)" ::: "memory")
#define RAW_BAR() __builtin_amdgcn_s_barrier()

constexpr int A_BUFA = 20480;
constexpr int A_BUFB = 16384;
constexpr int A_OFF_A = 0;
constexpr int A_OFF_B = 2 * A_BUFA;
constexpr int A_OFF_Q2 = A_OFF_B + 2 * A_BUFB;
constexpr int A_LDS = A_OFF_Q2 + 4096;

template <int N> struct IC { static constexpr int value = N; };
#define OPAQUE(v) asm volatile("" : "+v"(v))

__global__ __launch_bounds__(256, 2) void attn_fused_kernel(
    const short* __restrict__ x, const short* __restrict__ kc, const short* __restrict__ vc,
    const short* __restrict__ wq, short* __restrict__ og) {
  extern __shared__ __attribute__((aligned(16))) char smem[];
  const int tid = threadIdx.x, lane = tid & 63;
  const int w = __builtin_amdgcn_readfirstlane(tid >> 6);
  const int pidx = (blockIdx.x & 7) * 128 + (blockIdx.x >> 3);
  const int b = pidx >> 5, h = pidx & 31;

  const char* xb = (const char*)(x + (size_t)b * SQ * DM);
  const char* wqh = (const char*)(wq + h * DH);
  const char* kb = (const char*)(kc + (size_t)pidx * SK * DH);
  const char* vb = (const char*)(vc + (size_t)pidx * SK * DH);

  const int l31 = lane & 31, hh = lane >> 5, l15 = lane & 15, g = lane >> 4;
  const int qp = l15 >> 2, p4 = lane & 3, blk = (lane >> 4) & 1;

  f32x16 acc[4];
#pragma unroll
  for (int i = 0; i < 4; ++i) acc[i] = f32x16{};
  f32x4 acc2[8];
#pragma unroll
  for (int i = 0; i < 8; ++i) acc2[i] = f32x4{};

  int ksrc = (16 * w + g) * 256 + ((l15 ^ g) << 4);
  int vsrc = (16 * w + g) * 256 + ((l15 ^ (g << 2)) << 4);
  auto stage_kv = [&](int t, int buf) {
    char* la = smem + A_OFF_A + buf * A_BUFA + w * 4096 + lane * 16;
    char* lb = smem + A_OFF_B + buf * A_BUFB + w * 4096 + lane * 16;
    int ko = ksrc, vo = vsrc;
    OPAQUE(ko); OPAQUE(vo);
    const char* kt = kb + t * 16384;
    const char* vt = vb + t * 16384;
#pragma unroll
    for (int ii = 0; ii < 4; ++ii) glds16(kt + ((ko ^ (ii << 6)) + ii * 1024), la + ii * 1024);
#pragma unroll
    for (int ii = 0; ii < 4; ++ii) glds16(vt + (vo + ii * 1024), lb + ii * 1024);
  };

  {
    int xsrc = lane;
    auto stage_q = [&](int t, int buf) {
      char* la = smem + A_OFF_A + buf * A_BUFA + w * 5120 + lane * 16;
      char* lb = smem + A_OFF_B + buf * A_BUFB + w * 4096 + lane * 16;
      int ln = xsrc;
      OPAQUE(ln);
      const char* xt = xb + t * 128;
      const char* wt = wqh + (size_t)t * 64 * HD * 2;
#pragma unroll
      for (int ii = 0; ii < 5; ++ii) {
        int r = 8 * (w * 5 + ii) + (ln >> 3), cp = ln & 7;
        int c = cp ^ ((r >> 1) & 7);
        int rs = r < SQ ? r : SQ - 1;
        glds16(xt + (rs * 1024 + c * 16), la + ii * 1024);
      }
      int wo0 = (16 * w + (ln >> 4)) * (HD * 2) + (((ln & 15) ^ ((ln >> 4) << 2)) << 4);
#pragma unroll
      for (int ii = 0; ii < 4; ++ii) glds16(wt + (wo0 + ii * 4 * HD * 2), lb + ii * 1024);
    };
    int xa = A_OFF_A + (32 * w + l31) * 128 + ((hh ^ ((l31 >> 1) & 7)) << 4);
    int wa = A_OFF_B + (8 * hh + qp) * 256 + (((2 * blk + (p4 >> 1)) ^ (qp << 2)) << 4) + 8 * (p4 & 1);
    int xa2 = A_OFF_A + (128 + l15) * 128 + ((g ^ ((l15 >> 1) & 7)) << 4);
    int wa2 = A_OFF_B + (8 * g + qp) * 256 + (((4 * w + (p4 >> 1)) ^ (qp << 2)) << 4) + 8 * (p4 & 1);
    auto body = [&](auto BC, int t) {
      constexpr int buf = decltype(BC)::value;
      WAIT_VM0();
      RAW_BAR();
      if (t < 7) stage_q(t + 1, buf ^ 1);
      else stage_kv(0, buf ^ 1);
      int xa_ = xa, wa_ = wa, xa2_ = xa2, wa2_ = wa2;
      OPAQUE(xa_); OPAQUE(wa_); OPAQUE(xa2_); OPAQUE(wa2_);
#pragma unroll
      for (int s = 0; s < 4; ++s) {
        bf16x8 xf = *(const bf16x8*)(smem + (xa_ ^ (s << 5)) + buf * A_BUFA);
#pragma unroll
        for (int dt = 0; dt < 4; ++dt) {
          int off = (wa_ ^ (dt << 6)) + buf * A_BUFB + s * 4096;
          s16x4 lo = tr_read(smem, off);
          s16x4 hi = tr_read(smem, off + 1024);
          acc[dt] = MFMA32(cat8(lo, hi), xf, acc[dt]);
        }
      }
#pragma unroll
      for (int s2 = 0; s2 < 2; ++s2) {
        bf16x8 xf = *(const bf16x8*)(smem + (xa2_ ^ (s2 << 6)) + buf * A_BUFA);
#pragma unroll
        for (int e = 0; e < 2; ++e) {
          int off = (wa2_ ^ (e << 5)) + buf * A_BUFB + s2 * 8192;
          s16x4 lo = tr_read(smem, off);
          s16x4 hi = tr_read(smem, off + 1024);
          acc2[e] = MFMA16(cat8(lo, hi), xf, acc2[e]);
        }
      }
    };
    stage_q(0, 0);
    for (int t = 0; t < 8; t += 2) {
      body(IC<0>{}, t);
      body(IC<1>{}, t + 1);
    }
  }
  bf16x8 qf[8];
#pragma unroll
  for (int dt = 0; dt < 4; ++dt) {
#pragma unroll
    for (int s = 0; s < 2; ++s) {
      unsigned a0 = cvtpk(acc[dt][8 * s + 0], acc[dt][8 * s + 1]);
      unsigned a1 = cvtpk(acc[dt][8 * s + 2], acc[dt][8 * s + 3]);
      unsigned b0 = cvtpk(acc[dt][8 * s + 4], acc[dt][8 * s + 5]);
      unsigned b1 = cvtpk(acc[dt][8 * s + 6], acc[dt][8 * s + 7]);
      auto r0 = __builtin_amdgcn_permlane32_swap(a0, b0, false, false);
      auto r1 = __builtin_amdgcn_permlane32_swap(a1, b1, false, false);
      u32x4 wv = {r0[0], r1[0], r0[1], r1[1]};
      qf[2 * dt + s] = __builtin_bit_cast(bf16x8, wv);
    }
  }
  {
#pragma unroll
    for (int e = 0; e < 2; ++e) {
      int dd = 2 * w + e;
      u32x2 pk = {cvtpk(acc2[e][0], acc2[e][1]), cvtpk(acc2[e][2], acc2[e][3])};
      int ch = (2 * dd + (g >> 1)) ^ l15;
      *(u32x2*)(smem + A_OFF_Q2 + l15 * 256 + (ch << 4) + 8 * (g & 1)) = pk;
    }
  }
#pragma unroll
  for (int i = 0; i < 4; ++i) acc[i] = f32x16{};
#pragma unroll
  for (int i = 0; i < 8; ++i) acc2[i] = f32x4{};

  constexpr float CS = 0.088388347648318440f * 1.4426950408889634f;
  float m1 = -1e30f, l1 = 0.f, m2 = -1e30f, l2 = 0.f;
  {
    int ka = A_OFF_A + l31 * 256 + ((hh ^ l15) << 4);
    int ka2 = A_OFF_A + (16 * w + l15) * 256 + ((g ^ l15) << 4);
    int q2a = A_OFF_Q2 + l15 * 256 + ((g ^ l15) << 4);
    int va = A_OFF_B + (4 * hh + qp) * 256 + (((2 * blk + (p4 >> 1)) ^ (qp << 2)) << 4) + 8 * (p4 & 1);
    int va2 = A_OFF_B + (16 * w + 4 * g + qp) * 256 + (((p4 >> 1) ^ (qp << 2)) << 4) + 8 * (p4 & 1);
    auto body = [&](auto BC, int t) {
      constexpr int buf = decltype(BC)::value;
      asm volatile("s_waitcnt vmcnt(0) lgkmcnt(0)" ::: "memory");
      RAW_BAR();
      if (t < 7) stage_kv(t + 1, buf ^ 1);
      int ka_ = ka, ka2_ = ka2, q2a_ = q2a, va_ = va, va2_ = va2;
      OPAQUE(ka_); OPAQUE(ka2_); OPAQUE(q2a_); OPAQUE(va_); OPAQUE(va2_);
      f32x16 s0 = f32x16{}, s1 = f32x16{};
#pragma unroll
      for (int ks = 0; ks < 8; ++ks) {
        int off = (ka_ ^ (ks << 5)) + buf * A_BUFA;
        bf16x8 k0 = *(const bf16x8*)(smem + off);
        bf16x8 k1 = *(const bf16x8*)(smem + off + 8192);
        s0 = MFMA32(k0, qf[ks], s0);
        s1 = MFMA32(k1, qf[ks], s1);
      }
      f32x4 sr = f32x4{};
#pragma unroll
      for (int ks = 0; ks < 4; ++ks) {
        bf16x8 kk = *(const bf16x8*)(smem + (ka2_ ^ (ks << 6)) + buf * A_BUFA);
        bf16x8 q2 = *(const bf16x8*)(smem + (q2a_ ^ (ks << 6)));
        sr = MFMA16(kk, q2, sr);
      }
      float tmax = s0[0];
#pragma unroll
      for (int r = 1; r < 16; ++r) tmax = fmaxf(tmax, s0[r]);
#pragma unroll
      for (int r = 0; r < 16; ++r) tmax = fmaxf(tmax, s1[r]);
      tmax = xmax32(tmax);
      float mn = fmaxf(m1, tmax);
      float alpha = __builtin_amdgcn_exp2f((m1 - mn) * CS);
      m1 = mn;
      float mc = -mn * CS;
      float ps = 0.f;
#pragma unroll
      for (int r = 0; r < 16; ++r) { s0[r] = __builtin_amdgcn_exp2f(fmaf(s0[r], CS, mc)); ps += s0[r]; }
#pragma unroll
      for (int r = 0; r < 16; ++r) { s1[r] = __builtin_amdgcn_exp2f(fmaf(s1[r], CS, mc)); ps += s1[r]; }
      l1 = l1 * alpha + ps;
      if (!__all(alpha == 1.f)) {
#pragma unroll
        for (int dt = 0; dt < 4; ++dt)
#pragma unroll
          for (int r = 0; r < 16; ++r) acc[dt][r] *= alpha;
      }
      bf16x8 pf[4];
#pragma unroll
      for (int s = 0; s < 2; ++s) {
        u32x4 a = {cvtpk(s0[8 * s + 0], s0[8 * s + 1]), cvtpk(s0[8 * s + 2], s0[8 * s + 3]),
                   cvtpk(s0[8 * s + 4], s0[8 * s + 5]), cvtpk(s0[8 * s + 6], s0[8 * s + 7])};
        pf[s] = __builtin_bit_cast(bf16x8, a);
        u32x4 c = {cvtpk(s1[8 * s + 0], s1[8 * s + 1]), cvtpk(s1[8 * s + 2], s1[8 * s + 3]),
                   cvtpk(s1[8 * s + 4], s1[8 * s + 5]), cvtpk(s1[8 * s + 6], s1[8 * s + 7])};
        pf[2 + s] = __builtin_bit_cast(bf16x8, c);
      }
      float t2 = fmaxf(fmaxf(sr[0], sr[1]), fmaxf(sr[2], sr[3]));
      t2 = xmax16(t2);
      t2 = xmax32(t2);
      float mn2 = fmaxf(m2, t2);
      float alpha2 = __builtin_amdgcn_exp2f((m2 - mn2) * CS);
      m2 = mn2;
      float mc2 = -mn2 * CS;
      float e0 = __builtin_amdgcn_exp2f(fmaf(sr[0], CS, mc2));
      float e1 = __builtin_amdgcn_exp2f(fmaf(sr[1], CS, mc2));
      float e2 = __builtin_amdgcn_exp2f(fmaf(sr[2], CS, mc2));
      float e3 = __builtin_amdgcn_exp2f(fmaf(sr[3], CS, mc2));
      l2 = l2 * alpha2 + (e0 + e1) + (e2 + e3);
      if (!__all(alpha2 == 1.f)) {
#pragma unroll
        for (int dd = 0; dd < 8; ++dd)
#pragma unroll
          for (int r = 0; r < 4; ++r) acc2[dd][r] *= alpha2;
      }
      u32x2 p2u = {cvtpk(e0, e1), cvtpk(e2, e3)};
      s16x4 p2 = __builtin_bit_cast(s16x4, p2u);
#pragma unroll
      for (int s = 0; s < 4; ++s) {
#pragma unroll
        for (int dt = 0; dt < 4; ++dt) {
          int off = (va_ ^ (dt << 6)) + buf * A_BUFB + s * 4096;
          s16x4 lo = tr_read(smem, off);
          s16x4 hi = tr_read(smem, off + 2048);
          acc[dt] = MFMA32(cat8(lo, hi), pf[s], acc[dt]);
        }
      }
#pragma unroll
      for (int dd = 0; dd < 8; ++dd) {
        s16x4 vv = tr_read(smem, (va2_ ^ (dd << 5)) + buf * A_BUFB);
        acc2[dd] = MFMA16K16(vv, p2, acc2[dd]);
      }
    };
    for (int t = 0; t < 8; t += 2) {
      body(IC<0>{}, t);
      body(IC<1>{}, t + 1);
    }
  }

  int tz = threadIdx.x;
  OPAQUE(tz);
  {
    const int l31 = tz & 31, hh = (tz >> 5) & 1;
    float lt = xsum32(l1);
    float inv = 1.f / lt;
    short* orow = og + (size_t)(b * SQ + 32 * w + l31) * HD + h * DH + 4 * hh;
#pragma unroll
    for (int dt = 0; dt < 4; ++dt)
#pragma unroll
      for (int i = 0; i < 4; ++i) {
        u32x2 pk = {cvtpk(acc[dt][4 * i] * inv, acc[dt][4 * i + 1] * inv),
                    cvtpk(acc[dt][4 * i + 2] * inv, acc[dt][4 * i + 3] * inv)};
        *(u32x2*)(orow + 32 * dt + 8 * i) = pk;
      }
  }
  __syncthreads();
  {
    const int l15 = tz & 15, g = (tz >> 4) & 3;
    float lt2 = xsum32(xsum16(l2));
    float* pw = (float*)(smem + w * 8192);
#pragma unroll
    for (int dd = 0; dd < 8; ++dd) *(f32x4*)(pw + l15 * 128 + 16 * dd + 4 * g) = acc2[dd];
    float* st = (float*)(smem + 32768);
    if (g == 0) { st[w * 32 + l15] = m2; st[w * 32 + 16 + l15] = lt2; }
  }
  __syncthreads();
  {
    const int q = tz >> 4, d0 = (tz & 15) * 8;
    const float* st = (const float*)(smem + 32768);
    float mm = fmaxf(fmaxf(st[q], st[32 + q]), fmaxf(st[64 + q], st[96 + q]));
    float o[8] = {0, 0, 0, 0, 0, 0, 0, 0};
    float ls = 0.f;
#pragma unroll
    for (int ww = 0; ww < 4; ++ww) {
      float sc = __builtin_amdgcn_exp2f((st[ww * 32 + q] - mm) * CS);
      ls += st[ww * 32 + 16 + q] * sc;
      const float* pw = (const float*)(smem + ww * 8192) + q * 128 + d0;
      f32x4 a = *(const f32x4*)pw, c = *(const f32x4*)(pw + 4);
#pragma unroll
      for (int j = 0; j < 4; ++j) { o[j] += a[j] * sc; o[4 + j] += c[j] * sc; }
    }
    float inv = 1.f / ls;
    u32x4 pk = {cvtpk(o[0] * inv, o[1] * inv), cvtpk(o[2] * inv, o[3] * inv),
                cvtpk(o[4] * inv, o[5] * inv), cvtpk(o[6] * inv, o[7] * inv)};
    *(u32x4*)(og + (size_t)(b * SQ + 128 + q) * HD + h * DH + d0) = pk;
  }
}

constexpr int B_NST = 4;
constexpr int B_BUFA = 20480;
constexpr int B_BUFB = 8192;
constexpr int B_STRIDE = B_BUFA + B_BUFB;
constexpr int B_LDS = B_NST * B_STRIDE;

__global__ __launch_bounds__(256, 1) void oproj_kernel(const short* __restrict__ og,
                                                        const short* __restrict__ wo,
                                                        short* __restrict__ out) {
  extern __shared__ __attribute__((aligned(16))) char smem[];
  const int tid = threadIdx.x, lane = tid & 63;
  const int w = __builtin_amdgcn_readfirstlane(tid >> 6);
  const int xg = blockIdx.x & 7, idx = blockIdx.x >> 3;
  const int b = xg * 4 + (idx >> 3), nt0 = idx & 7;
  const short* ob = og + (size_t)b * SQ * HD;
  const short* wob = wo + nt0 * 64;
  const int l31 = lane & 31, hh = lane >> 5, l15 = lane & 15, g = lane >> 4;
  const int qp = l15 >> 2, p4 = lane & 3, blk = (lane >> 4) & 1;

  auto stage = [&](int t, int buf) {
    char* la = smem + buf * B_STRIDE;
    char* lb = la + B_BUFA;
#pragma unroll
    for (int ii = 0; ii < 5; ++ii) {
      int i = w * 5 + ii;
      int r = 8 * i + (lane >> 3), cp = lane & 7;
      int c = cp ^ ((r >> 1) & 7);
      int rs = r < SQ ? r : SQ - 1;
      glds16(ob + (size_t)rs * HD + t * 64 + c * 8, la + i * 1024 + lane * 16);
    }
#pragma unroll
    for (int ii = 0; ii < 2; ++ii) {
      int i = w * 2 + ii;
      int r = 8 * i + (lane >> 3), cp = lane & 7;
      int c = cp ^ (((r >> 1) & 1) << 2);
      glds16(wob + (size_t)(t * 64 + r) * DM + c * 8, lb + i * 1024 + lane * 16);
    }
  };

  f32x16 acc[2] = {f32x16{}, f32x16{}};
  f32x4 acc2 = f32x4{};
  constexpr int NT = HD / 64;
  stage(0, 0);
  stage(1, 1);
  stage(2, 2);
  for (int t = 0; t < NT; ++t) {
    const int buf = t & 3;
    if (t < NT - 2) asm volatile("s_waitcnt vmcnt(14)" ::: "memory");
    else if (t == NT - 2) asm volatile("s_waitcnt vmcnt(7)" ::: "memory");
    else asm volatile("s_waitcnt vmcnt(0)" ::: "memory");
    RAW_BAR();
    if (t + 3 < NT) stage(t + 3, (t + 3) & 3);
    const char* la = smem + buf * B_STRIDE;
    const char* lb = la + B_BUFA;
    {
      const int r = 32 * w + l31;
#pragma unroll
      for (int s = 0; s < 4; ++s) {
        int c = 2 * s + hh;
        bf16x8 xf = *(const bf16x8*)(la + r * 128 + ((c ^ ((r >> 1) & 7)) << 4));
#pragma unroll
        for (int nt = 0; nt < 2; ++nt) {
          int row0 = 16 * s + 8 * hh + qp;
          int ch = (4 * nt + 2 * blk + (p4 >> 1)) ^ (((qp >> 1) & 1) << 2);
          int off = row0 * 128 + (ch << 4) + 8 * (p4 & 1);
          s16x4 lo = tr_read(lb, off);
          s16x4 hi = tr_read(lb, off + 4 * 128);
          acc[nt] = MFMA32(cat8(lo, hi), xf, acc[nt]);
        }
      }
    }
    {
      const int r = 128 + l15;
#pragma unroll
      for (int s2 = 0; s2 < 2; ++s2) {
        int c = 4 * s2 + g;
        bf16x8 xf = *(const bf16x8*)(la + r * 128 + ((c ^ ((r >> 1) & 7)) << 4));
        int row0 = 32 * s2 + 8 * g + qp;
        int ch = (2 * w + (p4 >> 1)) ^ (((qp >> 1) & 1) << 2);
        int off = row0 * 128 + (ch << 4) + 8 * (p4 & 1);
        s16x4 lo = tr_read(lb, off);
        s16x4 hi = tr_read(lb, off + 4 * 128);
        acc2 = MFMA16(cat8(lo, hi), xf, acc2);
      }
    }
  }
  {
    short* orow = out + (size_t)(b * SQ + 32 * w + l31) * DM + nt0 * 64 + 4 * hh;
#pragma unroll
    for (int nt = 0; nt < 2; ++nt)
#pragma unroll
      for (int i = 0; i < 4; ++i) {
        u32x2 pk = {cvtpk(acc[nt][4 * i], acc[nt][4 * i + 1]), cvtpk(acc[nt][4 * i + 2], acc[nt][4 * i + 3])};
        *(u32x2*)(orow + 32 * nt + 8 * i) = pk;
      }
    u32x2 pk = {cvtpk(acc2[0], acc2[1]), cvtpk(acc2[2], acc2[3])};
    *(u32x2*)(out + (size_t)(b * SQ + 128 + l15) * DM + nt0 * 64 + 16 * w + 4 * g) = pk;
  }
}

extern "C" void kernel_launch(void* const* d_in, const int* in_sizes, int n_in, void* d_out,
                              int out_size, void* d_ws, size_t ws_size, hipStream_t stream) {
  const short* x = (const short*)d_in[0];
  const short* kc = (const short*)d_in[1];
  const short* vc = (const short*)d_in[2];
  const short* wq = (const short*)d_in[3];
  const short* wo = (const short*)d_in[4];
  short* og = (short*)d_ws;
  short* out = (short*)d_out;
  hipFuncSetAttribute((const void*)attn_fused_kernel, hipFuncAttributeMaxDynamicSharedMemorySize, A_LDS);
  hipFuncSetAttribute((const void*)oproj_kernel, hipFuncAttributeMaxDynamicSharedMemorySize, B_LDS);
  attn_fused_kernel<<<NB * NH, 256, A_LDS, stream>>>(x, kc, vc, wq, og);
  oproj_kernel<<<256, 256, B_LDS, stream>>>(og, wo, out);
}
```

```cpp
#include <hip/hip_runtime.h>
#include <hip/hip_bf16.h>

typedef __attribute__((ext_vector_type(8))) short bf16x8;
typedef __attribute__((ext_vector_type(4))) short s16x4;
typedef __attribute__((ext_vector_type(16))) float f32x16;
typedef __attribute__((ext_vector_type(4))) float f32x4;
typedef __attribute__((ext_vector_type(4))) unsigned u32x4;
typedef __attribute__((ext_vector_type(2))) unsigned u32x2;
typedef __attribute__((address_space(3))) s16x4 lds_s16x4;
typedef __attribute__((address_space(3))) char lds_char;

constexpr int NB = 32, SQ = 144, DM = 512, NH = 32, SK = 512, DH = 128;
constexpr int HD = NH * DH;

#define MFMA32(a, b, c) __builtin_amdgcn_mfma_f32_32x32x16_bf16((a), (b), (c), 0, 0, 0)
#define MFMA16(a, b, c) __builtin_amdgcn_mfma_f32_16x16x32_bf16((a), (b), (c), 0, 0, 0)
#define MFMA16K16(a, b, c) __builtin_amdgcn_mfma_f32_16x16x16bf16_1k((a), (b), (c), 0, 0, 0)

__device__ __forceinline__ unsigned cvtpk(float lo, float hi) {
  unsigned r;
  asm volatile("v_cvt_pk_bf16_f32 %0, %1, %2" : "=v"(r) : "v"(lo), "v"(hi));
  return r;
}
__device__ __forceinline__ s16x4 tr_read(const char* smem_base, int off) {
  return __builtin_amdgcn_ds_read_tr16_b64_v4i16((lds_s16x4*)((lds_char*)smem_base + off));
}
__device__ __forceinline__ bf16x8 cat8(s16x4 lo, s16x4 hi) {
  return __builtin_shufflevector(lo, hi, 0, 1, 2, 3, 4, 5, 6, 7);
}
__device__ __forceinline__ void glds16(const void* g, char* l) {
  __builtin_amdgcn_global_load_lds((const unsigned*)g, (unsigned*)l, 16, 0, 0);
}
__device__ __forceinline__ float xmax32(float v) {
  auto rr = __builtin_amdgcn_permlane32_swap(__float_as_uint(v), __float_as_uint(v), false, false);
  return fmaxf(__uint_as_float(rr[0]), __uint_as_float(rr[1]));
}
__device__ __forceinline__ float xsum32(float v) {
  auto rr = __builtin_amdgcn_permlane32_swap(__float_as_uint(v), __float_as_uint(v), false, false);
  return __uint_as_float(rr[0]) + __uint_as_float(rr[1]);
}
__device__ __forceinline__ float xmax16(float v) {
  auto rr = __builtin_amdgcn_permlane16_swap(__float_as_uint(v), __float_as_uint(v), false, false);
  return fmaxf(__uint_as_float(rr[0]), __uint_as_float(rr[1]));
}
__device__ __forceinline__ float xsum16(float v) {
  auto rr = __builtin_amdgcn_permlane16_swap(__float_as_uint(v), __float_as_uint(v), false, false);
  return __uint_as_float(rr[0]) + __uint_as_float(rr[1]);
}
template <int OFF> __device__ __forceinline__ bf16x8 rd128(int a) {
  bf16x8 r;
  asm volatile("ds_read_b128 %0, %1 offset:%2" : "=v"(r) : "v"(a), "i"(OFF) : "memory");
  return r;
}
template <int OFF> __device__ __forceinline__ s16x4 rdtr(int a) {
  s16x4 r;
  asm volatile("ds_read_b64_tr_b16 %0, %1 offset:%2" : "=v"(r) : "v"(a), "i"(OFF) : "memory");
  return r;
}
#define WAITL1(N, a) asm volatile("s_waitcnt lgkmcnt(" #N ")" : "+v"(a) :: "memory")
#define WAITL2(N, a, b) asm volatile("s_waitcnt lgkmcnt(" #N ")" : "+v"(a), "+v"(b) :: "memory")
#define WAITL4(N, a, b, c, d) asm volatile("s_waitcnt lgkmcnt(" #N ")" : "+v"(a), "+v"(b), "+v"(c), "+v"(d) :: "memory")
#define WAITL8(N, a, b, c, d, e, f, g_, h_) asm volatile("s_waitcnt lgkmcnt(" #N ")" : "+v"(a), "+v"(b), "+v"(c), "+v"(d), "+v"(e), "+v"(f), "+v"(g_), "+v"(h_) :: "memory")
#define WAIT_VM0() asm volatile("s_waitcnt vmcnt(0)" ::: "memory")
#define RAW_BAR() __builtin_amdgcn_s_barrier()

constexpr int A_BUFA = 20480;
constexpr int A_BUFB = 16384;
constexpr int A_OFF_A = 0;
constexpr int A_OFF_B = 2 * A_BUFA;
constexpr int A_OFF_Q2 = A_OFF_B + 2 * A_BUFB;
constexpr int A_LDS = A_OFF_Q2 + 4096;

template <int N> struct IC { static constexpr int value = N; };
template <int I, int N, class F> __device__ __forceinline__ void sfor(F&& f) {
  if constexpr (I < N) { f(IC<I>{}); sfor<I + 1, N>(f); }
}
#define OPAQUE(v) asm volatile("" : "+v"(v))

__global__ __launch_bounds__(256, 2) void attn_fused_kernel(
    const short* __restrict__ x, const short* __restrict__ kc, const short* __restrict__ vc,
    const short* __restrict__ wq, short* __restrict__ og) {
  extern __shared__ __attribute__((aligned(16))) char smem[];
  const int tid = threadIdx.x, lane = tid & 63;
  const int w = __builtin_amdgcn_readfirstlane(tid >> 6);
  const int pidx = (blockIdx.x & 7) * 128 + (blockIdx.x >> 3);
  const int b = pidx >> 5, h = pidx & 31;

  const char* xb = (const char*)(x + (size_t)b * SQ * DM);
  const char* wqh = (const char*)(wq + h * DH);
  const char* kb = (const char*)(kc + (size_t)pidx * SK * DH);
  const char* vb = (const char*)(vc + (size_t)pidx * SK * DH);

  const int l31 = lane & 31, hh = lane >> 5, l15 = lane & 15, g = lane >> 4;
  const int qp = l15 >> 2, p4 = lane & 3, blk = (lane >> 4) & 1;
  const int sb = (int)(uintptr_t)smem;

  f32x16 acc[4];
#pragma unroll
  for (int i = 0; i < 4; ++i) acc[i] = f32x16{};
  f32x4 acc2[8];
#pragma unroll
  for (int i = 0; i < 8; ++i) acc2[i] = f32x4{};

  int ksrc = (16 * w + g) * 256 + ((l15 ^ g) << 4);
  int vsrc = (16 * w + g) * 256 + ((l15 ^ (g << 2)) << 4);
  auto stage_kv = [&](int t, int buf) {
    char* la = smem + A_OFF_A + buf * A_BUFA + w * 4096 + lane * 16;
    char* lb = smem + A_OFF_B + buf * A_BUFB + w * 4096 + lane * 16;
    int ko = ksrc, vo = vsrc;
    OPAQUE(ko); OPAQUE(vo);
    const char* kt = kb + t * 16384;
    const char* vt = vb + t * 16384;
#pragma unroll
    for (int ii = 0; ii < 4; ++ii) glds16(kt + ((ko ^ (ii << 6)) + ii * 1024), la + ii * 1024);
#pragma unroll
    for (int ii = 0; ii < 4; ++ii) glds16(vt + (vo + ii * 1024), lb + ii * 1024);
  };

  {
    int xsrc = lane;
    auto stage_q = [&](int t, int buf) {
      char* la = smem + A_OFF_A + buf * A_BUFA + w * 5120 + lane * 16;
      char* lb = smem + A_OFF_B + buf * A_BUFB + w * 4096 + lane * 16;
      int ln = xsrc;
      OPAQUE(ln);
      const char* xt = xb + t * 128;
      const char* wt = wqh + (size_t)t * 64 * HD * 2;
#pragma unroll
      for (int ii = 0; ii < 5; ++ii) {
        int r = 8 * (w * 5 + ii) + (ln >> 3), cp = ln & 7;
        int c = cp ^ ((r >> 1) & 7);
        int rs = r < SQ ? r : SQ - 1;
        glds16(xt + (rs * 1024 + c * 16), la + ii * 1024);
      }
      int wo0 = (16 * w + (ln >> 4)) * (HD * 2) + (((ln & 15) ^ ((ln >> 4) << 2)) << 4);
#pragma unroll
      for (int ii = 0; ii < 4; ++ii) glds16(wt + (wo0 + ii * 4 * HD * 2), lb + ii * 1024);
    };
    int xa = sb + A_OFF_A + (32 * w + l31) * 128 + ((hh ^ ((l31 >> 1) & 7)) << 4);
    int wa = sb + A_OFF_B + (8 * hh + qp) * 256 + (((2 * blk + (p4 >> 1)) ^ (qp << 2)) << 4) + 8 * (p4 & 1);
    int xa2 = sb + A_OFF_A + (128 + l15) * 128 + ((g ^ ((l15 >> 1) & 7)) << 4);
    int wa2 = sb + A_OFF_B + (8 * g + qp) * 256 + (((4 * w + (p4 >> 1)) ^ (qp << 2)) << 4) + 8 * (p4 & 1);
    auto body = [&](auto BC, int t) {
      constexpr int buf = decltype(BC)::value;
      constexpr int OA = buf * A_BUFA, OB = buf * A_BUFB;
      WAIT_VM0();
      RAW_BAR();
      if (t < 7) stage_q(t + 1, buf ^ 1);
      else stage_kv(0, buf ^ 1);
      int xa_ = xa, wa_ = wa, xa2_ = xa2, wa2_ = wa2;
      OPAQUE(xa_); OPAQUE(wa_); OPAQUE(xa2_); OPAQUE(wa2_);
      const int w0 = wa_, w1 = wa_ ^ 64, w2 = wa_ ^ 128, w3 = wa_ ^ 192;
      bf16x8 xf[2];
      s16x4 wl[2][4], wh[2][4];
      bf16x8 xr[2];
      s16x4 rl[2][2], rh[2][2];
      auto issue = [&](auto SC) {
        constexpr int s = decltype(SC)::value, u = s & 1;
        xf[u] = rd128<OA>(xa_ ^ (s << 5));
        wl[u][0] = rdtr<OB + s * 4096>(w0); wh[u][0] = rdtr<OB + s * 4096 + 1024>(w0);
        wl[u][1] = rdtr<OB + s * 4096>(w1); wh[u][1] = rdtr<OB + s * 4096 + 1024>(w1);
        wl[u][2] = rdtr<OB + s * 4096>(w2); wh[u][2] = rdtr<OB + s * 4096 + 1024>(w2);
        wl[u][3] = rdtr<OB + s * 4096>(w3); wh[u][3] = rdtr<OB + s * 4096 + 1024>(w3);
      };
      issue(IC<0>{});
      sfor<0, 4>([&](auto SC) {
        constexpr int s = decltype(SC)::value, u = s & 1;
        if constexpr (s < 3) {
          issue(IC<s + 1>{});
          asm volatile("s_waitcnt lgkmcnt(9)" : "+v"(xf[u]), "+v"(wl[u][0]), "+v"(wh[u][0]), "+v"(wl[u][1]), "+v"(wh[u][1]),
                       "+v"(wl[u][2]), "+v"(wh[u][2]), "+v"(wl[u][3]), "+v"(wh[u][3]) :: "memory");
        } else {
          xr[0] = rd128<OA>(xa2_); xr[1] = rd128<OA>(xa2_ ^ 64);
          rl[0][0] = rdtr<OB>(wa2_); rh[0][0] = rdtr<OB + 1024>(wa2_);
          rl[0][1] = rdtr<OB>(wa2_ ^ 32); rh[0][1] = rdtr<OB + 1024>(wa2_ ^ 32);
          rl[1][0] = rdtr<OB + 8192>(wa2_); rh[1][0] = rdtr<OB + 8192 + 1024>(wa2_);
          rl[1][1] = rdtr<OB + 8192>(wa2_ ^ 32); rh[1][1] = rdtr<OB + 8192 + 1024>(wa2_ ^ 32);
          asm volatile("s_waitcnt lgkmcnt(10)" : "+v"(xf[u]), "+v"(wl[u][0]), "+v"(wh[u][0]), "+v"(wl[u][1]), "+v"(wh[u][1]),
                       "+v"(wl[u][2]), "+v"(wh[u][2]), "+v"(wl[u][3]), "+v"(wh[u][3]) :: "memory");
        }
#pragma unroll
        for (int dt = 0; dt < 4; ++dt) acc[dt] = MFMA32(cat8(wl[u][dt], wh[u][dt]), xf[u], acc[dt]);
      });
      asm volatile("s_waitcnt lgkmcnt(0)" : "+v"(xr[0]), "+v"(xr[1]), "+v"(rl[0][0]), "+v"(rh[0][0]), "+v"(rl[0][1]), "+v"(rh[0][1]),
                   "+v"(rl[1][0]), "+v"(rh[1][0]), "+v"(rl[1][1]), "+v"(rh[1][1]) :: "memory");
#pragma unroll
      for (int s2 = 0; s2 < 2; ++s2)
#pragma unroll
        for (int e = 0; e < 2; ++e) acc2[e] = MFMA16(cat8(rl[s2][e], rh[s2][e]), xr[s2], acc2[e]);
    };
    stage_q(0, 0);
    for (int t = 0; t < 8; t += 2) {
      body(IC<0>{}, t);
      body(IC<1>{}, t + 1);
    }
  }
  bf16x8 qf[8];
#pragma unroll
  for (int dt = 0; dt < 4; ++dt) {
#pragma unroll
    for (int s = 0; s < 2; ++s) {
      unsigned a0 = cvtpk(acc[dt][8 * s + 0], acc[dt][8 * s + 1]);
      unsigned a1 = cvtpk(acc[dt][8 * s + 2], acc[dt][8 * s + 3]);
      unsigned b0 = cvtpk(acc[dt][8 * s + 4], acc[dt][8 * s + 5]);
      unsigned b1 = cvtpk(acc[dt][8 * s + 6], acc[dt][8 * s + 7]);
      auto r0 = __builtin_amdgcn_permlane32_swap(a0, b0, false, false);
      auto r1 = __builtin_amdgcn_permlane32_swap(a1, b1, false, false);
      u32x4 wv = {r0[0], r1[0], r0[1], r1[1]};
      qf[2 * dt + s] = __builtin_bit_cast(bf16x8, wv);
    }
  }
  {
#pragma unroll
    for (int e = 0; e < 2; ++e) {
      int dd = 2 * w + e;
      u32x2 pk = {cvtpk(acc2[e][0], acc2[e][1]), cvtpk(acc2[e][2], acc2[e][3])};
      int ch = (2 * dd + (g >> 1)) ^ l15;
      *(u32x2*)(smem + A_OFF_Q2 + l15 * 256 + (ch << 4) + 8 * (g & 1)) = pk;
    }
  }
#pragma unroll
  for (int i = 0; i < 4; ++i) acc[i] = f32x16{};
#pragma unroll
  for (int i = 0; i < 8; ++i) acc2[i] = f32x4{};

  constexpr float CS = 0.088388347648318440f * 1.4426950408889634f;
  float m1 = -1e30f, l1 = 0.f, m2 = -1e30f, l2 = 0.f;
  {
    int ka = sb + A_OFF_A + l31 * 256 + ((hh ^ l15) << 4);
    int ka2 = sb + A_OFF_A + (16 * w + l15) * 256 + ((g ^ l15) << 4);
    int q2a = sb + A_OFF_Q2 + l15 * 256 + ((g ^ l15) << 4);
    int va = sb + A_OFF_B + (4 * hh + qp) * 256 + (((2 * blk + (p4 >> 1)) ^ (qp << 2)) << 4) + 8 * (p4 & 1);
    int va2 = sb + A_OFF_B + (16 * w + 4 * g + qp) * 256 + (((p4 >> 1) ^ (qp << 2)) << 4) + 8 * (p4 & 1);
    auto body = [&](auto BC, int t) {
      constexpr int buf = decltype(BC)::value;
      asm volatile("s_waitcnt vmcnt(0) lgkmcnt(0)" ::: "memory");
      RAW_BAR();
      if (t < 7) stage_kv(t + 1, buf ^ 1);
      int ka_ = ka, ka2_ = ka2, q2a_ = q2a, va_ = va, va2_ = va2;
      OPAQUE(ka_); OPAQUE(ka2_); OPAQUE(q2a_); OPAQUE(va_); OPAQUE(va2_);
      constexpr int OA = buf * A_BUFA, OB = buf * A_BUFB;
      f32x16 s0 = f32x16{}, s1 = f32x16{};
      f32x4 sr = f32x4{};
      {
        bf16x8 kf[2][4];
        auto issue = [&](auto CC) {
          constexpr int c = decltype(CC)::value, u = c & 1;
          kf[u][0] = rd128<OA>(ka_ ^ (c << 6));
          kf[u][1] = rd128<OA + 8192>(ka_ ^ (c << 6));
          kf[u][2] = rd128<OA>(ka_ ^ ((c << 6) | 32));
          kf[u][3] = rd128<OA + 8192>(ka_ ^ ((c << 6) | 32));
        };
        {
          bf16x8 kr[4], qr[4];
#pragma unroll
          for (int ks = 0; ks < 4; ++ks) { kr[ks] = rd128<OA>(ka2_ ^ (ks << 6)); qr[ks] = rd128<0>(q2a_ ^ (ks << 6)); }
          issue(IC<0>{});
          WAITL8(4, kr[0], kr[1], kr[2], kr[3], qr[0], qr[1], qr[2], qr[3]);
#pragma unroll
          for (int ks = 0; ks < 4; ++ks) sr = MFMA16(kr[ks], qr[ks], sr);
        }
        sfor<0, 4>([&](auto CC) {
          constexpr int c = decltype(CC)::value, u = c & 1;
          if constexpr (c < 3) {
            issue(IC<c + 1>{});
            WAITL4(4, kf[u][0], kf[u][1], kf[u][2], kf[u][3]);
          } else {
            WAITL4(0, kf[u][0], kf[u][1], kf[u][2], kf[u][3]);
          }
          s0 = MFMA32(kf[u][0], qf[2 * c], s0);
          s1 = MFMA32(kf[u][1], qf[2 * c], s1);
          s0 = MFMA32(kf[u][2], qf[2 * c + 1], s0);
          s1 = MFMA32(kf[u][3], qf[2 * c + 1], s1);
        });
      }
      const int v0 = va_, v1 = va_ ^ 64, v2 = va_ ^ 128, v3 = va_ ^ 192;
      s16x4 vl[2][4], vh[2][4];
      auto issue_v = [&](auto SC) {
        constexpr int s = decltype(SC)::value, u = s & 1;
        vl[u][0] = rdtr<OB + s * 4096>(v0); vh[u][0] = rdtr<OB + s * 4096 + 2048>(v0);
        vl[u][1] = rdtr<OB + s * 4096>(v1); vh[u][1] = rdtr<OB + s * 4096 + 2048>(v1);
        vl[u][2] = rdtr<OB + s * 4096>(v2); vh[u][2] = rdtr<OB + s * 4096 + 2048>(v2);
        vl[u][3] = rdtr<OB + s * 4096>(v3); vh[u][3] = rdtr<OB + s * 4096 + 2048>(v3);
      };
      issue_v(IC<0>{});
      float tmax = s0[0];
#pragma unroll
      for (int r = 1; r < 16; ++r) tmax = fmaxf(tmax, s0[r]);
#pragma unroll
      for (int r = 0; r < 16; ++r) tmax = fmaxf(tmax, s1[r]);
      tmax = xmax32(tmax);
      float mn = fmaxf(m1, tmax);
      float alpha = __builtin_amdgcn_exp2f((m1 - mn) * CS);
      m1 = mn;
      float t2 = fmaxf(fmaxf(sr[0], sr[1]), fmaxf(sr[2], sr[3]));
      t2 = xmax16(t2);
      t2 = xmax32(t2);
      float mn2 = fmaxf(m2, t2);
      float alpha2 = __builtin_amdgcn_exp2f((m2 - mn2) * CS);
      m2 = mn2;
      if (!__all(alpha == 1.f && alpha2 == 1.f)) {
#pragma unroll
        for (int dt = 0; dt < 4; ++dt)
#pragma unroll
          for (int r = 0; r < 16; ++r) acc[dt][r] *= alpha;
#pragma unroll
        for (int dd = 0; dd < 8; ++dd)
#pragma unroll
          for (int r = 0; r < 4; ++r) acc2[dd][r] *= alpha2;
      }
      const float mc = -mn * CS, mc2 = -mn2 * CS;
      float ps = 0.f;
      bf16x8 pf[4];
#pragma unroll
      for (int s = 0; s < 2; ++s) {
        float e[8];
#pragma unroll
        for (int j = 0; j < 8; ++j) { e[j] = __builtin_amdgcn_exp2f(fmaf(s0[8 * s + j], CS, mc)); ps += e[j]; }
        u32x4 a = {cvtpk(e[0], e[1]), cvtpk(e[2], e[3]), cvtpk(e[4], e[5]), cvtpk(e[6], e[7])};
        pf[s] = __builtin_bit_cast(bf16x8, a);
        OPAQUE(ps);
      }
#pragma unroll
      for (int s = 0; s < 2; ++s) {
        float e[8];
#pragma unroll
        for (int j = 0; j < 8; ++j) { e[j] = __builtin_amdgcn_exp2f(fmaf(s1[8 * s + j], CS, mc)); ps += e[j]; }
        u32x4 a = {cvtpk(e[0], e[1]), cvtpk(e[2], e[3]), cvtpk(e[4], e[5]), cvtpk(e[6], e[7])};
        pf[2 + s] = __builtin_bit_cast(bf16x8, a);
        OPAQUE(ps);
      }
      l1 = l1 * alpha + ps;
      OPAQUE(l1);
      float e0 = __builtin_amdgcn_exp2f(fmaf(sr[0], CS, mc2));
      float e1 = __builtin_amdgcn_exp2f(fmaf(sr[1], CS, mc2));
      float e2 = __builtin_amdgcn_exp2f(fmaf(sr[2], CS, mc2));
      float e3 = __builtin_amdgcn_exp2f(fmaf(sr[3], CS, mc2));
      l2 = l2 * alpha2 + (e0 + e1) + (e2 + e3);
      OPAQUE(l2);
      u32x2 p2u = {cvtpk(e0, e1), cvtpk(e2, e3)};
      s16x4 p2 = __builtin_bit_cast(s16x4, p2u);
      s16x4 vr[8];
      sfor<0, 4>([&](auto SC) {
        constexpr int s = decltype(SC)::value, u = s & 1;
        if constexpr (s < 3) {
          issue_v(IC<s + 1>{});
        } else {
#pragma unroll
          for (int dd = 0; dd < 8; ++dd) vr[dd] = rdtr<OB>(va2_ ^ (dd << 5));
        }
        WAITL8(8, vl[u][0], vh[u][0], vl[u][1], vh[u][1], vl[u][2], vh[u][2], vl[u][3], vh[u][3]);
#pragma unroll
        for (int dt = 0; dt < 4; ++dt) acc[dt] = MFMA32(cat8(vl[u][dt], vh[u][dt]), pf[s], acc[dt]);
      });
      WAITL8(0, vr[0], vr[1], vr[2], vr[3], vr[4], vr[5], vr[6], vr[7]);
#pragma unroll
      for (int dd = 0; dd < 8; ++dd) acc2[dd] = MFMA16K16(vr[dd], p2, acc2[dd]);
    };
    for (int t = 0; t < 8; t += 2) {
      body(IC<0>{}, t);
      body(IC<1>{}, t + 1);
    }
  }

  int tz = threadIdx.x;
  OPAQUE(tz);
  {
    const int l31 = tz & 31, hh = (tz >> 5) & 1;
    float lt = xsum32(l1);
    float inv = 1.f / lt;
    short* orow = og + (size_t)(b * SQ + 32 * w + l31) * HD + h * DH + 4 * hh;
#pragma unroll
    for (int dt = 0; dt < 4; ++dt)
#pragma unroll
      for (int i = 0; i < 4; ++i) {
        u32x2 pk = {cvtpk(acc[dt][4 * i] * inv, acc[dt][4 * i + 1] * inv),
                    cvtpk(acc[dt][4 * i + 2] * inv, acc[dt][4 * i + 3] * inv)};
        *(u32x2*)(orow + 32 * dt + 8 * i) = pk;
      }
  }
  __syncthreads();
  {
    const int l15 = tz & 15, g = (tz >> 4) & 3;
    float lt2 = xsum32(xsum16(l2));
    float* pw = (float*)(smem + w * 8192);
#pragma unroll
    for (int dd = 0; dd < 8; ++dd) *(f32x4*)(pw + l15 * 128 + 16 * dd + 4 * g) = acc2[dd];
    float* st = (float*)(smem + 32768);
    if (g == 0) { st[w * 32 + l15] = m2; st[w * 32 + 16 + l15] = lt2; }
  }
  __syncthreads();
  {
    const int q = tz >> 4, d0 = (tz & 15) * 8;
    const float* st = (const float*)(smem + 32768);
    float mm = fmaxf(fmaxf(st[q], st[32 + q]), fmaxf(st[64 + q], st[96 + q]));
    float o[8] = {0, 0, 0, 0, 0, 0, 0, 0};
    float ls = 0.f;
#pragma unroll
    for (int ww = 0; ww < 4; ++ww) {
      float sc = __builtin_amdgcn_exp2f((st[ww * 32 + q] - mm) * CS);
      ls += st[ww * 32 + 16 + q] * sc;
      const float* pw = (const float*)(smem + ww * 8192) + q * 128 + d0;
      f32x4 a = *(const f32x4*)pw, c = *(const f32x4*)(pw + 4);
#pragma unroll
      for (int j = 0; j < 4; ++j) { o[j] += a[j] * sc; o[4 + j] += c[j] * sc; }
    }
    float inv = 1.f / ls;
    u32x4 pk = {cvtpk(o[0] * inv, o[1] * inv), cvtpk(o[2] * inv, o[3] * inv),
                cvtpk(o[4] * inv, o[5] * inv), cvtpk(o[6] * inv, o[7] * inv)};
    *(u32x4*)(og + (size_t)(b * SQ + 128 + q) * HD + h * DH + d0) = pk;
  }
}

constexpr int B_NST = 4;
constexpr int B_BUFA = 20480;
constexpr int B_BUFB = 8192;
constexpr int B_STRIDE = B_BUFA + B_BUFB;
constexpr int B_LDS = B_NST * B_STRIDE;

__global__ __launch_bounds__(256, 1) void oproj_kernel(const short* __restrict__ og,
                                                        const short* __restrict__ wo,
                                                        short* __restrict__ out) {
  extern __shared__ __attribute__((aligned(16))) char smem[];
  const int tid = threadIdx.x, lane = tid & 63;
  const int w = __builtin_amdgcn_readfirstlane(tid >> 6);
  const int xg = blockIdx.x & 7, idx = blockIdx.x >> 3;
  const int b = xg * 4 + (idx >> 3), nt0 = idx & 7;
  const short* ob = og + (size_t)b * SQ * HD;
  const short* wob = wo + nt0 * 64;
  const int l31 = lane & 31, hh = lane >> 5, l15 = lane & 15, g = lane >> 4;
  const int qp = l15 >> 2, p4 = lane & 3, blk = (lane >> 4) & 1;

  auto stage = [&](int t, int buf) {
    char* la = smem + buf * B_STRIDE;
    char* lb = la + B_BUFA;
#pragma unroll
    for (int ii = 0; ii < 5; ++ii) {
      int i = w * 5 + ii;
      int r = 8 * i + (lane >> 3), cp = lane & 7;
      int c = cp ^ ((r >> 1) & 7);
      int rs = r < SQ ? r : SQ - 1;
      glds16(ob + (size_t)rs * HD + t * 64 + c * 8, la + i * 1024 + lane * 16);
    }
#pragma unroll
    for (int ii = 0; ii < 2; ++ii) {
      int i = w * 2 + ii;
      int r = 8 * i + (lane >> 3), cp = lane & 7;
      int c = cp ^ (((r >> 1) & 1) << 2);
      glds16(wob + (size_t)(t * 64 + r) * DM + c * 8, lb + i * 1024 + lane * 16);
    }
  };

  f32x16 acc[2] = {f32x16{}, f32x16{}};
  f32x4 acc2 = f32x4{};
  constexpr int NT = HD / 64;
  const int sb = (int)(uintptr_t)smem;
  const int xa = sb + (32 * w + l31) * 128 + ((hh ^ ((l31 >> 1) & 7)) << 4);
  const int wa = sb + B_BUFA + (8 * hh + qp) * 128 + (((2 * blk + (p4 >> 1)) ^ ((qp >> 1) << 2)) << 4) + 8 * (p4 & 1);
  const int xa2 = sb + (128 + l15) * 128 + ((g ^ ((l15 >> 1) & 7)) << 4);
  const int wa2 = sb + B_BUFA + (8 * g + qp) * 128 + (((2 * w + (p4 >> 1)) ^ ((qp >> 1) << 2)) << 4) + 8 * (p4 & 1);
  stage(0, 0);
  stage(1, 1);
  stage(2, 2);
  auto body = [&](auto BC, int t) {
    constexpr int buf = decltype(BC)::value;
    constexpr int OB = (buf & 1) * B_STRIDE;
    const int hi2 = (buf >> 1) * 2 * B_STRIDE;
    if (t < NT - 2) asm volatile("s_waitcnt vmcnt(14)" ::: "memory");
    else if (t == NT - 2) asm volatile("s_waitcnt vmcnt(7)" ::: "memory");
    else asm volatile("s_waitcnt vmcnt(0)" ::: "memory");
    RAW_BAR();
    if (t + 3 < NT) stage(t + 3, (buf + 3) & 3);
    const int xa_ = xa + hi2, w0 = wa + hi2, w1 = (wa ^ 64) + hi2, xa2_ = xa2 + hi2, wa2_ = wa2 + hi2;
    bf16x8 xf[4], xr[2];
    s16x4 wl[4][2], wh[4][2], rl[2], rh[2];
    sfor<0, 4>([&](auto SC) {
      constexpr int s = decltype(SC)::value;
      xf[s] = rd128<OB>(xa_ ^ (s << 5));
      wl[s][0] = rdtr<OB + s * 2048>(w0); wh[s][0] = rdtr<OB + s * 2048 + 512>(w0);
      wl[s][1] = rdtr<OB + s * 2048>(w1); wh[s][1] = rdtr<OB + s * 2048 + 512>(w1);
      if constexpr (s == 1) {
        asm volatile("s_waitcnt lgkmcnt(5)" : "+v"(xf[0]), "+v"(wl[0][0]), "+v"(wh[0][0]), "+v"(wl[0][1]), "+v"(wh[0][1]) :: "memory");
        acc[0] = MFMA32(cat8(wl[0][0], wh[0][0]), xf[0], acc[0]);
        acc[1] = MFMA32(cat8(wl[0][1], wh[0][1]), xf[0], acc[1]);
      }
      if constexpr (s == 2) {
        asm volatile("s_waitcnt lgkmcnt(5)" : "+v"(xf[1]), "+v"(wl[1][0]), "+v"(wh[1][0]), "+v"(wl[1][1]), "+v"(wh[1][1]) :: "memory");
        acc[0] = MFMA32(cat8(wl[1][0], wh[1][0]), xf[1], acc[0]);
        acc[1] = MFMA32(cat8(wl[1][1], wh[1][1]), xf[1], acc[1]);
      }
      if constexpr (s == 3) {
        asm volatile("s_waitcnt lgkmcnt(5)" : "+v"(xf[2]), "+v"(wl[2][0]), "+v"(wh[2][0]), "+v"(wl[2][1]), "+v"(wh[2][1]) :: "memory");
        acc[0] = MFMA32(cat8(wl[2][0], wh[2][0]), xf[2], acc[0]);
        acc[1] = MFMA32(cat8(wl[2][1], wh[2][1]), xf[2], acc[1]);
      }
    });
    xr[0] = rd128<OB>(xa2_); rl[0] = rdtr<OB>(wa2_); rh[0] = rdtr<OB + 512>(wa2_);
    xr[1] = rd128<OB>(xa2_ ^ 64); rl[1] = rdtr<OB + 4096>(wa2_); rh[1] = rdtr<OB + 4096 + 512>(wa2_);
    asm volatile("s_waitcnt lgkmcnt(6)" : "+v"(xf[3]), "+v"(wl[3][0]), "+v"(wh[3][0]), "+v"(wl[3][1]), "+v"(wh[3][1]) :: "memory");
    acc[0] = MFMA32(cat8(wl[3][0], wh[3][0]), xf[3], acc[0]);
    acc[1] = MFMA32(cat8(wl[3][1], wh[3][1]), xf[3], acc[1]);
    asm volatile("s_waitcnt lgkmcnt(0)" : "+v"(xr[0]), "+v"(rl[0]), "+v"(rh[0]), "+v"(xr[1]), "+v"(rl[1]), "+v"(rh[1]) :: "memory");
    acc2 = MFMA16(cat8(rl[0], rh[0]), xr[0], acc2);
    acc2 = MFMA16(cat8(rl[1], rh[1]), xr[1], acc2);
  };
  for (int t = 0; t < NT; t += 4) {
    body(IC<0>{}, t);
    body(IC<1>{}, t + 1);
    body(IC<2>{}, t + 2);
    body(IC<3>{}, t + 3);
  }
  {
    short* orow = out + (size_t)(b * SQ + 32 * w + l31) * DM + nt0 * 64 + 4 * hh;
#pragma unroll
    for (int nt = 0; nt < 2; ++nt)
#pragma unroll
      for (int i = 0; i < 4; ++i) {
        u32x2 pk = {cvtpk(acc[nt][4 * i], acc[nt][4 * i + 1]), cvtpk(acc[nt][4 * i + 2], acc[nt][4 * i + 3])};
        *(u32x2*)(orow + 32 * nt + 8 * i) = pk;
      }
    u32x2 pk = {cvtpk(acc2[0], acc2[1]), cvtpk(acc2[2], acc2[3])};
    *(u32x2*)(out + (size_t)(b * SQ + 128 + l15) * DM + nt0 * 64 + 16 * w + 4 * g) = pk;
  }
}

extern "C" void kernel_launch(void* const* d_in, const int* in_sizes, int n_in, void* d_out,
                              int out_size, void* d_ws, size_t ws_size, hipStream_t stream) {
  const short* x = (const short*)d_in[0];
  const short* kc = (const short*)d_in[1];
  const short* vc = (const short*)d_in[2];
  const short* wq = (const short*)d_in[3];
  const short* wo = (const short*)d_in[4];
  short* og = (short*)d_ws;
  short* out = (short*)d_out;
  hipFuncSetAttribute((const void*)attn_fused_kernel, hipFuncAttributeMaxDynamicSharedMemorySize, A_LDS);
  hipFuncSetAttribute((const void*)oproj_kernel, hipFuncAttributeMaxDynamicSharedMemorySize, B_LDS);
  attn_fused_kernel<<<NB * NH, 256, A_LDS, stream>>>(x, kc, vc, wq, og);
  oproj_kernel<<<256, 256, B_LDS, stream>>>(og, wo, out);
}
```

```cpp
#include <hip/hip_runtime.h>
#include <hip/hip_bf16.h>

typedef __attribute__((ext_vector_type(8))) short bf16x8;
typedef __attribute__((ext_vector_type(4))) short s16x4;
typedef __attribute__((ext_vector_type(16))) float f32x16;
typedef __attribute__((ext_vector_type(4))) float f32x4;
typedef __attribute__((ext_vector_type(4))) unsigned u32x4;
typedef __attribute__((ext_vector_type(2))) unsigned u32x2;
typedef __attribute__((address_space(3))) s16x4 lds_s16x4;
typedef __attribute__((address_space(3))) char lds_char;

constexpr int NB = 32, SQ = 144, DM = 512, NH = 32, SK = 512, DH = 128;
constexpr int HD = NH * DH;

#define MFMA32(a, b, c) __builtin_amdgcn_mfma_f32_32x32x16_bf16((a), (b), (c), 0, 0, 0)
#define MFMA16(a, b, c) __builtin_amdgcn_mfma_f32_16x16x32_bf16((a), (b), (c), 0, 0, 0)
#define MFMA16K16(a, b, c) __builtin_amdgcn_mfma_f32_16x16x16bf16_1k((a), (b), (c), 0, 0, 0)

__device__ __forceinline__ unsigned cvtpk(float lo, float hi) {
  unsigned r;
  asm volatile("v_cvt_pk_bf16_f32 %0, %1, %2" : "=v"(r) : "v"(lo), "v"(hi));
  return r;
}
__device__ __forceinline__ s16x4 tr_read(const char* smem_base, int off) {
  return __builtin_amdgcn_ds_read_tr16_b64_v4i16((lds_s16x4*)((lds_char*)smem_base + off));
}
__device__ __forceinline__ bf16x8 cat8(s16x4 lo, s16x4 hi) {
  return __builtin_shufflevector(lo, hi, 0, 1, 2, 3, 4, 5, 6, 7);
}
__device__ __forceinline__ void glds16(const void* g, char* l) {
  __builtin_amdgcn_global_load_lds((const unsigned*)g, (unsigned*)l, 16, 0, 0);
}
__device__ __forceinline__ float xmax32(float v) {
  auto rr = __builtin_amdgcn_permlane32_swap(__float_as_uint(v), __float_as_uint(v), false, false);
  return fmaxf(__uint_as_float(rr[0]), __uint_as_float(rr[1]));
}
__device__ __forceinline__ float xsum32(float v) {
  auto rr = __builtin_amdgcn_permlane32_swap(__float_as_uint(v), __float_as_uint(v), false, false);
  return __uint_as_float(rr[0]) + __uint_as_float(rr[1]);
}
__device__ __forceinline__ float xmax16(float v) {
  auto rr = __builtin_amdgcn_permlane16_swap(__float_as_uint(v), __float_as_uint(v), false, false);
  return fmaxf(__uint_as_float(rr[0]), __uint_as_float(rr[1]));
}
__device__ __forceinline__ float xsum16(float v) {
  auto rr = __builtin_amdgcn_permlane16_swap(__float_as_uint(v), __float_as_uint(v), false, false);
  return __uint_as_float(rr[0]) + __uint_as_float(rr[1]);
}
template <int OFF> __device__ __forceinline__ bf16x8 rd128(int a) {
  bf16x8 r;
  asm volatile("ds_read_b128 %0, %1 offset:%2" : "=v"(r) : "v"(a), "i"(OFF) : "memory");
  return r;
}
template <int OFF> __device__ __forceinline__ s16x4 rdtr(int a) {
  s16x4 r;
  asm volatile("ds_read_b64_tr_b16 %0, %1 offset:%2" : "=v"(r) : "v"(a), "i"(OFF) : "memory");
  return r;
}
#define WAITL1(N, a) asm volatile("s_waitcnt lgkmcnt(" #N ")" : "+v"(a) :: "memory")
#define WAITL2(N, a, b) asm volatile("s_waitcnt lgkmcnt(" #N ")" : "+v"(a), "+v"(b) :: "memory")
#define WAITL4(N, a, b, c, d) asm volatile("s_waitcnt lgkmcnt(" #N ")" : "+v"(a), "+v"(b), "+v"(c), "+v"(d) :: "memory")
#define WAITL8(N, a, b, c, d, e, f, g_, h_) asm volatile("s_waitcnt lgkmcnt(" #N ")" : "+v"(a), "+v"(b), "+v"(c), "+v"(d), "+v"(e), "+v"(f), "+v"(g_), "+v"(h_) :: "memory")
#define WAIT_VM0() asm volatile("s_waitcnt vmcnt(0)" ::: "memory")
#define RAW_BAR() __builtin_amdgcn_s_barrier()
#define SBAR() __builtin_amdgcn_sched_barrier(0)

constexpr int A_BUFA = 20480;
constexpr int A_BUFB = 16384;
constexpr int A_OFF_A = 0;
constexpr int A_OFF_B = 2 * A_BUFA;
constexpr int A_OFF_Q2 = A_OFF_B + 2 * A_BUFB;
constexpr int A_LDS = A_OFF_Q2 + 4096;

template <int N> struct IC { static constexpr int value = N; };
template <int I, int N, class F> __device__ __forceinline__ void sfor(F&& f) {
  if constexpr (I < N) { f(IC<I>{}); sfor<I + 1, N>(f); }
}
#define OPAQUE(v) asm volatile("" : "+v"(v))
#define STAMP(i)

__global__ __launch_bounds__(256, 2) void attn_fused_kernel(
    const short* __restrict__ x, const short* __restrict__ kc, const short* __restrict__ vc,
    const short* __restrict__ wq, short* __restrict__ og) {
  extern __shared__ __attribute__((aligned(16))) char smem[];
  const int tid = threadIdx.x, lane = tid & 63;
  const int w = __builtin_amdgcn_readfirstlane(tid >> 6);
  const int pidx = (blockIdx.x & 7) * 128 + (blockIdx.x >> 3);
  const int b = pidx >> 5, h = pidx & 31;

  const char* xb = (const char*)(x + (size_t)b * SQ * DM);
  const char* wqh = (const char*)(wq + h * DH);
  const char* kb = (const char*)(kc + (size_t)pidx * SK * DH);
  const char* vb = (const char*)(vc + (size_t)pidx * SK * DH);

  const int l31 = lane & 31, hh = lane >> 5, l15 = lane & 15, g = lane >> 4;
  const int qp = l15 >> 2, p4 = lane & 3, blk = (lane >> 4) & 1;
  const int sb = (int)(uintptr_t)smem;

  f32x16 acc[4];
#pragma unroll
  for (int i = 0; i < 4; ++i) acc[i] = f32x16{};
  f32x4 acc2[8];
#pragma unroll
  for (int i = 0; i < 8; ++i) acc2[i] = f32x4{};

  unsigned ksrc = (16 * w + g) * 256 + ((l15 ^ g) << 4);
  unsigned vsrc = (16 * w + g) * 256 + ((l15 ^ (g << 2)) << 4);
  auto kv_piece = [&](int t, int buf, int i, unsigned ko, unsigned vo) {
    if (i < 4) glds16(kb + t * 16384 + ((ko ^ (i << 6)) + i * 1024), smem + A_OFF_A + buf * A_BUFA + w * 4096 + i * 1024);
    else glds16(vb + t * 16384 + (vo + (i - 4) * 1024), smem + A_OFF_B + buf * A_BUFB + w * 4096 + (i - 4) * 1024);
  };

  {
    unsigned xsrc[5];
#pragma unroll
    for (int ii = 0; ii < 5; ++ii) {
      int r = 8 * (w * 5 + ii) + (lane >> 3), cp = lane & 7;
      int c = cp ^ ((r >> 1) & 7);
      int rs = r < SQ ? r : SQ - 1;
      xsrc[ii] = rs * 1024 + c * 16;
    }
    const unsigned wsrc = (16 * w + g) * (HD * 2) + ((l15 ^ (g << 2)) << 4);
    auto q_piece = [&](int t, int buf, int i) {
      if (i < 5) glds16(xb + t * 128 + xsrc[i], smem + A_OFF_A + buf * A_BUFA + w * 5120 + i * 1024);
      else glds16(wqh + (size_t)t * (64 * HD * 2) + (wsrc + (i - 5) * 4 * HD * 2), smem + A_OFF_B + buf * A_BUFB + w * 4096 + (i - 5) * 1024);
    };
    const int xa = sb + A_OFF_A + (32 * w + l31) * 128 + ((hh ^ ((l31 >> 1) & 7)) << 4);
    const int wa = sb + A_OFF_B + (8 * hh + qp) * 256 + (((2 * blk + (p4 >> 1)) ^ (qp << 2)) << 4) + 8 * (p4 & 1);
    const int xa2 = sb + A_OFF_A + (128 + l15) * 128 + ((g ^ ((l15 >> 1) & 7)) << 4);
    const int wa2 = sb + A_OFF_B + (8 * g + qp) * 256 + (((4 * w + (p4 >> 1)) ^ (qp << 2)) << 4) + 8 * (p4 & 1);
    auto body = [&](auto BC, int t) {
      constexpr int buf = decltype(BC)::value;
      constexpr int OA = buf * A_BUFA, OB = buf * A_BUFB;
      WAIT_VM0();
      RAW_BAR();
      STAMP(0);
      auto piece = [&](int i) {
        if (t < 7) q_piece(t + 1, buf ^ 1, i);
        else if (i < 8) kv_piece(0, buf ^ 1, i, ksrc, vsrc);
      };
      const int w0 = wa, w1 = wa ^ 64, w2 = wa ^ 128, w3 = wa ^ 192;
      bf16x8 xf[2];
      s16x4 wl[2][4], wh[2][4];
      bf16x8 xr[2];
      s16x4 rl[2][2], rh[2][2];
      auto issue = [&](auto SC) {
        constexpr int s = decltype(SC)::value, u = s & 1;
        xf[u] = rd128<OA>(xa ^ (s << 5));
        wl[u][0] = rdtr<OB + s * 4096>(w0); wh[u][0] = rdtr<OB + s * 4096 + 1024>(w0);
        wl[u][1] = rdtr<OB + s * 4096>(w1); wh[u][1] = rdtr<OB + s * 4096 + 1024>(w1);
        wl[u][2] = rdtr<OB + s * 4096>(w2); wh[u][2] = rdtr<OB + s * 4096 + 1024>(w2);
        wl[u][3] = rdtr<OB + s * 4096>(w3); wh[u][3] = rdtr<OB + s * 4096 + 1024>(w3);
      };
      issue(IC<0>{});
      sfor<0, 4>([&](auto SC) {
        constexpr int s = decltype(SC)::value, u = s & 1;
        if constexpr (s < 3) {
          issue(IC<s + 1>{});
          asm volatile("s_waitcnt lgkmcnt(9)" : "+v"(xf[u]), "+v"(wl[u][0]), "+v"(wh[u][0]), "+v"(wl[u][1]), "+v"(wh[u][1]),
                       "+v"(wl[u][2]), "+v"(wh[u][2]), "+v"(wl[u][3]), "+v"(wh[u][3]) :: "memory");
        } else {
          xr[0] = rd128<OA>(xa2); xr[1] = rd128<OA>(xa2 ^ 64);
          rl[0][0] = rdtr<OB>(wa2); rh[0][0] = rdtr<OB + 1024>(wa2);
          rl[0][1] = rdtr<OB>(wa2 ^ 32); rh[0][1] = rdtr<OB + 1024>(wa2 ^ 32);
          rl[1][0] = rdtr<OB + 8192>(wa2); rh[1][0] = rdtr<OB + 8192 + 1024>(wa2);
          rl[1][1] = rdtr<OB + 8192>(wa2 ^ 32); rh[1][1] = rdtr<OB + 8192 + 1024>(wa2 ^ 32);
          asm volatile("s_waitcnt lgkmcnt(10)" : "+v"(xf[u]), "+v"(wl[u][0]), "+v"(wh[u][0]), "+v"(wl[u][1]), "+v"(wh[u][1]),
                       "+v"(wl[u][2]), "+v"(wh[u][2]), "+v"(wl[u][3]), "+v"(wh[u][3]) :: "memory");
        }
        acc[0] = MFMA32(cat8(wl[u][0], wh[u][0]), xf[u], acc[0]);
        acc[1] = MFMA32(cat8(wl[u][1], wh[u][1]), xf[u], acc[1]);
        SBAR();
        piece(2 * s);
        SBAR();
        acc[2] = MFMA32(cat8(wl[u][2], wh[u][2]), xf[u], acc[2]);
        acc[3] = MFMA32(cat8(wl[u][3], wh[u][3]), xf[u], acc[3]);
        SBAR();
        piece(2 * s + 1);
        SBAR();
      });
      asm volatile("s_waitcnt lgkmcnt(0)" : "+v"(xr[0]), "+v"(xr[1]), "+v"(rl[0][0]), "+v"(rh[0][0]), "+v"(rl[0][1]), "+v"(rh[0][1]),
                   "+v"(rl[1][0]), "+v"(rh[1][0]), "+v"(rl[1][1]), "+v"(rh[1][1]) :: "memory");
#pragma unroll
      for (int s2 = 0; s2 < 2; ++s2)
#pragma unroll
        for (int e = 0; e < 2; ++e) acc2[e] = MFMA16(cat8(rl[s2][e], rh[s2][e]), xr[s2], acc2[e]);
      SBAR();
      piece(8);
      STAMP(2);
    };
#pragma unroll
    for (int i = 0; i < 9; ++i) q_piece(0, 0, i);
    for (int t = 0; t < 8; t += 2) {
      body(IC<0>{}, t);
      body(IC<1>{}, t + 1);
    }
  }
  bf16x8 qf[8];
#pragma unroll
  for (int dt = 0; dt < 4; ++dt) {
#pragma unroll
    for (int s = 0; s < 2; ++s) {
      unsigned a0 = cvtpk(acc[dt][8 * s + 0], acc[dt][8 * s + 1]);
      unsigned a1 = cvtpk(acc[dt][8 * s + 2], acc[dt][8 * s + 3]);
      unsigned b0 = cvtpk(acc[dt][8 * s + 4], acc[dt][8 * s + 5]);
      unsigned b1 = cvtpk(acc[dt][8 * s + 6], acc[dt][8 * s + 7]);
      auto r0 = __builtin_amdgcn_permlane32_swap(a0, b0, false, false);
      auto r1 = __builtin_amdgcn_permlane32_swap(a1, b1, false, false);
      u32x4 wv = {r0[0], r1[0], r0[1], r1[1]};
      qf[2 * dt + s] = __builtin_bit_cast(bf16x8, wv);
    }
  }
  {
#pragma unroll
    for (int e = 0; e < 2; ++e) {
      int dd = 2 * w + e;
      u32x2 pk = {cvtpk(acc2[e][0], acc2[e][1]), cvtpk(acc2[e][2], acc2[e][3])};
      int ch = (2 * dd + (g >> 1)) ^ l15;
      *(u32x2*)(smem + A_OFF_Q2 + l15 * 256 + (ch << 4) + 8 * (g & 1)) = pk;
    }
  }
#pragma unroll
  for (int i = 0; i < 4; ++i) acc[i] = f32x16{};
#pragma unroll
  for (int i = 0; i < 8; ++i) acc2[i] = f32x4{};

  constexpr float CS = 0.088388347648318440f * 1.4426950408889634f;
  float m1 = -1e30f, l1 = 0.f, m2 = -1e30f, l2 = 0.f;
  {
    int ka = sb + A_OFF_A + l31 * 256 + ((hh ^ l15) << 4);
    int ka2 = sb + A_OFF_A + (16 * w + l15) * 256 + ((g ^ l15) << 4);
    int q2a = sb + A_OFF_Q2 + l15 * 256 + ((g ^ l15) << 4);
    int va = sb + A_OFF_B + (4 * hh + qp) * 256 + (((2 * blk + (p4 >> 1)) ^ (qp << 2)) << 4) + 8 * (p4 & 1);
    int va2 = sb + A_OFF_B + (16 * w + 4 * g + qp) * 256 + (((p4 >> 1) ^ (qp << 2)) << 4) + 8 * (p4 & 1);
    auto body = [&](auto BC, int t) {
      constexpr int buf = decltype(BC)::value;
      constexpr int OA = buf * A_BUFA, OB = buf * A_BUFB;
      asm volatile("s_waitcnt vmcnt(0) lgkmcnt(0)" ::: "memory");
      RAW_BAR();
      STAMP(3);
      int ka_ = ka, ka2_ = ka2, q2a_ = q2a, va_ = va, va2_ = va2;
      unsigned ko = ksrc, vo = vsrc;
      OPAQUE(ka_); OPAQUE(ka2_); OPAQUE(q2a_); OPAQUE(va_); OPAQUE(va2_); OPAQUE(ko); OPAQUE(vo);
      auto piece = [&](int i) { if (t < 7) kv_piece(t + 1, buf ^ 1, i, ko, vo); };
      f32x16 s0 = f32x16{}, s1 = f32x16{};
      f32x4 sr = f32x4{};
      bf16x8 kA[4], kB[4];
#pragma unroll
      for (int i = 0; i < 4; ++i) kA[i] = rd128<OA>(ka_ ^ (i << 5));
#pragma unroll
      for (int i = 0; i < 4; ++i) kB[i] = rd128<OA>(ka_ ^ ((4 + i) << 5));
      piece(0);
      WAITL4(4, kA[0], kA[1], kA[2], kA[3]);
      s0 = MFMA32(kA[0], qf[0], s0);
      s0 = MFMA32(kA[1], qf[1], s0);
      SBAR(); piece(1); SBAR();
      s0 = MFMA32(kA[2], qf[2], s0);
      s0 = MFMA32(kA[3], qf[3], s0);
      SBAR();
#pragma unroll
      for (int i = 0; i < 4; ++i) kA[i] = rd128<OA + 8192>(ka_ ^ (i << 5));
      piece(2);
      WAITL4(4, kB[0], kB[1], kB[2], kB[3]);
      s0 = MFMA32(kB[0], qf[4], s0);
      s0 = MFMA32(kB[1], qf[5], s0);
      SBAR(); piece(3); SBAR();
      s0 = MFMA32(kB[2], qf[6], s0);
      s0 = MFMA32(kB[3], qf[7], s0);
      SBAR();
#pragma unroll
      for (int i = 0; i < 4; ++i) kB[i] = rd128<OA + 8192>(ka_ ^ ((4 + i) << 5));
      piece(4);
      WAITL4(4, kA[0], kA[1], kA[2], kA[3]);
      s1 = MFMA32(kA[0], qf[0], s1);
      SBAR(); piece(5); SBAR();
      s1 = MFMA32(kA[1], qf[1], s1);
      float tmax = fmaxf(fmaxf(s0[0], s0[1]), s0[2]);
#pragma unroll
      for (int r = 3; r < 9; r += 2) tmax = fmaxf(fmaxf(tmax, s0[r]), s0[r + 1]);
      SBAR();
      s1 = MFMA32(kA[2], qf[2], s1);
#pragma unroll
      for (int r = 9; r < 15; r += 2) tmax = fmaxf(fmaxf(tmax, s0[r]), s0[r + 1]);
      tmax = fmaxf(tmax, s0[15]);
      tmax = xmax32(tmax);
      float mn = fmaxf(m1, tmax);
      float alpha = __builtin_amdgcn_exp2f((m1 - mn) * CS);
      m1 = mn;
      SBAR();
      s1 = MFMA32(kA[3], qf[3], s1);
      SBAR();
      if (!__all(alpha == 1.f)) {
#pragma unroll
        for (int dt = 0; dt < 4; ++dt)
#pragma unroll
          for (int r = 0; r < 16; ++r) acc[dt][r] *= alpha;
      }
      STAMP(4);
      bf16x8 kr[4], qr[4];
#pragma unroll
      for (int ks = 0; ks < 4; ++ks) { kr[ks] = rd128<OA>(ka2_ ^ (ks << 6)); qr[ks] = rd128<0>(q2a_ ^ (ks << 6)); }
      WAITL4(8, kB[0], kB[1], kB[2], kB[3]);
      bf16x8 pf[4];
      float mc = -mn * CS;
      float ps = 0.f;
      float e[8];
      s1 = MFMA32(kB[0], qf[4], s1);
#pragma unroll
      for (int j = 0; j < 4; ++j) { e[j] = __builtin_amdgcn_exp2f(fmaf(s0[j], CS, mc)); ps += e[j]; }
      SBAR();
      s1 = MFMA32(kB[1], qf[5], s1);
#pragma unroll
      for (int j = 4; j < 8; ++j) { e[j] = __builtin_amdgcn_exp2f(fmaf(s0[j], CS, mc)); ps += e[j]; }
      { u32x4 a = {cvtpk(e[0], e[1]), cvtpk(e[2], e[3]), cvtpk(e[4], e[5]), cvtpk(e[6], e[7])}; pf[0] = __builtin_bit_cast(bf16x8, a); }
      SBAR();
      s1 = MFMA32(kB[2], qf[6], s1);
#pragma unroll
      for (int j = 0; j < 4; ++j) { e[j] = __builtin_amdgcn_exp2f(fmaf(s0[8 + j], CS, mc)); ps += e[j]; }
      SBAR();
      s1 = MFMA32(kB[3], qf[7], s1);
#pragma unroll
      for (int j = 4; j < 8; ++j) { e[j] = __builtin_amdgcn_exp2f(fmaf(s0[8 + j], CS, mc)); ps += e[j]; }
      { u32x4 a = {cvtpk(e[0], e[1]), cvtpk(e[2], e[3]), cvtpk(e[4], e[5]), cvtpk(e[6], e[7])}; pf[1] = __builtin_bit_cast(bf16x8, a); }
      l1 = l1 * alpha + ps;
      OPAQUE(l1);
      SBAR();
      piece(6);
      SBAR();
      const int v0 = va_, v1 = va_ ^ 64, v2 = va_ ^ 128, v3 = va_ ^ 192;
      s16x4 vl[2][4], vh[2][4];
      auto issue_v = [&](auto SC) {
        constexpr int s = decltype(SC)::value, u = s & 1;
        vl[u][0] = rdtr<OB + s * 4096>(v0); vh[u][0] = rdtr<OB + s * 4096 + 2048>(v0);
        vl[u][1] = rdtr<OB + s * 4096>(v1); vh[u][1] = rdtr<OB + s * 4096 + 2048>(v1);
        vl[u][2] = rdtr<OB + s * 4096>(v2); vh[u][2] = rdtr<OB + s * 4096 + 2048>(v2);
        vl[u][3] = rdtr<OB + s * 4096>(v3); vh[u][3] = rdtr<OB + s * 4096 + 2048>(v3);
      };
      issue_v(IC<0>{});
      WAITL8(8, kr[0], kr[1], kr[2], kr[3], qr[0], qr[1], qr[2], qr[3]);
#pragma unroll
      for (int ks = 0; ks < 4; ++ks) sr = MFMA16(kr[ks], qr[ks], sr);
      SBAR();
      piece(7);
      SBAR();
      STAMP(5);
      issue_v(IC<1>{});
      WAITL8(8, vl[0][0], vh[0][0], vl[0][1], vh[0][1], vl[0][2], vh[0][2], vl[0][3], vh[0][3]);
      acc[0] = MFMA32(cat8(vl[0][0], vh[0][0]), pf[0], acc[0]);
      float tm1 = fmaxf(fmaxf(s1[0], s1[1]), s1[2]);
#pragma unroll
      for (int r = 3; r < 9; r += 2) tm1 = fmaxf(fmaxf(tm1, s1[r]), s1[r + 1]);
      SBAR();
      acc[1] = MFMA32(cat8(vl[0][1], vh[0][1]), pf[0], acc[1]);
#pragma unroll
      for (int r = 9; r < 15; r += 2) tm1 = fmaxf(fmaxf(tm1, s1[r]), s1[r + 1]);
      tm1 = fmaxf(tm1, s1[15]);
      tm1 = xmax32(tm1);
      float mnb = fmaxf(m1, tm1);
      float alphab = __builtin_amdgcn_exp2f((m1 - mnb) * CS);
      m1 = mnb;
      mc = -mnb * CS;
      ps = 0.f;
      SBAR();
      acc[2] = MFMA32(cat8(vl[0][2], vh[0][2]), pf[0], acc[2]);
#pragma unroll
      for (int j = 0; j < 4; ++j) { e[j] = __builtin_amdgcn_exp2f(fmaf(s1[j], CS, mc)); ps += e[j]; }
      SBAR();
      acc[3] = MFMA32(cat8(vl[0][3], vh[0][3]), pf[0], acc[3]);
#pragma unroll
      for (int j = 4; j < 8; ++j) { e[j] = __builtin_amdgcn_exp2f(fmaf(s1[j], CS, mc)); ps += e[j]; }
      { u32x4 a = {cvtpk(e[0], e[1]), cvtpk(e[2], e[3]), cvtpk(e[4], e[5]), cvtpk(e[6], e[7])}; pf[2] = __builtin_bit_cast(bf16x8, a); }
      SBAR();
      issue_v(IC<2>{});
      WAITL8(8, vl[1][0], vh[1][0], vl[1][1], vh[1][1], vl[1][2], vh[1][2], vl[1][3], vh[1][3]);
      acc[0] = MFMA32(cat8(vl[1][0], vh[1][0]), pf[1], acc[0]);
#pragma unroll
      for (int j = 0; j < 4; ++j) { e[j] = __builtin_amdgcn_exp2f(fmaf(s1[8 + j], CS, mc)); ps += e[j]; }
      SBAR();
      acc[1] = MFMA32(cat8(vl[1][1], vh[1][1]), pf[1], acc[1]);
#pragma unroll
      for (int j = 4; j < 8; ++j) { e[j] = __builtin_amdgcn_exp2f(fmaf(s1[8 + j], CS, mc)); ps += e[j]; }
      { u32x4 a = {cvtpk(e[0], e[1]), cvtpk(e[2], e[3]), cvtpk(e[4], e[5]), cvtpk(e[6], e[7])}; pf[3] = __builtin_bit_cast(bf16x8, a); }
      l1 = l1 * alphab + ps;
      OPAQUE(l1);
      SBAR();
      acc[2] = MFMA32(cat8(vl[1][2], vh[1][2]), pf[1], acc[2]);
      float t2 = fmaxf(fmaxf(sr[0], sr[1]), fmaxf(sr[2], sr[3]));
      t2 = xmax16(t2);
      t2 = xmax32(t2);
      float mn2 = fmaxf(m2, t2);
      float alpha2 = __builtin_amdgcn_exp2f((m2 - mn2) * CS);
      m2 = mn2;
      SBAR();
      acc[3] = MFMA32(cat8(vl[1][3], vh[1][3]), pf[1], acc[3]);
      const float mc2 = -mn2 * CS;
      float e0 = __builtin_amdgcn_exp2f(fmaf(sr[0], CS, mc2));
      float e1 = __builtin_amdgcn_exp2f(fmaf(sr[1], CS, mc2));
      float e2 = __builtin_amdgcn_exp2f(fmaf(sr[2], CS, mc2));
      float e3 = __builtin_amdgcn_exp2f(fmaf(sr[3], CS, mc2));
      l2 = l2 * alpha2 + (e0 + e1) + (e2 + e3);
      OPAQUE(l2);
      u32x2 p2u = {cvtpk(e0, e1), cvtpk(e2, e3)};
      s16x4 p2 = __builtin_bit_cast(s16x4, p2u);
      SBAR();
      if (!__all(alphab == 1.f && alpha2 == 1.f)) {
#pragma unroll
        for (int dt = 0; dt < 4; ++dt)
#pragma unroll
          for (int r = 0; r < 16; ++r) acc[dt][r] *= alphab;
#pragma unroll
        for (int dd = 0; dd < 8; ++dd)
#pragma unroll
          for (int r = 0; r < 4; ++r) acc2[dd][r] *= alpha2;
      }
      STAMP(6);
      issue_v(IC<3>{});
      WAITL8(8, vl[0][0], vh[0][0], vl[0][1], vh[0][1], vl[0][2], vh[0][2], vl[0][3], vh[0][3]);
#pragma unroll
      for (int dt = 0; dt < 4; ++dt) acc[dt] = MFMA32(cat8(vl[0][dt], vh[0][dt]), pf[2], acc[dt]);
      SBAR();
      s16x4 vr[8];
#pragma unroll
      for (int dd = 0; dd < 8; ++dd) vr[dd] = rdtr<OB>(va2_ ^ (dd << 5));
      WAITL8(8, vl[1][0], vh[1][0], vl[1][1], vh[1][1], vl[1][2], vh[1][2], vl[1][3], vh[1][3]);
#pragma unroll
      for (int dt = 0; dt < 4; ++dt) acc[dt] = MFMA32(cat8(vl[1][dt], vh[1][dt]), pf[3], acc[dt]);
      WAITL8(0, vr[0], vr[1], vr[2], vr[3], vr[4], vr[5], vr[6], vr[7]);
#pragma unroll
      for (int dd = 0; dd < 8; ++dd) acc2[dd] = MFMA16K16(vr[dd], p2, acc2[dd]);
      STAMP(7);
    };
    for (int t = 0; t < 8; t += 2) {
      body(IC<0>{}, t);
      body(IC<1>{}, t + 1);
    }
  }

  int tz = threadIdx.x;
  OPAQUE(tz);
  {
    const int l31 = tz & 31, hh = (tz >> 5) & 1;
    float lt = xsum32(l1);
    float inv = 1.f / lt;
    short* orow = og + (size_t)(b * SQ + 32 * w + l31) * HD + h * DH + 4 * hh;
#pragma unroll
    for (int dt = 0; dt < 4; ++dt)
#pragma unroll
      for (int i = 0; i < 4; ++i) {
        u32x2 pk = {cvtpk(acc[dt][4 * i] * inv, acc[dt][4 * i + 1] * inv),
                    cvtpk(acc[dt][4 * i + 2] * inv, acc[dt][4 * i + 3] * inv)};
        *(u32x2*)(orow + 32 * dt + 8 * i) = pk;
      }
  }
  __syncthreads();
  {
    const int l15 = tz & 15, g = (tz >> 4) & 3;
    float lt2 = xsum32(xsum16(l2));
    float* pw = (float*)(smem + w * 8192);
#pragma unroll
    for (int dd = 0; dd < 8; ++dd) *(f32x4*)(pw + l15 * 128 + 16 * dd + 4 * g) = acc2[dd];
    float* st = (float*)(smem + 32768);
    if (g == 0) { st[w * 32 + l15] = m2; st[w * 32 + 16 + l15] = lt2; }
  }
  __syncthreads();
  {
    const int q = tz >> 4, d0 = (tz & 15) * 8;
    const float* st = (const float*)(smem + 32768);
    float mm = fmaxf(fmaxf(st[q], st[32 + q]), fmaxf(st[64 + q], st[96 + q]));
    float o[8] = {0, 0, 0, 0, 0, 0, 0, 0};
    float ls = 0.f;
#pragma unroll
    for (int ww = 0; ww < 4; ++ww) {
      float sc = __builtin_amdgcn_exp2f((st[ww * 32 + q] - mm) * CS);
      ls += st[ww * 32 + 16 + q] * sc;
      const float* pw = (const float*)(smem + ww * 8192) + q * 128 + d0;
      f32x4 a = *(const f32x4*)pw, c = *(const f32x4*)(pw + 4);
#pragma unroll
      for (int j = 0; j < 4; ++j) { o[j] += a[j] * sc; o[4 + j] += c[j] * sc; }
    }
    float inv = 1.f / ls;
    u32x4 pk = {cvtpk(o[0] * inv, o[1] * inv), cvtpk(o[2] * inv, o[3] * inv),
                cvtpk(o[4] * inv, o[5] * inv), cvtpk(o[6] * inv, o[7] * inv)};
    *(u32x4*)(og + (size_t)(b * SQ + 128 + q) * HD + h * DH + d0) = pk;
  }
}

constexpr int B_BUFB = 8192;
constexpr int B_LDS = 4 * B_BUFB;

template <int OFF> __device__ __forceinline__ bf16x8 gld128(const char* p) {
  bf16x8 r;
  asm volatile("global_load_dwordx4 %0, %1, off offset:%2" : "=v"(r) : "v"(p), "i"(OFF) : "memory");
  return r;
}

__global__ __launch_bounds__(256, 1) void oproj_kernel(const short* __restrict__ og,
                                                        const short* __restrict__ wo,
                                                        short* __restrict__ out) {
  extern __shared__ __attribute__((aligned(16))) char smem[];
  const int tid = threadIdx.x, lane = tid & 63;
  const int w = __builtin_amdgcn_readfirstlane(tid >> 6);
  const int xg = blockIdx.x & 7, idx = blockIdx.x >> 3;
  const int b = xg * 4 + (idx >> 3), nt0 = idx & 7;
  const int l31 = lane & 31, hh = lane >> 5, l15 = lane & 15, g = lane >> 4;
  const int qp = l15 >> 2, p4 = lane & 3, blk = (lane >> 4) & 1;
  const char* om = (const char*)(og + (size_t)(b * SQ + 32 * w + l31) * HD + 8 * hh);
  const char* orr = (const char*)(og + (size_t)(b * SQ + 128 + l15) * HD + 8 * g);
  const char* wsrc;
  {
    int r = 16 * w + (lane >> 3), cp = lane & 7;
    int c = cp ^ (((r >> 1) & 1) << 2);
    wsrc = (const char*)(wo + (size_t)r * DM + nt0 * 64 + c * 8);
  }
  bf16x8 of[4][6];
  auto stage = [&](auto BC, int t) {
    constexpr int buf = decltype(BC)::value;
    const char* ws = wsrc + (size_t)t * (64 * DM * 2);
    glds16(ws, smem + buf * B_BUFB + w * 2048);
    glds16(ws + 8 * DM * 2, smem + buf * B_BUFB + w * 2048 + 1024);
    const char* a = om + t * 128;
    const char* c = orr + t * 128;
    of[buf][0] = gld128<0>(a); of[buf][1] = gld128<32>(a); of[buf][2] = gld128<64>(a); of[buf][3] = gld128<96>(a);
    of[buf][4] = gld128<0>(c); of[buf][5] = gld128<64>(c);
  };

  f32x16 acc[2] = {f32x16{}, f32x16{}};
  f32x4 acc2 = f32x4{};
  constexpr int NT = HD / 64;
  const int sb = (int)(uintptr_t)smem;
  const int w0 = sb + (8 * hh + qp) * 128 + (((2 * blk + (p4 >> 1)) ^ ((qp >> 1) << 2)) << 4) + 8 * (p4 & 1);
  const int w1 = w0 ^ 64;
  const int wa2 = sb + (8 * g + qp) * 128 + (((2 * w + (p4 >> 1)) ^ ((qp >> 1) << 2)) << 4) + 8 * (p4 & 1);
  stage(IC<0>{}, 0);
  stage(IC<1>{}, 1);
  stage(IC<2>{}, 2);
  auto body = [&](auto BC, int t) {
    constexpr int buf = decltype(BC)::value;
    constexpr int OB = buf * B_BUFB;
    if (t < NT - 2) asm volatile("s_waitcnt vmcnt(16)" ::: "memory");
    else if (t == NT - 2) asm volatile("s_waitcnt vmcnt(8)" ::: "memory");
    else asm volatile("s_waitcnt vmcnt(0)" ::: "memory");
    asm volatile("" : "+v"(of[buf][0]), "+v"(of[buf][1]), "+v"(of[buf][2]), "+v"(of[buf][3]), "+v"(of[buf][4]), "+v"(of[buf][5]));
    RAW_BAR();
    s16x4 wl[4][2], wh[4][2], rl[2], rh[2];
    sfor<0, 4>([&](auto SC) {
      constexpr int s = decltype(SC)::value;
      wl[s][0] = rdtr<OB + s * 2048>(w0); wh[s][0] = rdtr<OB + s * 2048 + 512>(w0);
      wl[s][1] = rdtr<OB + s * 2048>(w1); wh[s][1] = rdtr<OB + s * 2048 + 512>(w1);
    });
    rl[0] = rdtr<OB>(wa2); rh[0] = rdtr<OB + 512>(wa2);
    rl[1] = rdtr<OB + 4096>(wa2); rh[1] = rdtr<OB + 4096 + 512>(wa2);
    bf16x8 x0 = of[buf][0], x1 = of[buf][1], x2 = of[buf][2], x3 = of[buf][3], x4 = of[buf][4], x5 = of[buf][5];
    WAITL4(15, wl[0][0], wh[0][0], wl[0][1], wh[0][1]);
    asm volatile("s_waitcnt lgkmcnt(15)" ::: "memory");
    WAITL4(12, wl[0][0], wh[0][0], wl[0][1], wh[0][1]);
    acc[0] = MFMA32(cat8(wl[0][0], wh[0][0]), x0, acc[0]);
    acc[1] = MFMA32(cat8(wl[0][1], wh[0][1]), x0, acc[1]);
    WAITL4(8, wl[1][0], wh[1][0], wl[1][1], wh[1][1]);
    acc[0] = MFMA32(cat8(wl[1][0], wh[1][0]), x1, acc[0]);
    acc[1] = MFMA32(cat8(wl[1][1], wh[1][1]), x1, acc[1]);
    WAITL4(4, wl[2][0], wh[2][0], wl[2][1], wh[2][1]);
    acc[0] = MFMA32(cat8(wl[2][0], wh[2][0]), x2, acc[0]);
    acc[1] = MFMA32(cat8(wl[2][1], wh[2][1]), x2, acc[1]);
    WAITL8(0, wl[3][0], wh[3][0], wl[3][1], wh[3][1], rl[0], rh[0], rl[1], rh[1]);
    acc[0] = MFMA32(cat8(wl[3][0], wh[3][0]), x3, acc[0]);
    acc[1] = MFMA32(cat8(wl[3][1], wh[3][1]), x3, acc[1]);
    acc2 = MFMA16(cat8(rl[0], rh[0]), x4, acc2);
    acc2 = MFMA16(cat8(rl[1], rh[1]), x5, acc2);
    SBAR();
    if (t + 3 < NT) stage(IC<(buf + 3) & 3>{}, t + 3);
    SBAR();
  };
  for (int t = 0; t < NT; t += 4) {
    body(IC<0>{}, t);
    body(IC<1>{}, t + 1);
    body(IC<2>{}, t + 2);
    body(IC<3>{}, t + 3);
  }
  {
    short* orow = out + (size_t)(b * SQ + 32 * w + l31) * DM + nt0 * 64 + 4 * hh;
#pragma unroll
    for (int nt = 0; nt < 2; ++nt)
#pragma unroll
      for (int i = 0; i < 4; ++i) {
        u32x2 pk = {cvtpk(acc[nt][4 * i], acc[nt][4 * i + 1]), cvtpk(acc[nt][4 * i + 2], acc[nt][4 * i + 3])};
        *(u32x2*)(orow + 32 * nt + 8 * i) = pk;
      }
    u32x2 pk = {cvtpk(acc2[0], acc2[1]), cvtpk(acc2[2], acc2[3])};
    *(u32x2*)(out + (size_t)(b * SQ + 128 + l15) * DM + nt0 * 64 + 16 * w + 4 * g) = pk;
  }
}

extern "C" void kernel_launch(void* const* d_in, const int* in_sizes, int n_in, void* d_out,
                              int out_size, void* d_ws, size_t ws_size, hipStream_t stream) {
  const short* x = (const short*)d_in[0];
  const short* kc = (const short*)d_in[1];
  const short* vc = (const short*)d_in[2];
  const short* wq = (const short*)d_in[3];
  const short* wo = (const short*)d_in[4];
  short* og = (short*)d_ws;
  short* out = (short*)d_out;
  hipFuncSetAttribute((const void*)attn_fused_kernel, hipFuncAttributeMaxDynamicSharedMemorySize, A_LDS);
  hipFuncSetAttribute((const void*)oproj_kernel, hipFuncAttributeMaxDynamicSharedMemorySize, B_LDS);
  attn_fused_kernel<<<NB * NH, 256, A_LDS, stream>>>(x, kc, vc, wq, og);
  oproj_kernel<<<256, 256, B_LDS, stream>>>(og, wo, out);
}
```

```cpp
#include <hip/hip_runtime.h>
#include <hip/hip_bf16.h>

typedef __attribute__((ext_vector_type(8))) short bf16x8;
typedef __attribute__((ext_vector_type(4))) short s16x4;
typedef __attribute__((ext_vector_type(16))) float f32x16;
typedef __attribute__((ext_vector_type(4))) float f32x4;
typedef __attribute__((ext_vector_type(4))) unsigned u32x4;
typedef __attribute__((ext_vector_type(2))) unsigned u32x2;
typedef __attribute__((address_space(3))) s16x4 lds_s16x4;
typedef __attribute__((address_space(3))) char lds_char;

constexpr int NB = 32, SQ = 144, DM = 512, NH = 32, SK = 512, DH = 128;
constexpr int HD = NH * DH;

#define MFMA32(a, b, c) __builtin_amdgcn_mfma_f32_32x32x16_bf16((a), (b), (c), 0, 0, 0)
#define MFMA16(a, b, c) __builtin_amdgcn_mfma_f32_16x16x32_bf16((a), (b), (c), 0, 0, 0)
#define MFMA16K16(a, b, c) __builtin_amdgcn_mfma_f32_16x16x16bf16_1k((a), (b), (c), 0, 0, 0)

__device__ __forceinline__ unsigned cvtpk(float lo, float hi) {
  unsigned r;
  asm volatile("v_cvt_pk_bf16_f32 %0, %1, %2" : "=v"(r) : "v"(lo), "v"(hi));
  return r;
}
__device__ __forceinline__ s16x4 tr_read(const char* smem_base, int off) {
  return __builtin_amdgcn_ds_read_tr16_b64_v4i16((lds_s16x4*)((lds_char*)smem_base + off));
}
__device__ __forceinline__ bf16x8 cat8(s16x4 lo, s16x4 hi) {
  return __builtin_shufflevector(lo, hi, 0, 1, 2, 3, 4, 5, 6, 7);
}
__device__ __forceinline__ void glds16(const void* g, char* l) {
  __builtin_amdgcn_global_load_lds((const unsigned*)g, (unsigned*)l, 16, 0, 0);
}
__device__ __forceinline__ float xmax32(float v) {
  auto rr = __builtin_amdgcn_permlane32_swap(__float_as_uint(v), __float_as_uint(v), false, false);
  return fmaxf(__uint_as_float(rr[0]), __uint_as_float(rr[1]));
}
__device__ __forceinline__ float xsum32(float v) {
  auto rr = __builtin_amdgcn_permlane32_swap(__float_as_uint(v), __float_as_uint(v), false, false);
  return __uint_as_float(rr[0]) + __uint_as_float(rr[1]);
}
__device__ __forceinline__ float xmax16(float v) {
  auto rr = __builtin_amdgcn_permlane16_swap(__float_as_uint(v), __float_as_uint(v), false, false);
  return fmaxf(__uint_as_float(rr[0]), __uint_as_float(rr[1]));
}
__device__ __forceinline__ float xsum16(float v) {
  auto rr = __builtin_amdgcn_permlane16_swap(__float_as_uint(v), __float_as_uint(v), false, false);
  return __uint_as_float(rr[0]) + __uint_as_float(rr[1]);
}
template <int OFF> __device__ __forceinline__ bf16x8 rd128(int a) {
  bf16x8 r;
  asm volatile("ds_read_b128 %0, %1 offset:%2" : "=v"(r) : "v"(a), "i"(OFF) : "memory");
  return r;
}
template <int OFF> __device__ __forceinline__ s16x4 rdtr(int a) {
  s16x4 r;
  asm volatile("ds_read_b64_tr_b16 %0, %1 offset:%2" : "=v"(r) : "v"(a), "i"(OFF) : "memory");
  return r;
}
#define WAITL1(N, a) asm volatile("s_waitcnt lgkmcnt(" #N ")" : "+v"(a) :: "memory")
#define WAITL2(N, a, b) asm volatile("s_waitcnt lgkmcnt(" #N ")" : "+v"(a), "+v"(b) :: "memory")
#define WAITL4(N, a, b, c, d) asm volatile("s_waitcnt lgkmcnt(" #N ")" : "+v"(a), "+v"(b), "+v"(c), "+v"(d) :: "memory")
#define WAITL8(N, a, b, c, d, e, f, g_, h_) asm volatile("s_waitcnt lgkmcnt(" #N ")" : "+v"(a), "+v"(b), "+v"(c), "+v"(d), "+v"(e), "+v"(f), "+v"(g_), "+v"(h_) :: "memory")
#define WAIT_VM0() asm volatile("s_waitcnt vmcnt(0)" ::: "memory")
#define RAW_BAR() __builtin_amdgcn_s_barrier()
#define SBAR() __builtin_amdgcn_sched_barrier(0)

constexpr int A_BUFA = 20480;
constexpr int A_BUFB = 16384;
constexpr int A_OFF_A = 0;
constexpr int A_OFF_B = 2 * A_BUFA;
constexpr int A_OFF_Q2 = A_OFF_B + 2 * A_BUFB;
constexpr int A_LDS = A_OFF_Q2 + 4096;

template <int N> struct IC { static constexpr int value = N; };
template <int I, int N, class F> __device__ __forceinline__ void sfor(F&& f) {
  if constexpr (I < N) { f(IC<I>{}); sfor<I + 1, N>(f); }
}
#define OPAQUE(v) asm volatile("" : "+v"(v))
#define STAMP(i)

__global__ __launch_bounds__(256, 2) void attn_fused_kernel(
    const short* __restrict__ x, const short* __restrict__ kc, const short* __restrict__ vc,
    const short* __restrict__ wq, short* __restrict__ og) {
  extern __shared__ __attribute__((aligned(16))) char smem[];
  const int tid = threadIdx.x, lane = tid & 63;
  const int w = __builtin_amdgcn_readfirstlane(tid >> 6);
  const int pidx = (blockIdx.x & 7) * 128 + (blockIdx.x >> 3);
  const int b = pidx >> 5, h = pidx & 31;

  const char* xb = (const char*)(x + (size_t)b * SQ * DM);
  const char* wqh = (const char*)(wq + h * DH);
  const char* kb = (const char*)(kc + (size_t)pidx * SK * DH);
  const char* vb = (const char*)(vc + (size_t)pidx * SK * DH);

  const int l31 = lane & 31, hh = lane >> 5, l15 = lane & 15, g = lane >> 4;
  const int qp = l15 >> 2, p4 = lane & 3, blk = (lane >> 4) & 1;
  const int sb = (int)(uintptr_t)smem;

  f32x16 acc[4];
#pragma unroll
  for (int i = 0; i < 4; ++i) acc[i] = f32x16{};
  f32x4 acc2[8];
#pragma unroll
  for (int i = 0; i < 8; ++i) acc2[i] = f32x4{};

  const unsigned kvsrc = (16 * w + g) * 256 + (l15 << 4);
  const int kdst = A_OFF_A + (16 * w + g) * 256 + ((l15 ^ g) << 4);
  const int vdst = A_OFF_B + (16 * w + g) * 256 + ((l15 ^ (g << 2)) << 4);
  bf16x8 st0, st1, st2, st3;
#define LD_KV(base, t, so) do { const char* p_ = (base) + (t) * 16384; \
    st0 = *(const bf16x8*)(p_ + (so)); st1 = *(const bf16x8*)(p_ + ((so) + 1024u)); \
    st2 = *(const bf16x8*)(p_ + ((so) + 2048u)); st3 = *(const bf16x8*)(p_ + ((so) + 3072u)); } while (0)
#define WR_K(buf, d) do { char* p_ = smem + (buf) * A_BUFA; \
    *(bf16x8*)(p_ + (d)) = st0; *(bf16x8*)(p_ + ((d) ^ 64) + 1024) = st1; \
    *(bf16x8*)(p_ + ((d) ^ 128) + 2048) = st2; *(bf16x8*)(p_ + ((d) ^ 192) + 3072) = st3; } while (0)
#define WR_V(buf, d) do { char* p_ = smem + (buf) * A_BUFB + (d); \
    *(bf16x8*)(p_) = st0; *(bf16x8*)(p_ + 1024) = st1; *(bf16x8*)(p_ + 2048) = st2; *(bf16x8*)(p_ + 3072) = st3; } while (0)

  {
    unsigned xsrc[5];
    int xdst[5];
#pragma unroll
    for (int ii = 0; ii < 5; ++ii) {
      int r = 8 * (w * 5 + ii) + (lane >> 3), cp = lane & 7;
      int rs = r < SQ ? r : SQ - 1;
      xsrc[ii] = rs * 1024 + cp * 16;
      xdst[ii] = A_OFF_A + r * 128 + ((cp ^ ((r >> 1) & 7)) << 4);
    }
    const unsigned wsrc = (16 * w + g) * (HD * 2) + (l15 << 4);
    bf16x8 sq0, sq1, sq2, sq3, sq4, sq5, sq6, sq7, sq8;
#define LD_Q(t) do { const char* px_ = xb + (t) * 128; const char* pw_ = wqh + (size_t)(t) * (64 * HD * 2); \
    sq0 = *(const bf16x8*)(px_ + xsrc[0]); sq1 = *(const bf16x8*)(px_ + xsrc[1]); sq2 = *(const bf16x8*)(px_ + xsrc[2]); \
    sq3 = *(const bf16x8*)(px_ + xsrc[3]); sq4 = *(const bf16x8*)(px_ + xsrc[4]); \
    sq5 = *(const bf16x8*)(pw_ + wsrc); sq6 = *(const bf16x8*)(pw_ + (wsrc + 4u * HD * 2)); \
    sq7 = *(const bf16x8*)(pw_ + (wsrc + 8u * HD * 2)); sq8 = *(const bf16x8*)(pw_ + (wsrc + 12u * HD * 2)); } while (0)
#define WR_Q(buf) do { char* pa_ = smem + (buf) * A_BUFA; char* pb_ = smem + (buf) * A_BUFB + vdst; \
    *(bf16x8*)(pa_ + xdst[0]) = sq0; *(bf16x8*)(pa_ + xdst[1]) = sq1; *(bf16x8*)(pa_ + xdst[2]) = sq2; \
    *(bf16x8*)(pa_ + xdst[3]) = sq3; *(bf16x8*)(pa_ + xdst[4]) = sq4; \
    *(bf16x8*)(pb_) = sq5; *(bf16x8*)(pb_ + 1024) = sq6; *(bf16x8*)(pb_ + 2048) = sq7; *(bf16x8*)(pb_ + 3072) = sq8; } while (0)
    const int xa = sb + A_OFF_A + (32 * w + l31) * 128 + ((hh ^ ((l31 >> 1) & 7)) << 4);
    const int wa = sb + A_OFF_B + (8 * hh + qp) * 256 + (((2 * blk + (p4 >> 1)) ^ (qp << 2)) << 4) + 8 * (p4 & 1);
    const int xa2 = sb + A_OFF_A + (128 + l15) * 128 + ((g ^ ((l15 >> 1) & 7)) << 4);
    const int wa2 = sb + A_OFF_B + (8 * g + qp) * 256 + (((4 * w + (p4 >> 1)) ^ (qp << 2)) << 4) + 8 * (p4 & 1);
    auto body = [&](auto BC, int t) {
      constexpr int buf = decltype(BC)::value;
      constexpr int OA = buf * A_BUFA, OB = buf * A_BUFB;
      asm volatile("s_waitcnt lgkmcnt(0)" ::: "memory");
      RAW_BAR();
      STAMP(0);
      if (t < 7) {
        WR_Q(buf ^ 1);
        if (t < 6) LD_Q(t + 2);
        else LD_KV(kb, 0, kvsrc);
      } else {
        WR_K(buf ^ 1, kdst);
        LD_KV(vb, 0, kvsrc);
      }
      SBAR();
      STAMP(1);
      const int w0 = wa, w1 = wa ^ 64, w2 = wa ^ 128, w3 = wa ^ 192;
      bf16x8 xf[2];
      s16x4 wl[2][4], wh[2][4];
      bf16x8 xr[2];
      s16x4 rl[2][2], rh[2][2];
      auto issue = [&](auto SC) {
        constexpr int s = decltype(SC)::value, u = s & 1;
        xf[u] = rd128<OA>(xa ^ (s << 5));
        wl[u][0] = rdtr<OB + s * 4096>(w0); wh[u][0] = rdtr<OB + s * 4096 + 1024>(w0);
        wl[u][1] = rdtr<OB + s * 4096>(w1); wh[u][1] = rdtr<OB + s * 4096 + 1024>(w1);
        wl[u][2] = rdtr<OB + s * 4096>(w2); wh[u][2] = rdtr<OB + s * 4096 + 1024>(w2);
        wl[u][3] = rdtr<OB + s * 4096>(w3); wh[u][3] = rdtr<OB + s * 4096 + 1024>(w3);
      };
      issue(IC<0>{});
      sfor<0, 4>([&](auto SC) {
        constexpr int s = decltype(SC)::value, u = s & 1;
        if constexpr (s < 3) {
          issue(IC<s + 1>{});
          asm volatile("s_waitcnt lgkmcnt(9)" : "+v"(xf[u]), "+v"(wl[u][0]), "+v"(wh[u][0]), "+v"(wl[u][1]), "+v"(wh[u][1]),
                       "+v"(wl[u][2]), "+v"(wh[u][2]), "+v"(wl[u][3]), "+v"(wh[u][3]) :: "memory");
        } else {
          xr[0] = rd128<OA>(xa2); xr[1] = rd128<OA>(xa2 ^ 64);
          rl[0][0] = rdtr<OB>(wa2); rh[0][0] = rdtr<OB + 1024>(wa2);
          rl[0][1] = rdtr<OB>(wa2 ^ 32); rh[0][1] = rdtr<OB + 1024>(wa2 ^ 32);
          rl[1][0] = rdtr<OB + 8192>(wa2); rh[1][0] = rdtr<OB + 8192 + 1024>(wa2);
          rl[1][1] = rdtr<OB + 8192>(wa2 ^ 32); rh[1][1] = rdtr<OB + 8192 + 1024>(wa2 ^ 32);
          asm volatile("s_waitcnt lgkmcnt(10)" : "+v"(xf[u]), "+v"(wl[u][0]), "+v"(wh[u][0]), "+v"(wl[u][1]), "+v"(wh[u][1]),
                       "+v"(wl[u][2]), "+v"(wh[u][2]), "+v"(wl[u][3]), "+v"(wh[u][3]) :: "memory");
        }
#pragma unroll
        for (int dt = 0; dt < 4; ++dt) acc[dt] = MFMA32(cat8(wl[u][dt], wh[u][dt]), xf[u], acc[dt]);
        SBAR();
      });
      asm volatile("s_waitcnt lgkmcnt(0)" : "+v"(xr[0]), "+v"(xr[1]), "+v"(rl[0][0]), "+v"(rh[0][0]), "+v"(rl[0][1]), "+v"(rh[0][1]),
                   "+v"(rl[1][0]), "+v"(rh[1][0]), "+v"(rl[1][1]), "+v"(rh[1][1]) :: "memory");
#pragma unroll
      for (int s2 = 0; s2 < 2; ++s2)
#pragma unroll
        for (int e = 0; e < 2; ++e) acc2[e] = MFMA16(cat8(rl[s2][e], rh[s2][e]), xr[s2], acc2[e]);
      if (t == 7) {
        SBAR();
        WR_V(buf ^ 1, vdst);
        LD_KV(kb, 1, kvsrc);
      }
      STAMP(2);
    };
    LD_Q(0);
    WR_Q(0);
    LD_Q(1);
    for (int t = 0; t < 8; t += 2) {
      body(IC<0>{}, t);
      body(IC<1>{}, t + 1);
    }
  }
  bf16x8 qf[8];
#pragma unroll
  for (int dt = 0; dt < 4; ++dt) {
#pragma unroll
    for (int s = 0; s < 2; ++s) {
      unsigned a0 = cvtpk(acc[dt][8 * s + 0], acc[dt][8 * s + 1]);
      unsigned a1 = cvtpk(acc[dt][8 * s + 2], acc[dt][8 * s + 3]);
      unsigned b0 = cvtpk(acc[dt][8 * s + 4], acc[dt][8 * s + 5]);
      unsigned b1 = cvtpk(acc[dt][8 * s + 6], acc[dt][8 * s + 7]);
      auto r0 = __builtin_amdgcn_permlane32_swap(a0, b0, false, false);
      auto r1 = __builtin_amdgcn_permlane32_swap(a1, b1, false, false);
      u32x4 wv = {r0[0], r1[0], r0[1], r1[1]};
      qf[2 * dt + s] = __builtin_bit_cast(bf16x8, wv);
    }
  }
  {
#pragma unroll
    for (int e = 0; e < 2; ++e) {
      int dd = 2 * w + e;
      u32x2 pk = {cvtpk(acc2[e][0], acc2[e][1]), cvtpk(acc2[e][2], acc2[e][3])};
      int ch = (2 * dd + (g >> 1)) ^ l15;
      *(u32x2*)(smem + A_OFF_Q2 + l15 * 256 + (ch << 4) + 8 * (g & 1)) = pk;
    }
  }
#pragma unroll
  for (int i = 0; i < 4; ++i) acc[i] = f32x16{};
#pragma unroll
  for (int i = 0; i < 8; ++i) acc2[i] = f32x4{};

  constexpr float CS = 0.088388347648318440f * 1.4426950408889634f;
  float m1 = -1e30f, l1 = 0.f, m2 = -1e30f, l2 = 0.f;
  {
    int ka = sb + A_OFF_A + l31 * 256 + ((hh ^ l15) << 4);
    int ka2 = sb + A_OFF_A + (16 * w + l15) * 256 + ((g ^ l15) << 4);
    int q2a = sb + A_OFF_Q2 + l15 * 256 + ((g ^ l15) << 4);
    int va = sb + A_OFF_B + (4 * hh + qp) * 256 + (((2 * blk + (p4 >> 1)) ^ (qp << 2)) << 4) + 8 * (p4 & 1);
    int va2 = sb + A_OFF_B + (16 * w + 4 * g + qp) * 256 + (((p4 >> 1) ^ (qp << 2)) << 4) + 8 * (p4 & 1);
    auto body = [&](auto BC, int t) {
      constexpr int buf = decltype(BC)::value;
      constexpr int OA = buf * A_BUFA, OB = buf * A_BUFB;
      asm volatile("s_waitcnt lgkmcnt(0)" ::: "memory");
      RAW_BAR();
      STAMP(3);
      int ka_ = ka, ka2_ = ka2, q2a_ = q2a, va_ = va, va2_ = va2;
      unsigned so = kvsrc;
      OPAQUE(ka_); OPAQUE(ka2_); OPAQUE(q2a_); OPAQUE(va_); OPAQUE(va2_); OPAQUE(so);
      const int kd = A_OFF_A + (so ^ (((so >> 8) & 3) << 4));
      const int vd = A_OFF_B + (so ^ (((so >> 8) & 3) << 6));
      if (t < 7) {
        WR_K(buf ^ 1, kd);
        LD_KV(vb, t + 1, so);
      }
      SBAR();
      f32x16 s0 = f32x16{}, s1 = f32x16{};
      f32x4 sr = f32x4{};
      bf16x8 kA[4], kB[4];
#pragma unroll
      for (int i = 0; i < 4; ++i) kA[i] = rd128<OA>(ka_ ^ (i << 5));
#pragma unroll
      for (int i = 0; i < 4; ++i) kB[i] = rd128<OA>(ka_ ^ ((4 + i) << 5));
      WAITL4(4, kA[0], kA[1], kA[2], kA[3]);
      s0 = MFMA32(kA[0], qf[0], s0);
      s0 = MFMA32(kA[1], qf[1], s0);
      SBAR();
      s0 = MFMA32(kA[2], qf[2], s0);
      s0 = MFMA32(kA[3], qf[3], s0);
      SBAR();
#pragma unroll
      for (int i = 0; i < 4; ++i) kA[i] = rd128<OA + 8192>(ka_ ^ (i << 5));
      WAITL4(4, kB[0], kB[1], kB[2], kB[3]);
      s0 = MFMA32(kB[0], qf[4], s0);
      s0 = MFMA32(kB[1], qf[5], s0);
      SBAR();
      s0 = MFMA32(kB[2], qf[6], s0);
      s0 = MFMA32(kB[3], qf[7], s0);
      SBAR();
#pragma unroll
      for (int i = 0; i < 4; ++i) kB[i] = rd128<OA + 8192>(ka_ ^ ((4 + i) << 5));
      WAITL4(4, kA[0], kA[1], kA[2], kA[3]);
      s1 = MFMA32(kA[0], qf[0], s1);
      SBAR();
      s1 = MFMA32(kA[1], qf[1], s1);
      float tmax = fmaxf(fmaxf(s0[0], s0[1]), s0[2]);
#pragma unroll
      for (int r = 3; r < 9; r += 2) tmax = fmaxf(fmaxf(tmax, s0[r]), s0[r + 1]);
      SBAR();
      s1 = MFMA32(kA[2], qf[2], s1);
#pragma unroll
      for (int r = 9; r < 15; r += 2) tmax = fmaxf(fmaxf(tmax, s0[r]), s0[r + 1]);
      tmax = fmaxf(tmax, s0[15]);
      tmax = xmax32(tmax);
      float mn = fmaxf(m1, tmax);
      float alpha = __builtin_amdgcn_exp2f((m1 - mn) * CS);
      m1 = mn;
      SBAR();
      s1 = MFMA32(kA[3], qf[3], s1);
      SBAR();
      if (!__all(alpha == 1.f)) {
#pragma unroll
        for (int dt = 0; dt < 4; ++dt)
#pragma unroll
          for (int r = 0; r < 16; ++r) acc[dt][r] *= alpha;
      }
      STAMP(4);
      bf16x8 kr[4], qr[4];
#pragma unroll
      for (int ks = 0; ks < 4; ++ks) { kr[ks] = rd128<OA>(ka2_ ^ (ks << 6)); qr[ks] = rd128<0>(q2a_ ^ (ks << 6)); }
      WAITL4(8, kB[0], kB[1], kB[2], kB[3]);
      bf16x8 pf[4];
      float mc = -mn * CS;
      float ps = 0.f;
      float e[8];
      s1 = MFMA32(kB[0], qf[4], s1);
#pragma unroll
      for (int j = 0; j < 4; ++j) { e[j] = __builtin_amdgcn_exp2f(fmaf(s0[j], CS, mc)); ps += e[j]; }
      SBAR();
      s1 = MFMA32(kB[1], qf[5], s1);
#pragma unroll
      for (int j = 4; j < 8; ++j) { e[j] = __builtin_amdgcn_exp2f(fmaf(s0[j], CS, mc)); ps += e[j]; }
      { u32x4 a = {cvtpk(e[0], e[1]), cvtpk(e[2], e[3]), cvtpk(e[4], e[5]), cvtpk(e[6], e[7])}; pf[0] = __builtin_bit_cast(bf16x8, a); }
      SBAR();
      s1 = MFMA32(kB[2], qf[6], s1);
#pragma unroll
      for (int j = 0; j < 4; ++j) { e[j] = __builtin_amdgcn_exp2f(fmaf(s0[8 + j], CS, mc)); ps += e[j]; }
      SBAR();
      s1 = MFMA32(kB[3], qf[7], s1);
#pragma unroll
      for (int j = 4; j < 8; ++j) { e[j] = __builtin_amdgcn_exp2f(fmaf(s0[8 + j], CS, mc)); ps += e[j]; }
      { u32x4 a = {cvtpk(e[0], e[1]), cvtpk(e[2], e[3]), cvtpk(e[4], e[5]), cvtpk(e[6], e[7])}; pf[1] = __builtin_bit_cast(bf16x8, a); }
      l1 = l1 * alpha + ps;
      OPAQUE(l1);
      SBAR();
      SBAR();
      const int v0 = va_, v1 = va_ ^ 64, v2 = va_ ^ 128, v3 = va_ ^ 192;
      s16x4 vl[2][4], vh[2][4];
      auto issue_v = [&](auto SC) {
        constexpr int s = decltype(SC)::value, u = s & 1;
        vl[u][0] = rdtr<OB + s * 4096>(v0); vh[u][0] = rdtr<OB + s * 4096 + 2048>(v0);
        vl[u][1] = rdtr<OB + s * 4096>(v1); vh[u][1] = rdtr<OB + s * 4096 + 2048>(v1);
        vl[u][2] = rdtr<OB + s * 4096>(v2); vh[u][2] = rdtr<OB + s * 4096 + 2048>(v2);
        vl[u][3] = rdtr<OB + s * 4096>(v3); vh[u][3] = rdtr<OB + s * 4096 + 2048>(v3);
      };
      issue_v(IC<0>{});
      WAITL8(8, kr[0], kr[1], kr[2], kr[3], qr[0], qr[1], qr[2], qr[3]);
#pragma unroll
      for (int ks = 0; ks < 4; ++ks) sr = MFMA16(kr[ks], qr[ks], sr);
      SBAR();
      if (t < 7) {
        WR_V(buf ^ 1, vd);
        if (t < 6) LD_KV(kb, t + 2, so);
      }
      SBAR();
      STAMP(5);
      issue_v(IC<1>{});
      WAITL8(8, vl[0][0], vh[0][0], vl[0][1], vh[0][1], vl[0][2], vh[0][2], vl[0][3], vh[0][3]);
      acc[0] = MFMA32(cat8(vl[0][0], vh[0][0]), pf[0], acc[0]);
      float tm1 = fmaxf(fmaxf(s1[0], s1[1]), s1[2]);
#pragma unroll
      for (int r = 3; r < 9; r += 2) tm1 = fmaxf(fmaxf(tm1, s1[r]), s1[r + 1]);
      SBAR();
      acc[1] = MFMA32(cat8(vl[0][1], vh[0][1]), pf[0], acc[1]);
#pragma unroll
      for (int r = 9; r < 15; r += 2) tm1 = fmaxf(fmaxf(tm1, s1[r]), s1[r + 1]);
      tm1 = fmaxf(tm1, s1[15]);
      tm1 = xmax32(tm1);
      float mnb = fmaxf(m1, tm1);
      float alphab = __builtin_amdgcn_exp2f((m1 - mnb) * CS);
      m1 = mnb;
      mc = -mnb * CS;
      ps = 0.f;
      SBAR();
      acc[2] = MFMA32(cat8(vl[0][2], vh[0][2]), pf[0], acc[2]);
#pragma unroll
      for (int j = 0; j < 4; ++j) { e[j] = __builtin_amdgcn_exp2f(fmaf(s1[j], CS, mc)); ps += e[j]; }
      SBAR();
      acc[3] = MFMA32(cat8(vl[0][3], vh[0][3]), pf[0], acc[3]);
#pragma unroll
      for (int j = 4; j < 8; ++j) { e[j] = __builtin_amdgcn_exp2f(fmaf(s1[j], CS, mc)); ps += e[j]; }
      { u32x4 a = {cvtpk(e[0], e[1]), cvtpk(e[2], e[3]), cvtpk(e[4], e[5]), cvtpk(e[6], e[7])}; pf[2] = __builtin_bit_cast(bf16x8, a); }
      SBAR();
      issue_v(IC<2>{});
      WAITL8(8, vl[1][0], vh[1][0], vl[1][1], vh[1][1], vl[1][2], vh[1][2], vl[1][3], vh[1][3]);
      acc[0] = MFMA32(cat8(vl[1][0], vh[1][0]), pf[1], acc[0]);
#pragma unroll
      for (int j = 0; j < 4; ++j) { e[j] = __builtin_amdgcn_exp2f(fmaf(s1[8 + j], CS, mc)); ps += e[j]; }
      SBAR();
      acc[1] = MFMA32(cat8(vl[1][1], vh[1][1]), pf[1], acc[1]);
#pragma unroll
      for (int j = 4; j < 8; ++j) { e[j] = __builtin_amdgcn_exp2f(fmaf(s1[8 + j], CS, mc)); ps += e[j]; }
      { u32x4 a = {cvtpk(e[0], e[1]), cvtpk(e[2], e[3]), cvtpk(e[4], e[5]), cvtpk(e[6], e[7])}; pf[3] = __builtin_bit_cast(bf16x8, a); }
      l1 = l1 * alphab + ps;
      OPAQUE(l1);
      SBAR();
      acc[2] = MFMA32(cat8(vl[1][2], vh[1][2]), pf[1], acc[2]);
      float t2 = fmaxf(fmaxf(sr[0], sr[1]), fmaxf(sr[2], sr[3]));
      t2 = xmax16(t2);
      t2 = xmax32(t2);
      float mn2 = fmaxf(m2, t2);
      float alpha2 = __builtin_amdgcn_exp2f((m2 - mn2) * CS);
      m2 = mn2;
      SBAR();
      acc[3] = MFMA32(cat8(vl[1][3], vh[1][3]), pf[1], acc[3]);
      const float mc2 = -mn2 * CS;
      float e0 = __builtin_amdgcn_exp2f(fmaf(sr[0], CS, mc2));
      float e1 = __builtin_amdgcn_exp2f(fmaf(sr[1], CS, mc2));
      float e2 = __builtin_amdgcn_exp2f(fmaf(sr[2], CS, mc2));
      float e3 = __builtin_amdgcn_exp2f(fmaf(sr[3], CS, mc2));
      l2 = l2 * alpha2 + (e0 + e1) + (e2 + e3);
      OPAQUE(l2);
      u32x2 p2u = {cvtpk(e0, e1), cvtpk(e2, e3)};
      s16x4 p2 = __builtin_bit_cast(s16x4, p2u);
      SBAR();
      if (!__all(alphab == 1.f && alpha2 == 1.f)) {
#pragma unroll
        for (int dt = 0; dt < 4; ++dt)
#pragma unroll
          for (int r = 0; r < 16; ++r) acc[dt][r] *= alphab;
#pragma unroll
        for (int dd = 0; dd < 8; ++dd)
#pragma unroll
          for (int r = 0; r < 4; ++r) acc2[dd][r] *= alpha2;
      }
      STAMP(6);
      issue_v(IC<3>{});
      WAITL8(8, vl[0][0], vh[0][0], vl[0][1], vh[0][1], vl[0][2], vh[0][2], vl[0][3], vh[0][3]);
#pragma unroll
      for (int dt = 0; dt < 4; ++dt) acc[dt] = MFMA32(cat8(vl[0][dt], vh[0][dt]), pf[2], acc[dt]);
      SBAR();
      s16x4 vr[8];
#pragma unroll
      for (int dd = 0; dd < 8; ++dd) vr[dd] = rdtr<OB>(va2_ ^ (dd << 5));
      WAITL8(8, vl[1][0], vh[1][0], vl[1][1], vh[1][1], vl[1][2], vh[1][2], vl[1][3], vh[1][3]);
#pragma unroll
      for (int dt = 0; dt < 4; ++dt) acc[dt] = MFMA32(cat8(vl[1][dt], vh[1][dt]), pf[3], acc[dt]);
      WAITL8(0, vr[0], vr[1], vr[2], vr[3], vr[4], vr[5], vr[6], vr[7]);
#pragma unroll
      for (int dd = 0; dd < 8; ++dd) acc2[dd] = MFMA16K16(vr[dd], p2, acc2[dd]);
      STAMP(7);
    };
    for (int t = 0; t < 8; t += 2) {
      body(IC<0>{}, t);
      body(IC<1>{}, t + 1);
    }
  }

  int tz = threadIdx.x;
  OPAQUE(tz);
  {
    const int l31 = tz & 31, hh = (tz >> 5) & 1;
    float lt = xsum32(l1);
    float inv = 1.f / lt;
    short* orow = og + (size_t)(b * SQ + 32 * w + l31) * HD + h * DH + 4 * hh;
#pragma unroll
    for (int dt = 0; dt < 4; ++dt)
#pragma unroll
      for (int i = 0; i < 4; ++i) {
        u32x2 pk = {cvtpk(acc[dt][4 * i] * inv, acc[dt][4 * i + 1] * inv),
                    cvtpk(acc[dt][4 * i + 2] * inv, acc[dt][4 * i + 3] * inv)};
        *(u32x2*)(orow + 32 * dt + 8 * i) = pk;
      }
  }
  __syncthreads();
  {
    const int l15 = tz & 15, g = (tz >> 4) & 3;
    float lt2 = xsum32(xsum16(l2));
    float* pw = (float*)(smem + w * 8192);
#pragma unroll
    for (int dd = 0; dd < 8; ++dd) *(f32x4*)(pw + l15 * 128 + 16 * dd + 4 * g) = acc2[dd];
    float* st = (float*)(smem + 32768);
    if (g == 0) { st[w * 32 + l15] = m2; st[w * 32 + 16 + l15] = lt2; }
  }
  __syncthreads();
  {
    const int q = tz >> 4, d0 = (tz & 15) * 8;
    const float* st = (const float*)(smem + 32768);
    float mm = fmaxf(fmaxf(st[q], st[32 + q]), fmaxf(st[64 + q], st[96 + q]));
    float o[8] = {0, 0, 0, 0, 0, 0, 0, 0};
    float ls = 0.f;
#pragma unroll
    for (int ww = 0; ww < 4; ++ww) {
      float sc = __builtin_amdgcn_exp2f((st[ww * 32 + q] - mm) * CS);
      ls += st[ww * 32 + 16 + q] * sc;
      const float* pw = (const float*)(smem + ww * 8192) + q * 128 + d0;
      f32x4 a = *(const f32x4*)pw, c = *(const f32x4*)(pw + 4);
#pragma unroll
      for (int j = 0; j < 4; ++j) { o[j] += a[j] * sc; o[4 + j] += c[j] * sc; }
    }
    float inv = 1.f / ls;
    u32x4 pk = {cvtpk(o[0] * inv, o[1] * inv), cvtpk(o[2] * inv, o[3] * inv),
                cvtpk(o[4] * inv, o[5] * inv), cvtpk(o[6] * inv, o[7] * inv)};
    *(u32x4*)(og + (size_t)(b * SQ + 128 + q) * HD + h * DH + d0) = pk;
  }
}

constexpr int B_BUFB = 8192;
constexpr int B_LDS = 4 * B_BUFB;

template <int OFF> __device__ __forceinline__ bf16x8 gld128(const char* p) {
  bf16x8 r;
  asm volatile("global_load_dwordx4 %0, %1, off offset:%2" : "=v"(r) : "v"(p), "i"(OFF) : "memory");
  return r;
}

__global__ __launch_bounds__(256, 1) void oproj_kernel(const short* __restrict__ og,
                                                        const short* __restrict__ wo,
                                                        short* __restrict__ out) {
  extern __shared__ __attribute__((aligned(16))) char smem[];
  const int tid = threadIdx.x, lane = tid & 63;
  const int w = __builtin_amdgcn_readfirstlane(tid >> 6);
  const int xg = blockIdx.x & 7, idx = blockIdx.x >> 3;
  const int b = xg * 4 + (idx >> 3), nt0 = idx & 7;
  const int l31 = lane & 31, hh = lane >> 5, l15 = lane & 15, g = lane >> 4;
  const int qp = l15 >> 2, p4 = lane & 3, blk = (lane >> 4) & 1;
  const char* om = (const char*)(og + (size_t)(b * SQ + 32 * w + l31) * HD + 8 * hh);
  const char* orr = (const char*)(og + (size_t)(b * SQ + 128 + l15) * HD + 8 * g);
  const char* wsrc;
  {
    int r = 16 * w + (lane >> 3), cp = lane & 7;
    int c = cp ^ (((r >> 1) & 1) << 2);
    wsrc = (const char*)(wo + (size_t)r * DM + nt0 * 64 + c * 8);
  }
  bf16x8 of[4][6];
  auto stage = [&](auto BC, int t) {
    constexpr int buf = decltype(BC)::value;
    const char* ws = wsrc + (size_t)t * (64 * DM * 2);
    glds16(ws, smem + buf * B_BUFB + w * 2048);
    glds16(ws + 8 * DM * 2, smem + buf * B_BUFB + w * 2048 + 1024);
    const char* a = om + t * 128;
    const char* c = orr + t * 128;
    of[buf][0] = gld128<0>(a); of[buf][1] = gld128<32>(a); of[buf][2] = gld128<64>(a); of[buf][3] = gld128<96>(a);
    of[buf][4] = gld128<0>(c); of[buf][5] = gld128<64>(c);
  };

  f32x16 acc[2] = {f32x16{}, f32x16{}};
  f32x4 acc2 = f32x4{};
  constexpr int NT = HD / 64;
  const int sb = (int)(uintptr_t)smem;
  const int w0 = sb + (8 * hh + qp) * 128 + (((2 * blk + (p4 >> 1)) ^ ((qp >> 1) << 2)) << 4) + 8 * (p4 & 1);
  const int w1 = w0 ^ 64;
  const int wa2 = sb + (8 * g + qp) * 128 + (((2 * w + (p4 >> 1)) ^ ((qp >> 1) << 2)) << 4) + 8 * (p4 & 1);
  stage(IC<0>{}, 0);
  stage(IC<1>{}, 1);
  stage(IC<2>{}, 2);
  auto body = [&](auto BC, int t) {
    constexpr int buf = decltype(BC)::value;
    constexpr int OB = buf * B_BUFB;
    if (t < NT - 2) asm volatile("s_waitcnt vmcnt(16)" ::: "memory");
    else if (t == NT - 2) asm volatile("s_waitcnt vmcnt(8)" ::: "memory");
    else asm volatile("s_waitcnt vmcnt(0)" ::: "memory");
    asm volatile("" : "+v"(of[buf][0]), "+v"(of[buf][1]), "+v"(of[buf][2]), "+v"(of[buf][3]), "+v"(of[buf][4]), "+v"(of[buf][5]));
    RAW_BAR();
    s16x4 wl[4][2], wh[4][2], rl[2], rh[2];
    sfor<0, 4>([&](auto SC) {
      constexpr int s = decltype(SC)::value;
      wl[s][0] = rdtr<OB + s * 2048>(w0); wh[s][0] = rdtr<OB + s * 2048 + 512>(w0);
      wl[s][1] = rdtr<OB + s * 2048>(w1); wh[s][1] = rdtr<OB + s * 2048 + 512>(w1);
    });
    rl[0] = rdtr<OB>(wa2); rh[0] = rdtr<OB + 512>(wa2);
    rl[1] = rdtr<OB + 4096>(wa2); rh[1] = rdtr<OB + 4096 + 512>(wa2);
    bf16x8 x0 = of[buf][0], x1 = of[buf][1], x2 = of[buf][2], x3 = of[buf][3], x4 = of[buf][4], x5 = of[buf][5];
    WAITL4(15, wl[0][0], wh[0][0], wl[0][1], wh[0][1]);
    asm volatile("s_waitcnt lgkmcnt(15)" ::: "memory");
    WAITL4(12, wl[0][0], wh[0][0], wl[0][1], wh[0][1]);
    acc[0] = MFMA32(cat8(wl[0][0], wh[0][0]), x0, acc[0]);
    acc[1] = MFMA32(cat8(wl[0][1], wh[0][1]), x0, acc[1]);
    WAITL4(8, wl[1][0], wh[1][0], wl[1][1], wh[1][1]);
    acc[0] = MFMA32(cat8(wl[1][0], wh[1][0]), x1, acc[0]);
    acc[1] = MFMA32(cat8(wl[1][1], wh[1][1]), x1, acc[1]);
    WAITL4(4, wl[2][0], wh[2][0], wl[2][1], wh[2][1]);
    acc[0] = MFMA32(cat8(wl[2][0], wh[2][0]), x2, acc[0]);
    acc[1] = MFMA32(cat8(wl[2][1], wh[2][1]), x2, acc[1]);
    WAITL8(0, wl[3][0], wh[3][0], wl[3][1], wh[3][1], rl[0], rh[0], rl[1], rh[1]);
    acc[0] = MFMA32(cat8(wl[3][0], wh[3][0]), x3, acc[0]);
    acc[1] = MFMA32(cat8(wl[3][1], wh[3][1]), x3, acc[1]);
    acc2 = MFMA16(cat8(rl[0], rh[0]), x4, acc2);
    acc2 = MFMA16(cat8(rl[1], rh[1]), x5, acc2);
    SBAR();
    if (t + 3 < NT) stage(IC<(buf + 3) & 3>{}, t + 3);
    SBAR();
  };
  for (int t = 0; t < NT; t += 4) {
    body(IC<0>{}, t);
    body(IC<1>{}, t + 1);
    body(IC<2>{}, t + 2);
    body(IC<3>{}, t + 3);
  }
  {
    short* orow = out + (size_t)(b * SQ + 32 * w + l31) * DM + nt0 * 64 + 4 * hh;
#pragma unroll
    for (int nt = 0; nt < 2; ++nt)
#pragma unroll
      for (int i = 0; i < 4; ++i) {
        u32x2 pk = {cvtpk(acc[nt][4 * i], acc[nt][4 * i + 1]), cvtpk(acc[nt][4 * i + 2], acc[nt][4 * i + 3])};
        *(u32x2*)(orow + 32 * nt + 8 * i) = pk;
      }
    u32x2 pk = {cvtpk(acc2[0], acc2[1]), cvtpk(acc2[2], acc2[3])};
    *(u32x2*)(out + (size_t)(b * SQ + 128 + l15) * DM + nt0 * 64 + 16 * w + 4 * g) = pk;
  }
}

extern "C" void kernel_launch(void* const* d_in, const int* in_sizes, int n_in, void* d_out,
                              int out_size, void* d_ws, size_t ws_size, hipStream_t stream) {
  const short* x = (const short*)d_in[0];
  const short* kc = (const short*)d_in[1];
  const short* vc = (const short*)d_in[2];
  const short* wq = (const short*)d_in[3];
  const short* wo = (const short*)d_in[4];
  short* og = (short*)d_ws;
  short* out = (short*)d_out;
  hipFuncSetAttribute((const void*)attn_fused_kernel, hipFuncAttributeMaxDynamicSharedMemorySize, A_LDS);
  hipFuncSetAttribute((const void*)oproj_kernel, hipFuncAttributeMaxDynamicSharedMemorySize, B_LDS);
  attn_fused_kernel<<<NB * NH, 256, A_LDS, stream>>>(x, kc, vc, wq, og);
  oproj_kernel<<<256, 256, B_LDS, stream>>>(og, wo, out);
}
```

```cpp
#include <hip/hip_runtime.h>
#include <hip/hip_bf16.h>

typedef __attribute__((ext_vector_type(8))) short bf16x8;
typedef __attribute__((ext_vector_type(4))) short s16x4;
typedef __attribute__((ext_vector_type(16))) float f32x16;
typedef __attribute__((ext_vector_type(4))) float f32x4;
typedef __attribute__((ext_vector_type(4))) unsigned u32x4;
typedef __attribute__((ext_vector_type(2))) unsigned u32x2;

constexpr int NB = 32, SQ = 144, DM = 512, NH = 32, SK = 512, DH = 128;
constexpr int HD = NH * DH;
constexpr int MROWS = NB * SQ;

#define MFMA32(a, b, c) __builtin_amdgcn_mfma_f32_32x32x16_bf16((a), (b), (c), 0, 0, 0)
#define MFMA16(a, b, c) __builtin_amdgcn_mfma_f32_16x16x32_bf16((a), (b), (c), 0, 0, 0)
#define MFMA16K16(a, b, c) __builtin_amdgcn_mfma_f32_16x16x16bf16_1k((a), (b), (c), 0, 0, 0)

__device__ __forceinline__ unsigned cvtpk(float lo, float hi) {
  unsigned r;
  asm volatile("v_cvt_pk_bf16_f32 %0, %1, %2" : "=v"(r) : "v"(lo), "v"(hi));
  return r;
}
__device__ __forceinline__ bf16x8 cat8(s16x4 lo, s16x4 hi) {
  return __builtin_shufflevector(lo, hi, 0, 1, 2, 3, 4, 5, 6, 7);
}
__device__ __forceinline__ void glds16(const void* g, char* l) {
  __builtin_amdgcn_global_load_lds((const unsigned*)g, (unsigned*)l, 16, 0, 0);
}
__device__ __forceinline__ float xmax32(float v) {
  auto rr = __builtin_amdgcn_permlane32_swap(__float_as_uint(v), __float_as_uint(v), false, false);
  return fmaxf(__uint_as_float(rr[0]), __uint_as_float(rr[1]));
}
__device__ __forceinline__ float xsum32(float v) {
  auto rr = __builtin_amdgcn_permlane32_swap(__float_as_uint(v), __float_as_uint(v), false, false);
  return __uint_as_float(rr[0]) + __uint_as_float(rr[1]);
}
__device__ __forceinline__ float xmax16(float v) {
  auto rr = __builtin_amdgcn_permlane16_swap(__float_as_uint(v), __float_as_uint(v), false, false);
  return fmaxf(__uint_as_float(rr[0]), __uint_as_float(rr[1]));
}
__device__ __forceinline__ float xsum16(float v) {
  auto rr = __builtin_amdgcn_permlane16_swap(__float_as_uint(v), __float_as_uint(v), false, false);
  return __uint_as_float(rr[0]) + __uint_as_float(rr[1]);
}
template <int OFF> __device__ __forceinline__ bf16x8 rd128(int a) {
  bf16x8 r;
  asm volatile("ds_read_b128 %0, %1 offset:%2" : "=v"(r) : "v"(a), "i"(OFF) : "memory");
  return r;
}
template <int OFF> __device__ __forceinline__ s16x4 rdtr(int a) {
  s16x4 r;
  asm volatile("ds_read_b64_tr_b16 %0, %1 offset:%2" : "=v"(r) : "v"(a), "i"(OFF) : "memory");
  return r;
}
#define WAITL2(N, a, b) asm volatile("s_waitcnt lgkmcnt(" #N ")" : "+v"(a), "+v"(b) :: "memory")
#define WAITL4(N, a, b, c, d) asm volatile("s_waitcnt lgkmcnt(" #N ")" : "+v"(a), "+v"(b), "+v"(c), "+v"(d) :: "memory")
#define WAITL8(N, a, b, c, d, e, f, g_, h_) asm volatile("s_waitcnt lgkmcnt(" #N ")" : "+v"(a), "+v"(b), "+v"(c), "+v"(d), "+v"(e), "+v"(f), "+v"(g_), "+v"(h_) :: "memory")
#define RAW_BAR() __builtin_amdgcn_s_barrier()
#define SBAR() __builtin_amdgcn_sched_barrier(0)
#define OPAQUE(v) asm volatile("" : "+v"(v))
template <int N> struct IC { static constexpr int value = N; };
template <int I, int N, class F> __device__ __forceinline__ void sfor(F&& f) {
  if constexpr (I < N) { f(IC<I>{}); sfor<I + 1, N>(f); }
}

constexpr int G1_BUFX = 288 * 128;
constexpr int G1_BUFW = 64 * 512;
constexpr int G1_STRIDE = G1_BUFX + G1_BUFW;
constexpr int G1_LDS = 2 * G1_STRIDE;

__global__ __launch_bounds__(512, 2) void qproj_kernel(const short* __restrict__ x, const short* __restrict__ wq,
                                                        short* __restrict__ qg) {
  extern __shared__ __attribute__((aligned(16))) char smem[];
  const int tid = threadIdx.x, lane = tid & 63;
  const int w = __builtin_amdgcn_readfirstlane(tid >> 6);
  const int xg = blockIdx.x & 7, idx = blockIdx.x >> 3;
  const int mt = xg * 2 + (idx >> 4), nt = idx & 15;
  const int l31 = lane & 31, hh = lane >> 5, l15 = lane & 15;
  const int qp = l15 >> 2, p4 = lane & 3, blk = (lane >> 4) & 1;
  const char* xb = (const char*)(x + (size_t)mt * 288 * DM);
  const char* wb = (const char*)(wq + nt * 256);
  unsigned xsrc[5];
#pragma unroll
  for (int j = 0; j < 5; ++j) {
    int r = 8 * (w + 8 * j) + (lane >> 3), cp = lane & 7;
    if (r > 287) r = 287;
    xsrc[j] = r * 1024 + ((cp ^ ((r >> 1) & 7)) << 4);
  }
  unsigned wsrc[4];
#pragma unroll
  for (int j = 0; j < 4; ++j) {
    int r = 2 * (4 * w + j) + (lane >> 5), cp = lane & 31;
    wsrc[j] = r * (HD * 2) + ((cp ^ ((r & 3) << 2)) << 4);
  }
  auto stage = [&](int t, int buf) {
    char* lx = smem + buf * G1_STRIDE;
    char* lw = lx + G1_BUFX;
    const char* xt = xb + t * 128;
    const char* wt = wb + (size_t)t * (64 * HD * 2);
#pragma unroll
    for (int j = 0; j < 4; ++j) glds16(xt + xsrc[j], lx + (w + 8 * j) * 1024);
    if (w < 4) glds16(xt + xsrc[4], lx + (w + 32) * 1024);
#pragma unroll
    for (int j = 0; j < 4; ++j) glds16(wt + wsrc[j], lw + (4 * w + j) * 1024);
  };
  f32x16 acc[9];
#pragma unroll
  for (int i = 0; i < 9; ++i) acc[i] = f32x16{};
  const int sb = (int)(uintptr_t)smem;
  const int xa = sb + l31 * 128 + ((hh ^ ((l31 >> 1) & 7)) << 4);
  const int wa = sb + G1_BUFX + (8 * hh + qp) * 512 + (((4 * w + 2 * blk + (p4 >> 1)) ^ (qp << 2)) << 4) + 8 * (p4 & 1);
  stage(0, 0);
  auto body = [&](auto BC, int t) {
    constexpr int buf = decltype(BC)::value;
    asm volatile("s_waitcnt vmcnt(0) lgkmcnt(0)" ::: "memory");
    RAW_BAR();
    if (t < 7) stage(t + 1, buf ^ 1);
    SBAR();
    const int xa_ = xa + buf * G1_STRIDE, wa_ = wa + buf * G1_STRIDE;
    sfor<0, 4>([&](auto SC) {
      constexpr int s = decltype(SC)::value;
      const int xs = xa_ ^ (s << 5);
      s16x4 wl = rdtr<s * 8192>(wa_), wh = rdtr<s * 8192 + 2048>(wa_);
      bf16x8 xf[9];
      sfor<0, 9>([&](auto MC) { constexpr int mi = decltype(MC)::value; xf[mi] = rd128<mi * 4096>(xs); });
      asm volatile("s_waitcnt lgkmcnt(6)" : "+v"(wl), "+v"(wh), "+v"(xf[0]), "+v"(xf[1]), "+v"(xf[2]) :: "memory");
      bf16x8 wf = cat8(wl, wh);
      acc[0] = MFMA32(wf, xf[0], acc[0]);
      acc[1] = MFMA32(wf, xf[1], acc[1]);
      acc[2] = MFMA32(wf, xf[2], acc[2]);
      asm volatile("s_waitcnt lgkmcnt(3)" : "+v"(xf[3]), "+v"(xf[4]), "+v"(xf[5]) :: "memory");
      acc[3] = MFMA32(wf, xf[3], acc[3]);
      acc[4] = MFMA32(wf, xf[4], acc[4]);
      acc[5] = MFMA32(wf, xf[5], acc[5]);
      asm volatile("s_waitcnt lgkmcnt(0)" : "+v"(xf[6]), "+v"(xf[7]), "+v"(xf[8]) :: "memory");
      acc[6] = MFMA32(wf, xf[6], acc[6]);
      acc[7] = MFMA32(wf, xf[7], acc[7]);
      acc[8] = MFMA32(wf, xf[8], acc[8]);
    });
  };
  for (int t = 0; t < 8; t += 2) {
    body(IC<0>{}, t);
    body(IC<1>{}, t + 1);
  }
  short* qrow = qg + (size_t)(mt * 288 + l31) * HD + nt * 256 + 32 * w + 4 * hh;
#pragma unroll
  for (int mi = 0; mi < 9; ++mi)
#pragma unroll
    for (int i = 0; i < 4; ++i) {
      u32x2 pk = {cvtpk(acc[mi][4 * i], acc[mi][4 * i + 1]), cvtpk(acc[mi][4 * i + 2], acc[mi][4 * i + 3])};
      *(u32x2*)(qrow + (size_t)mi * 32 * HD + 8 * i) = pk;
    }
}

constexpr int A_BUFA = 16384;
constexpr int A_BUFB = 16384;
constexpr int A_OFF_A = 0;
constexpr int A_OFF_B = 2 * A_BUFA;
constexpr int A_OFF_Q2 = A_OFF_B + 2 * A_BUFB;
constexpr int A_LDS = A_OFF_Q2 + 4096;

__global__ __launch_bounds__(256, 2) void attn_kernel(
    const short* __restrict__ qg, const short* __restrict__ kc, const short* __restrict__ vc,
    short* __restrict__ og) {
  extern __shared__ __attribute__((aligned(16))) char smem[];
  const int tid = threadIdx.x, lane = tid & 63;
  const int w = __builtin_amdgcn_readfirstlane(tid >> 6);
  const int pidx = (blockIdx.x & 7) * 128 + (blockIdx.x >> 3);
  const int b = pidx >> 5, h = pidx & 31;
  const char* kb = (const char*)(kc + (size_t)pidx * SK * DH);
  const char* vb = (const char*)(vc + (size_t)pidx * SK * DH);

  const int l31 = lane & 31, hh = lane >> 5, l15 = lane & 15, g = lane >> 4;
  const int qp = l15 >> 2, p4 = lane & 3, blk = (lane >> 4) & 1;
  const int sb = (int)(uintptr_t)smem;

  const unsigned ksrc = (16 * w + g) * 256 + ((l15 ^ g) << 4);
  const unsigned vsrc = (16 * w + g) * 256 + ((l15 ^ (g << 2)) << 4);
  auto stage_kv = [&](int t, int buf, unsigned ko, unsigned vo) {
#pragma unroll
    for (int i = 0; i < 4; ++i)
      glds16(kb + t * 16384 + ((ko ^ (i << 6)) + i * 1024), smem + A_OFF_A + buf * A_BUFA + w * 4096 + i * 1024);
#pragma unroll
    for (int i = 0; i < 4; ++i)
      glds16(vb + t * 16384 + (vo + i * 1024), smem + A_OFF_B + buf * A_BUFB + w * 4096 + i * 1024);
  };
  stage_kv(0, 0, ksrc, vsrc);
  bf16x8 qf[8];
  {
    const short* qrow = qg + (size_t)(b * SQ + 32 * w + l31) * HD + h * DH + 8 * hh;
#pragma unroll
    for (int ks = 0; ks < 8; ++ks) qf[ks] = *(const bf16x8*)(qrow + 16 * ks);
    const int q = tid >> 4, c = tid & 15;
    bf16x8 v = *(const bf16x8*)(qg + (size_t)(b * SQ + 128 + q) * HD + h * DH + 8 * c);
    *(bf16x8*)(smem + A_OFF_Q2 + q * 256 + ((c ^ q) << 4)) = v;
  }
  f32x16 acc[4];
#pragma unroll
  for (int i = 0; i < 4; ++i) acc[i] = f32x16{};
  f32x4 acc2[8];
#pragma unroll
  for (int i = 0; i < 8; ++i) acc2[i] = f32x4{};

  constexpr float CS = 0.088388347648318440f * 1.4426950408889634f;
  float m1 = -1e30f, l1 = 0.f, m2 = -1e30f, l2 = 0.f;
  {
    int ka = sb + A_OFF_A + l31 * 256 + ((hh ^ l15) << 4);
    int ka2 = sb + A_OFF_A + (16 * w + l15) * 256 + ((g ^ l15) << 4);
    int q2a = sb + A_OFF_Q2 + l15 * 256 + ((g ^ l15) << 4);
    int va = sb + A_OFF_B + (4 * hh + qp) * 256 + (((2 * blk + (p4 >> 1)) ^ (qp << 2)) << 4) + 8 * (p4 & 1);
    int va2 = sb + A_OFF_B + (16 * w + 4 * g + qp) * 256 + (((p4 >> 1) ^ (qp << 2)) << 4) + 8 * (p4 & 1);
    auto body = [&](auto BC, int t) {
      constexpr int buf = decltype(BC)::value;
      constexpr int OA = buf * A_BUFA, OB = buf * A_BUFB;
      asm volatile("s_waitcnt vmcnt(0) lgkmcnt(0)" ::: "memory");
      RAW_BAR();
      int ka_ = ka, ka2_ = ka2, q2a_ = q2a, va_ = va, va2_ = va2;
      unsigned ko = ksrc, vo = vsrc;
      OPAQUE(ka_); OPAQUE(ka2_); OPAQUE(q2a_); OPAQUE(va_); OPAQUE(va2_); OPAQUE(ko); OPAQUE(vo);
      if (t < 7) stage_kv(t + 1, buf ^ 1, ko, vo);
      SBAR();
      f32x16 s0 = f32x16{}, s1 = f32x16{};
      f32x4 sr = f32x4{};
      bf16x8 kA[4], kB[4];
#pragma unroll
      for (int i = 0; i < 4; ++i) kA[i] = rd128<OA>(ka_ ^ (i << 5));
#pragma unroll
      for (int i = 0; i < 4; ++i) kB[i] = rd128<OA>(ka_ ^ ((4 + i) << 5));
      WAITL4(4, kA[0], kA[1], kA[2], kA[3]);
      s0 = MFMA32(kA[0], qf[0], s0);
      s0 = MFMA32(kA[1], qf[1], s0);
      SBAR();
      s0 = MFMA32(kA[2], qf[2], s0);
      s0 = MFMA32(kA[3], qf[3], s0);
      SBAR();
#pragma unroll
      for (int i = 0; i < 4; ++i) kA[i] = rd128<OA + 8192>(ka_ ^ (i << 5));
      WAITL4(4, kB[0], kB[1], kB[2], kB[3]);
      s0 = MFMA32(kB[0], qf[4], s0);
      s0 = MFMA32(kB[1], qf[5], s0);
      SBAR();
      s0 = MFMA32(kB[2], qf[6], s0);
      s0 = MFMA32(kB[3], qf[7], s0);
      SBAR();
#pragma unroll
      for (int i = 0; i < 4; ++i) kB[i] = rd128<OA + 8192>(ka_ ^ ((4 + i) << 5));
      WAITL4(4, kA[0], kA[1], kA[2], kA[3]);
      s1 = MFMA32(kA[0], qf[0], s1);
      SBAR();
      s1 = MFMA32(kA[1], qf[1], s1);
      float tmax = fmaxf(fmaxf(s0[0], s0[1]), s0[2]);
#pragma unroll
      for (int r = 3; r < 9; r += 2) tmax = fmaxf(fmaxf(tmax, s0[r]), s0[r + 1]);
      SBAR();
      s1 = MFMA32(kA[2], qf[2], s1);
#pragma unroll
      for (int r = 9; r < 15; r += 2) tmax = fmaxf(fmaxf(tmax, s0[r]), s0[r + 1]);
      tmax = fmaxf(tmax, s0[15]);
      tmax = xmax32(tmax);
      float mn = fmaxf(m1, tmax);
      float alpha = __builtin_amdgcn_exp2f((m1 - mn) * CS);
      m1 = mn;
      SBAR();
      s1 = MFMA32(kA[3], qf[3], s1);
      SBAR();
      if (!__all(alpha == 1.f)) {
#pragma unroll
        for (int dt = 0; dt < 4; ++dt)
#pragma unroll
          for (int r = 0; r < 16; ++r) acc[dt][r] *= alpha;
      }
      bf16x8 kr[4], qr[4];
#pragma unroll
      for (int ks = 0; ks < 4; ++ks) { kr[ks] = rd128<OA>(ka2_ ^ (ks << 6)); qr[ks] = rd128<0>(q2a_ ^ (ks << 6)); }
      WAITL4(8, kB[0], kB[1], kB[2], kB[3]);
      bf16x8 pf[4];
      float mc = -mn * CS;
      float ps = 0.f;
      float e[8];
      s1 = MFMA32(kB[0], qf[4], s1);
#pragma unroll
      for (int j = 0; j < 4; ++j) { e[j] = __builtin_amdgcn_exp2f(fmaf(s0[j], CS, mc)); ps += e[j]; }
      SBAR();
      s1 = MFMA32(kB[1], qf[5], s1);
#pragma unroll
      for (int j = 4; j < 8; ++j) { e[j] = __builtin_amdgcn_exp2f(fmaf(s0[j], CS, mc)); ps += e[j]; }
      { u32x4 a = {cvtpk(e[0], e[1]), cvtpk(e[2], e[3]), cvtpk(e[4], e[5]), cvtpk(e[6], e[7])}; pf[0] = __builtin_bit_cast(bf16x8, a); }
      SBAR();
      s1 = MFMA32(kB[2], qf[6], s1);
#pragma unroll
      for (int j = 0; j < 4; ++j) { e[j] = __builtin_amdgcn_exp2f(fmaf(s0[8 + j], CS, mc)); ps += e[j]; }
      SBAR();
      s1 = MFMA32(kB[3], qf[7], s1);
#pragma unroll
      for (int j = 4; j < 8; ++j) { e[j] = __builtin_amdgcn_exp2f(fmaf(s0[8 + j], CS, mc)); ps += e[j]; }
      { u32x4 a = {cvtpk(e[0], e[1]), cvtpk(e[2], e[3]), cvtpk(e[4], e[5]), cvtpk(e[6], e[7])}; pf[1] = __builtin_bit_cast(bf16x8, a); }
      l1 = l1 * alpha + ps;
      OPAQUE(l1);
      SBAR();
      SBAR();
      const int v0 = va_, v1 = va_ ^ 64, v2 = va_ ^ 128, v3 = va_ ^ 192;
      s16x4 vl[2][4], vh[2][4];
      auto issue_v = [&](auto SC) {
        constexpr int s = decltype(SC)::value, u = s & 1;
        vl[u][0] = rdtr<OB + s * 4096>(v0); vh[u][0] = rdtr<OB + s * 4096 + 2048>(v0);
        vl[u][1] = rdtr<OB + s * 4096>(v1); vh[u][1] = rdtr<OB + s * 4096 + 2048>(v1);
        vl[u][2] = rdtr<OB + s * 4096>(v2); vh[u][2] = rdtr<OB + s * 4096 + 2048>(v2);
        vl[u][3] = rdtr<OB + s * 4096>(v3); vh[u][3] = rdtr<OB + s * 4096 + 2048>(v3);
      };
      issue_v(IC<0>{});
      WAITL8(8, kr[0], kr[1], kr[2], kr[3], qr[0], qr[1], qr[2], qr[3]);
#pragma unroll
      for (int ks = 0; ks < 4; ++ks) sr = MFMA16(kr[ks], qr[ks], sr);
      SBAR();
      SBAR();
      issue_v(IC<1>{});
      WAITL8(8, vl[0][0], vh[0][0], vl[0][1], vh[0][1], vl[0][2], vh[0][2], vl[0][3], vh[0][3]);
      acc[0] = MFMA32(cat8(vl[0][0], vh[0][0]), pf[0], acc[0]);
      float tm1 = fmaxf(fmaxf(s1[0], s1[1]), s1[2]);
#pragma unroll
      for (int r = 3; r < 9; r += 2) tm1 = fmaxf(fmaxf(tm1, s1[r]), s1[r + 1]);
      SBAR();
      acc[1] = MFMA32(cat8(vl[0][1], vh[0][1]), pf[0], acc[1]);
#pragma unroll
      for (int r = 9; r < 15; r += 2) tm1 = fmaxf(fmaxf(tm1, s1[r]), s1[r + 1]);
      tm1 = fmaxf(tm1, s1[15]);
      tm1 = xmax32(tm1);
      float mnb = fmaxf(m1, tm1);
      float alphab = __builtin_amdgcn_exp2f((m1 - mnb) * CS);
      m1 = mnb;
      mc = -mnb * CS;
      ps = 0.f;
      SBAR();
      acc[2] = MFMA32(cat8(vl[0][2], vh[0][2]), pf[0], acc[2]);
#pragma unroll
      for (int j = 0; j < 4; ++j) { e[j] = __builtin_amdgcn_exp2f(fmaf(s1[j], CS, mc)); ps += e[j]; }
      SBAR();
      acc[3] = MFMA32(cat8(vl[0][3], vh[0][3]), pf[0], acc[3]);
#pragma unroll
      for (int j = 4; j < 8; ++j) { e[j] = __builtin_amdgcn_exp2f(fmaf(s1[j], CS, mc)); ps += e[j]; }
      { u32x4 a = {cvtpk(e[0], e[1]), cvtpk(e[2], e[3]), cvtpk(e[4], e[5]), cvtpk(e[6], e[7])}; pf[2] = __builtin_bit_cast(bf16x8, a); }
      SBAR();
      issue_v(IC<2>{});
      WAITL8(8, vl[1][0], vh[1][0], vl[1][1], vh[1][1], vl[1][2], vh[1][2], vl[1][3], vh[1][3]);
      acc[0] = MFMA32(cat8(vl[1][0], vh[1][0]), pf[1], acc[0]);
#pragma unroll
      for (int j = 0; j < 4; ++j) { e[j] = __builtin_amdgcn_exp2f(fmaf(s1[8 + j], CS, mc)); ps += e[j]; }
      SBAR();
      acc[1] = MFMA32(cat8(vl[1][1], vh[1][1]), pf[1], acc[1]);
#pragma unroll
      for (int j = 4; j < 8; ++j) { e[j] = __builtin_amdgcn_exp2f(fmaf(s1[8 + j], CS, mc)); ps += e[j]; }
      { u32x4 a = {cvtpk(e[0], e[1]), cvtpk(e[2], e[3]), cvtpk(e[4], e[5]), cvtpk(e[6], e[7])}; pf[3] = __builtin_bit_cast(bf16x8, a); }
      l1 = l1 * alphab + ps;
      OPAQUE(l1);
      SBAR();
      acc[2] = MFMA32(cat8(vl[1][2], vh[1][2]), pf[1], acc[2]);
      float t2 = fmaxf(fmaxf(sr[0], sr[1]), fmaxf(sr[2], sr[3]));
      t2 = xmax16(t2);
      t2 = xmax32(t2);
      float mn2 = fmaxf(m2, t2);
      float alpha2 = __builtin_amdgcn_exp2f((m2 - mn2) * CS);
      m2 = mn2;
      SBAR();
      acc[3] = MFMA32(cat8(vl[1][3], vh[1][3]), pf[1], acc[3]);
      const float mc2 = -mn2 * CS;
      float e0 = __builtin_amdgcn_exp2f(fmaf(sr[0], CS, mc2));
      float e1 = __builtin_amdgcn_exp2f(fmaf(sr[1], CS, mc2));
      float e2 = __builtin_amdgcn_exp2f(fmaf(sr[2], CS, mc2));
      float e3 = __builtin_amdgcn_exp2f(fmaf(sr[3], CS, mc2));
      l2 = l2 * alpha2 + (e0 + e1) + (e2 + e3);
      OPAQUE(l2);
      u32x2 p2u = {cvtpk(e0, e1), cvtpk(e2, e3)};
      s16x4 p2 = __builtin_bit_cast(s16x4, p2u);
      SBAR();
      if (!__all(alphab == 1.f && alpha2 == 1.f)) {
#pragma unroll
        for (int dt = 0; dt < 4; ++dt)
#pragma unroll
          for (int r = 0; r < 16; ++r) acc[dt][r] *= alphab;
#pragma unroll
        for (int dd = 0; dd < 8; ++dd)
#pragma unroll
          for (int r = 0; r < 4; ++r) acc2[dd][r] *= alpha2;
      }
      issue_v(IC<3>{});
      WAITL8(8, vl[0][0], vh[0][0], vl[0][1], vh[0][1], vl[0][2], vh[0][2], vl[0][3], vh[0][3]);
#pragma unroll
      for (int dt = 0; dt < 4; ++dt) acc[dt] = MFMA32(cat8(vl[0][dt], vh[0][dt]), pf[2], acc[dt]);
      SBAR();
      s16x4 vr[8];
#pragma unroll
      for (int dd = 0; dd < 8; ++dd) vr[dd] = rdtr<OB>(va2_ ^ (dd << 5));
      WAITL8(8, vl[1][0], vh[1][0], vl[1][1], vh[1][1], vl[1][2], vh[1][2], vl[1][3], vh[1][3]);
#pragma unroll
      for (int dt = 0; dt < 4; ++dt) acc[dt] = MFMA32(cat8(vl[1][dt], vh[1][dt]), pf[3], acc[dt]);
      WAITL8(0, vr[0], vr[1], vr[2], vr[3], vr[4], vr[5], vr[6], vr[7]);
#pragma unroll
      for (int dd = 0; dd < 8; ++dd) acc2[dd] = MFMA16K16(vr[dd], p2, acc2[dd]);
    };
    for (int t = 0; t < 8; t += 2) {
      body(IC<0>{}, t);
      body(IC<1>{}, t + 1);
    }
  }

  int tz = threadIdx.x;
  OPAQUE(tz);
  {
    const int l31 = tz & 31, hh = (tz >> 5) & 1;
    float lt = xsum32(l1);
    float inv = 1.f / lt;
    short* orow = og + (size_t)(b * SQ + 32 * w + l31) * HD + h * DH + 4 * hh;
#pragma unroll
    for (int dt = 0; dt < 4; ++dt)
#pragma unroll
      for (int i = 0; i < 4; ++i) {
        u32x2 pk = {cvtpk(acc[dt][4 * i] * inv, acc[dt][4 * i + 1] * inv),
                    cvtpk(acc[dt][4 * i + 2] * inv, acc[dt][4 * i + 3] * inv)};
        *(u32x2*)(orow + 32 * dt + 8 * i) = pk;
      }
  }
  __syncthreads();
  {
    const int l15 = tz & 15, g = (tz >> 4) & 3;
    float lt2 = xsum32(xsum16(l2));
    float* pw = (float*)(smem + w * 8192);
#pragma unroll
    for (int dd = 0; dd < 8; ++dd) *(f32x4*)(pw + l15 * 128 + 16 * dd + 4 * g) = acc2[dd];
    float* st = (float*)(smem + 32768);
    if (g == 0) { st[w * 32 + l15] = m2; st[w * 32 + 16 + l15] = lt2; }
  }
  __syncthreads();
  {
    const int q = tz >> 4, d0 = (tz & 15) * 8;
    const float* st = (const float*)(smem + 32768);
    float mm = fmaxf(fmaxf(st[q], st[32 + q]), fmaxf(st[64 + q], st[96 + q]));
    float o[8] = {0, 0, 0, 0, 0, 0, 0, 0};
    float ls = 0.f;
#pragma unroll
    for (int ww = 0; ww < 4; ++ww) {
      float sc = __builtin_amdgcn_exp2f((st[ww * 32 + q] - mm) * CS);
      ls += st[ww * 32 + 16 + q] * sc;
      const float* pw = (const float*)(smem + ww * 8192) + q * 128 + d0;
      f32x4 a = *(const f32x4*)pw, c = *(const f32x4*)(pw + 4);
#pragma unroll
      for (int j = 0; j < 4; ++j) { o[j] += a[j] * sc; o[4 + j] += c[j] * sc; }
    }
    float inv = 1.f / ls;
    u32x4 pk = {cvtpk(o[0] * inv, o[1] * inv), cvtpk(o[2] * inv, o[3] * inv),
                cvtpk(o[4] * inv, o[5] * inv), cvtpk(o[6] * inv, o[7] * inv)};
    *(u32x4*)(og + (size_t)(b * SQ + 128 + q) * HD + h * DH + d0) = pk;
  }
}

constexpr int B_NST = 4;
constexpr int B_BUFA = 20480;
constexpr int B_BUFB = 8192;
constexpr int B_STRIDE = B_BUFA + B_BUFB;
constexpr int B_LDS = B_NST * B_STRIDE;

__global__ __launch_bounds__(256, 1) void oproj_kernel(const short* __restrict__ og,
                                                        const short* __restrict__ wo,
                                                        short* __restrict__ out) {
  extern __shared__ __attribute__((aligned(16))) char smem[];
  const int tid = threadIdx.x, lane = tid & 63;
  const int w = __builtin_amdgcn_readfirstlane(tid >> 6);
  const int xg = blockIdx.x & 7, idx = blockIdx.x >> 3;
  const int b = xg * 4 + (idx >> 3), nt0 = idx & 7;
  const char* ob = (const char*)(og + (size_t)b * SQ * HD);
  const char* wob = (const char*)(wo + nt0 * 64);
  const int l31 = lane & 31, hh = lane >> 5, l15 = lane & 15, g = lane >> 4;
  const int qp = l15 >> 2, p4 = lane & 3, blk = (lane >> 4) & 1;
  unsigned osrc[5], wsrc[2];
#pragma unroll
  for (int ii = 0; ii < 5; ++ii) {
    int r = 8 * (w * 5 + ii) + (lane >> 3), cp = lane & 7;
    int rs = r < SQ ? r : SQ - 1;
    osrc[ii] = rs * (HD * 2) + ((cp ^ ((r >> 1) & 7)) << 4);
  }
#pragma unroll
  for (int ii = 0; ii < 2; ++ii) {
    int r = 8 * (w * 2 + ii) + (lane >> 3), cp = lane & 7;
    wsrc[ii] = r * (DM * 2) + ((cp ^ (((r >> 1) & 1) << 2)) << 4);
  }
  auto stage = [&](int t, int buf) {
    char* la = smem + buf * B_STRIDE + w * 5120;
    char* lb = smem + buf * B_STRIDE + B_BUFA + w * 2048;
    const char* ot = ob + t * 128;
    const char* wt = wob + (size_t)t * (64 * DM * 2);
#pragma unroll
    for (int ii = 0; ii < 5; ++ii) glds16(ot + osrc[ii], la + ii * 1024);
#pragma unroll
    for (int ii = 0; ii < 2; ++ii) glds16(wt + wsrc[ii], lb + ii * 1024);
  };

  f32x16 acc[2] = {f32x16{}, f32x16{}};
  f32x4 acc2 = f32x4{};
  constexpr int NT = HD / 64;
  const int sb = (int)(uintptr_t)smem;
  const int xa = sb + (32 * w + l31) * 128 + ((hh ^ ((l31 >> 1) & 7)) << 4);
  const int wa = sb + B_BUFA + (8 * hh + qp) * 128 + (((2 * blk + (p4 >> 1)) ^ ((qp >> 1) << 2)) << 4) + 8 * (p4 & 1);
  const int xa2 = sb + (128 + l15) * 128 + ((g ^ ((l15 >> 1) & 7)) << 4);
  const int wa2 = sb + B_BUFA + (8 * g + qp) * 128 + (((2 * w + (p4 >> 1)) ^ ((qp >> 1) << 2)) << 4) + 8 * (p4 & 1);
  stage(0, 0);
  stage(1, 1);
  stage(2, 2);
  auto body = [&](auto BC, int t) {
    constexpr int buf = decltype(BC)::value;
    constexpr int OB = (buf & 1) * B_STRIDE;
    const int hi2 = (buf >> 1) * 2 * B_STRIDE;
    if (t < NT - 2) asm volatile("s_waitcnt vmcnt(14)" ::: "memory");
    else if (t == NT - 2) asm volatile("s_waitcnt vmcnt(7)" ::: "memory");
    else asm volatile("s_waitcnt vmcnt(0)" ::: "memory");
    RAW_BAR();
    if (t + 3 < NT) stage(t + 3, (buf + 3) & 3);
    const int xa_ = xa + hi2, w0 = wa + hi2, w1 = (wa ^ 64) + hi2, xa2_ = xa2 + hi2, wa2_ = wa2 + hi2;
    bf16x8 xf[4], xr[2];
    s16x4 wl[4][2], wh[4][2], rl[2], rh[2];
    sfor<0, 4>([&](auto SC) {
      constexpr int s = decltype(SC)::value;
      xf[s] = rd128<OB>(xa_ ^ (s << 5));
      wl[s][0] = rdtr<OB + s * 2048>(w0); wh[s][0] = rdtr<OB + s * 2048 + 512>(w0);
      wl[s][1] = rdtr<OB + s * 2048>(w1); wh[s][1] = rdtr<OB + s * 2048 + 512>(w1);
      if constexpr (s >= 1) {
        asm volatile("s_waitcnt lgkmcnt(5)" : "+v"(xf[s - 1]), "+v"(wl[s - 1][0]), "+v"(wh[s - 1][0]), "+v"(wl[s - 1][1]), "+v"(wh[s - 1][1]) :: "memory");
        acc[0] = MFMA32(cat8(wl[s - 1][0], wh[s - 1][0]), xf[s - 1], acc[0]);
        acc[1] = MFMA32(cat8(wl[s - 1][1], wh[s - 1][1]), xf[s - 1], acc[1]);
      }
    });
    xr[0] = rd128<OB>(xa2_); rl[0] = rdtr<OB>(wa2_); rh[0] = rdtr<OB + 512>(wa2_);
    xr[1] = rd128<OB>(xa2_ ^ 64); rl[1] = rdtr<OB + 4096>(wa2_); rh[1] = rdtr<OB + 4096 + 512>(wa2_);
    asm volatile("s_waitcnt lgkmcnt(6)" : "+v"(xf[3]), "+v"(wl[3][0]), "+v"(wh[3][0]), "+v"(wl[3][1]), "+v"(wh[3][1]) :: "memory");
    acc[0] = MFMA32(cat8(wl[3][0], wh[3][0]), xf[3], acc[0]);
    acc[1] = MFMA32(cat8(wl[3][1], wh[3][1]), xf[3], acc[1]);
    asm volatile("s_waitcnt lgkmcnt(0)" : "+v"(xr[0]), "+v"(rl[0]), "+v"(rh[0]), "+v"(xr[1]), "+v"(rl[1]), "+v"(rh[1]) :: "memory");
    acc2 = MFMA16(cat8(rl[0], rh[0]), xr[0], acc2);
    acc2 = MFMA16(cat8(rl[1], rh[1]), xr[1], acc2);
  };
  for (int t = 0; t < NT; t += 4) {
    body(IC<0>{}, t);
    body(IC<1>{}, t + 1);
    body(IC<2>{}, t + 2);
    body(IC<3>{}, t + 3);
  }
  {
    short* orow = out + (size_t)(b * SQ + 32 * w + l31) * DM + nt0 * 64 + 4 * hh;
#pragma unroll
    for (int nt = 0; nt < 2; ++nt)
#pragma unroll
      for (int i = 0; i < 4; ++i) {
        u32x2 pk = {cvtpk(acc[nt][4 * i], acc[nt][4 * i + 1]), cvtpk(acc[nt][4 * i + 2], acc[nt][4 * i + 3])};
        *(u32x2*)(orow + 32 * nt + 8 * i) = pk;
      }
    u32x2 pk = {cvtpk(acc2[0], acc2[1]), cvtpk(acc2[2], acc2[3])};
    *(u32x2*)(out + (size_t)(b * SQ + 128 + l15) * DM + nt0 * 64 + 16 * w + 4 * g) = pk;
  }
}

extern "C" void kernel_launch(void* const* d_in, const int* in_sizes, int n_in, void* d_out,
                              int out_size, void* d_ws, size_t ws_size, hipStream_t stream) {
  const short* x = (const short*)d_in[0];
  const short* kc = (const short*)d_in[1];
  const short* vc = (const short*)d_in[2];
  const short* wq = (const short*)d_in[3];
  const short* wo = (const short*)d_in[4];
  short* qg = (short*)d_ws;
  short* og = qg + (size_t)MROWS * HD;
  short* out = (short*)d_out;
  (void)hipFuncSetAttribute((const void*)qproj_kernel, hipFuncAttributeMaxDynamicSharedMemorySize, G1_LDS);
  (void)hipFuncSetAttribute((const void*)attn_kernel, hipFuncAttributeMaxDynamicSharedMemorySize, A_LDS);
  (void)hipFuncSetAttribute((const void*)oproj_kernel, hipFuncAttributeMaxDynamicSharedMemorySize, B_LDS);
  qproj_kernel<<<256, 512, G1_LDS, stream>>>(x, wq, qg);
  attn_kernel<<<NB * NH, 256, A_LDS, stream>>>(qg, kc, vc, og);
  oproj_kernel<<<256, 256, B_LDS, stream>>>(og, wo, out);
}
```

```cpp
#include <hip/hip_runtime.h>
#include <hip/hip_bf16.h>

typedef __attribute__((ext_vector_type(8))) short bf16x8;
typedef __attribute__((ext_vector_type(4))) short s16x4;
typedef __attribute__((ext_vector_type(16))) float f32x16;
typedef __attribute__((ext_vector_type(4))) float f32x4;
typedef __attribute__((ext_vector_type(4))) unsigned u32x4;
typedef __attribute__((ext_vector_type(2))) unsigned u32x2;

constexpr int NB = 32, SQ = 144, DM = 512, NH = 32, SK = 512, DH = 128;
constexpr int HD = NH * DH;
constexpr int MROWS = NB * SQ;

#define MFMA32(a, b, c) __builtin_amdgcn_mfma_f32_32x32x16_bf16((a), (b), (c), 0, 0, 0)
#define MFMA16(a, b, c) __builtin_amdgcn_mfma_f32_16x16x32_bf16((a), (b), (c), 0, 0, 0)
#define MFMA16K16(a, b, c) __builtin_amdgcn_mfma_f32_16x16x16bf16_1k((a), (b), (c), 0, 0, 0)

__device__ __forceinline__ unsigned cvtpk(float lo, float hi) {
  unsigned r;
  asm volatile("v_cvt_pk_bf16_f32 %0, %1, %2" : "=v"(r) : "v"(lo), "v"(hi));
  return r;
}
__device__ __forceinline__ bf16x8 cat8(s16x4 lo, s16x4 hi) {
  return __builtin_shufflevector(lo, hi, 0, 1, 2, 3, 4, 5, 6, 7);
}
__device__ __forceinline__ void glds16(const void* g, char* l) {
  __builtin_amdgcn_global_load_lds((const unsigned*)g, (unsigned*)l, 16, 0, 0);
}
__device__ __forceinline__ float xmax32(float v) {
  auto rr = __builtin_amdgcn_permlane32_swap(__float_as_uint(v), __float_as_uint(v), false, false);
  return fmaxf(__uint_as_float(rr[0]), __uint_as_float(rr[1]));
}
__device__ __forceinline__ float xsum32(float v) {
  auto rr = __builtin_amdgcn_permlane32_swap(__float_as_uint(v), __float_as_uint(v), false, false);
  return __uint_as_float(rr[0]) + __uint_as_float(rr[1]);
}
__device__ __forceinline__ float xmax16(float v) {
  auto rr = __builtin_amdgcn_permlane16_swap(__float_as_uint(v), __float_as_uint(v), false, false);
  return fmaxf(__uint_as_float(rr[0]), __uint_as_float(rr[1]));
}
__device__ __forceinline__ float xsum16(float v) {
  auto rr = __builtin_amdgcn_permlane16_swap(__float_as_uint(v), __float_as_uint(v), false, false);
  return __uint_as_float(rr[0]) + __uint_as_float(rr[1]);
}
template <int OFF> __device__ __forceinline__ bf16x8 rd128(int a) {
  bf16x8 r;
  asm volatile("ds_read_b128 %0, %1 offset:%2" : "=v"(r) : "v"(a), "i"(OFF) : "memory");
  return r;
}
template <int OFF> __device__ __forceinline__ s16x4 rdtr(int a) {
  s16x4 r;
  asm volatile("ds_read_b64_tr_b16 %0, %1 offset:%2" : "=v"(r) : "v"(a), "i"(OFF) : "memory");
  return r;
}
#define WAITL2(N, a, b) asm volatile("s_waitcnt lgkmcnt(" #N ")" : "+v"(a), "+v"(b) :: "memory")
#define WAITL4(N, a, b, c, d) asm volatile("s_waitcnt lgkmcnt(" #N ")" : "+v"(a), "+v"(b), "+v"(c), "+v"(d) :: "memory")
#define WAITL8(N, a, b, c, d, e, f, g_, h_) asm volatile("s_waitcnt lgkmcnt(" #N ")" : "+v"(a), "+v"(b), "+v"(c), "+v"(d), "+v"(e), "+v"(f), "+v"(g_), "+v"(h_) :: "memory")
#define RAW_BAR() __builtin_amdgcn_s_barrier()
#define SBAR() __builtin_amdgcn_sched_barrier(0)
#define OPAQUE(v) asm volatile("" : "+v"(v))
template <int N> struct IC { static constexpr int value = N; };
template <int I, int N, class F> __device__ __forceinline__ void sfor(F&& f) {
  if constexpr (I < N) { f(IC<I>{}); sfor<I + 1, N>(f); }
}

constexpr int G1_BUFX = 288 * 128;
constexpr int G1_BUFW = 64 * 512;
constexpr int G1_STRIDE = G1_BUFX + G1_BUFW;
constexpr int G1_LDS = 288 * 512;

__global__ __launch_bounds__(512, 2) void qproj_kernel(const short* __restrict__ x, const short* __restrict__ wq,
                                                        short* __restrict__ qg, unsigned* __restrict__ counter) {
  extern __shared__ __attribute__((aligned(16))) char smem[];
  const int tid = threadIdx.x, lane = tid & 63;
  const int w = __builtin_amdgcn_readfirstlane(tid >> 6);
  const int xg = blockIdx.x & 7, idx = blockIdx.x >> 3;
  const int mt = xg * 2 + (idx >> 4), nt = idx & 15;
  const int l31 = lane & 31, hh = lane >> 5, l15 = lane & 15;
  const int qp = l15 >> 2, p4 = lane & 3, blk = (lane >> 4) & 1;
  const char* xb = (const char*)(x + (size_t)mt * 288 * DM);
  const char* wb = (const char*)(wq + nt * 256);
  unsigned xsrc[5];
#pragma unroll
  for (int j = 0; j < 5; ++j) {
    int r = 8 * (w + 8 * j) + (lane >> 3), cp = lane & 7;
    if (r > 287) r = 287;
    xsrc[j] = r * 1024 + ((cp ^ ((r >> 1) & 7)) << 4);
  }
  unsigned wsrc[4];
#pragma unroll
  for (int j = 0; j < 4; ++j) {
    int r = 2 * (4 * w + j) + (lane >> 5), cp = lane & 31;
    wsrc[j] = r * (HD * 2) + ((cp ^ ((r & 3) << 2)) << 4);
  }
  auto stage = [&](int t, int buf) {
    char* lx = smem + buf * G1_STRIDE;
    char* lw = lx + G1_BUFX;
    const char* xt = xb + t * 128;
    const char* wt = wb + (size_t)t * (64 * HD * 2);
#pragma unroll
    for (int j = 0; j < 4; ++j) glds16(xt + xsrc[j], lx + (w + 8 * j) * 1024);
    if (w < 4) glds16(xt + xsrc[4], lx + (w + 32) * 1024);
#pragma unroll
    for (int j = 0; j < 4; ++j) glds16(wt + wsrc[j], lw + (4 * w + j) * 1024);
  };
  f32x16 acc[9];
#pragma unroll
  for (int i = 0; i < 9; ++i) acc[i] = f32x16{};
  const int sb = (int)(uintptr_t)smem;
  const int xa = sb + l31 * 128 + ((hh ^ ((l31 >> 1) & 7)) << 4);
  const int wa = sb + G1_BUFX + (8 * hh + qp) * 512 + (((4 * w + 2 * blk + (p4 >> 1)) ^ (qp << 2)) << 4) + 8 * (p4 & 1);
  stage(0, 0);
  auto body = [&](auto BC, int t) {
    constexpr int buf = decltype(BC)::value;
    asm volatile("s_waitcnt vmcnt(0) lgkmcnt(0)" ::: "memory");
    RAW_BAR();
    if (t < 7) stage(t + 1, buf ^ 1);
    SBAR();
    const int xa_ = xa + buf * G1_STRIDE, wa_ = wa + buf * G1_STRIDE;
    sfor<0, 4>([&](auto SC) {
      constexpr int s = decltype(SC)::value;
      const int xs = xa_ ^ (s << 5);
      s16x4 wl = rdtr<s * 8192>(wa_), wh = rdtr<s * 8192 + 2048>(wa_);
      bf16x8 xf[9];
      sfor<0, 9>([&](auto MC) { constexpr int mi = decltype(MC)::value; xf[mi] = rd128<mi * 4096>(xs); });
      asm volatile("s_waitcnt lgkmcnt(6)" : "+v"(wl), "+v"(wh), "+v"(xf[0]), "+v"(xf[1]), "+v"(xf[2]) :: "memory");
      bf16x8 wf = cat8(wl, wh);
      acc[0] = MFMA32(wf, xf[0], acc[0]);
      acc[1] = MFMA32(wf, xf[1], acc[1]);
      acc[2] = MFMA32(wf, xf[2], acc[2]);
      asm volatile("s_waitcnt lgkmcnt(3)" : "+v"(xf[3]), "+v"(xf[4]), "+v"(xf[5]) :: "memory");
      acc[3] = MFMA32(wf, xf[3], acc[3]);
      acc[4] = MFMA32(wf, xf[4], acc[4]);
      acc[5] = MFMA32(wf, xf[5], acc[5]);
      asm volatile("s_waitcnt lgkmcnt(0)" : "+v"(xf[6]), "+v"(xf[7]), "+v"(xf[8]) :: "memory");
      acc[6] = MFMA32(wf, xf[6], acc[6]);
      acc[7] = MFMA32(wf, xf[7], acc[7]);
      acc[8] = MFMA32(wf, xf[8], acc[8]);
    });
  };
  for (int t = 0; t < 8; t += 2) {
    body(IC<0>{}, t);
    body(IC<1>{}, t + 1);
  }
  if (blockIdx.x == 0 && tid == 0) *counter = 512u;
  asm volatile("s_waitcnt lgkmcnt(0)" ::: "memory");
  __syncthreads();
#pragma unroll
  for (int mi = 0; mi < 9; ++mi) {
    const int r = mi * 32 + l31;
#pragma unroll
    for (int i = 0; i < 4; ++i) {
      u32x2 pk = {cvtpk(acc[mi][4 * i], acc[mi][4 * i + 1]), cvtpk(acc[mi][4 * i + 2], acc[mi][4 * i + 3])};
      *(u32x2*)(smem + r * 512 + (((4 * w + i) ^ l31) << 4) + 8 * hh) = pk;
    }
  }
  __syncthreads();
  {
    const int c = tid & 31, r0 = tid >> 5;
    short* qo = qg + (size_t)(mt * 288) * HD + nt * 256 + c * 8;
#pragma unroll
    for (int j = 0; j < 18; ++j) {
      const int r = r0 + 16 * j;
      bf16x8 v = *(const bf16x8*)(smem + r * 512 + ((c ^ (r & 31)) << 4));
      *(bf16x8*)(qo + (size_t)r * HD) = v;
    }
  }
}

constexpr int A_BUFA = 16384;
constexpr int A_BUFB = 16384;
constexpr int A_OFF_A = 0;
constexpr int A_OFF_B = 2 * A_BUFA;
constexpr int A_OFF_Q2 = A_OFF_B + 2 * A_BUFB;
constexpr int A_OFF_SLOT = A_OFF_Q2 + 4096;
constexpr int A_LDS = A_OFF_SLOT + 16;

__global__ __launch_bounds__(256, 2) void attn_kernel(
    const short* __restrict__ qg, const short* __restrict__ kc, const short* __restrict__ vc,
    short* __restrict__ og, unsigned* __restrict__ counter) {
  extern __shared__ __attribute__((aligned(16))) char smem[];
  const int tid = threadIdx.x, lane = tid & 63;
  const int w = __builtin_amdgcn_readfirstlane(tid >> 6);
  const int l31 = lane & 31, hh = lane >> 5, l15 = lane & 15, g = lane >> 4;
  const int qp = l15 >> 2, p4 = lane & 3, blk = (lane >> 4) & 1;
  const int sb = (int)(uintptr_t)smem;
  volatile unsigned* slot = (volatile unsigned*)(smem + A_OFF_SLOT);

  const unsigned ksrc = (16 * w + g) * 256 + ((l15 ^ g) << 4);
  const unsigned vsrc = (16 * w + g) * 256 + ((l15 ^ (g << 2)) << 4);
  auto stage_kv = [&](int pi, int t, int buf, unsigned ko, unsigned vo) {
    const char* kb = (const char*)kc + (size_t)pi * (SK * DH * 2) + t * 16384;
    const char* vb = (const char*)vc + (size_t)pi * (SK * DH * 2) + t * 16384;
#pragma unroll
    for (int i = 0; i < 4; ++i)
      glds16(kb + ((ko ^ (i << 6)) + i * 1024), smem + A_OFF_A + buf * A_BUFA + w * 4096 + i * 1024);
#pragma unroll
    for (int i = 0; i < 4; ++i)
      glds16(vb + (vo + i * 1024), smem + A_OFF_B + buf * A_BUFB + w * 4096 + i * 1024);
  };
  int pidx = (blockIdx.x & 7) * 128 + (blockIdx.x >> 3);
  stage_kv(pidx, 0, 0, ksrc, vsrc);
  if (tid == 0) *slot = atomicAdd(counter, 1u);
  constexpr float CS = 0.088388347648318440f * 1.4426950408889634f;

  for (;;) {
  const int b = pidx >> 5, h = pidx & 31;
  bf16x8 qf[8];
  {
    const short* qrow = qg + (size_t)(b * SQ + 32 * w + l31) * HD + h * DH + 8 * hh;
#pragma unroll
    for (int ks = 0; ks < 8; ++ks) qf[ks] = *(const bf16x8*)(qrow + 16 * ks);
    const int q = tid >> 4, c = tid & 15;
    bf16x8 v = *(const bf16x8*)(qg + (size_t)(b * SQ + 128 + q) * HD + h * DH + 8 * c);
    *(bf16x8*)(smem + A_OFF_Q2 + q * 256 + ((c ^ q) << 4)) = v;
  }
  f32x16 acc[4];
#pragma unroll
  for (int i = 0; i < 4; ++i) acc[i] = f32x16{};
  f32x4 acc2[8];
#pragma unroll
  for (int i = 0; i < 8; ++i) acc2[i] = f32x4{};
  int nidx = 0;
  float m1 = -1e30f, l1 = 0.f, m2 = -1e30f, l2 = 0.f;
  {
    int ka = sb + A_OFF_A + l31 * 256 + ((hh ^ l15) << 4);
    int ka2 = sb + A_OFF_A + (16 * w + l15) * 256 + ((g ^ l15) << 4);
    int q2a = sb + A_OFF_Q2 + l15 * 256 + ((g ^ l15) << 4);
    int va = sb + A_OFF_B + (4 * hh + qp) * 256 + (((2 * blk + (p4 >> 1)) ^ (qp << 2)) << 4) + 8 * (p4 & 1);
    int va2 = sb + A_OFF_B + (16 * w + 4 * g + qp) * 256 + (((p4 >> 1) ^ (qp << 2)) << 4) + 8 * (p4 & 1);
    auto body = [&](auto BC, int t) {
      constexpr int buf = decltype(BC)::value;
      constexpr int OA = buf * A_BUFA, OB = buf * A_BUFB;
      asm volatile("s_waitcnt vmcnt(0) lgkmcnt(0)" ::: "memory");
      RAW_BAR();
      int ka_ = ka, ka2_ = ka2, q2a_ = q2a, va_ = va, va2_ = va2;
      unsigned ko = ksrc, vo = vsrc;
      OPAQUE(ka_); OPAQUE(ka2_); OPAQUE(q2a_); OPAQUE(va_); OPAQUE(va2_); OPAQUE(ko); OPAQUE(vo);
      if (t < 7) {
        stage_kv(pidx, t + 1, buf ^ 1, ko, vo);
      } else {
        const unsigned it = __builtin_amdgcn_readfirstlane(*slot);
        nidx = it < 1024u ? (int)((it & 7) * 128 + (it >> 3)) : -1;
        if (nidx >= 0) stage_kv(nidx, 0, 0, ko, vo);
      }
      SBAR();
      f32x16 s0 = f32x16{}, s1 = f32x16{};
      f32x4 sr = f32x4{};
      bf16x8 kA[4], kB[4];
#pragma unroll
      for (int i = 0; i < 4; ++i) kA[i] = rd128<OA>(ka_ ^ (i << 5));
#pragma unroll
      for (int i = 0; i < 4; ++i) kB[i] = rd128<OA>(ka_ ^ ((4 + i) << 5));
      WAITL4(4, kA[0], kA[1], kA[2], kA[3]);
      s0 = MFMA32(kA[0], qf[0], s0);
      s0 = MFMA32(kA[1], qf[1], s0);
      SBAR();
      s0 = MFMA32(kA[2], qf[2], s0);
      s0 = MFMA32(kA[3], qf[3], s0);
      SBAR();
#pragma unroll
      for (int i = 0; i < 4; ++i) kA[i] = rd128<OA + 8192>(ka_ ^ (i << 5));
      WAITL4(4, kB[0], kB[1], kB[2], kB[3]);
      s0 = MFMA32(kB[0], qf[4], s0);
      s0 = MFMA32(kB[1], qf[5], s0);
      SBAR();
      s0 = MFMA32(kB[2], qf[6], s0);
      s0 = MFMA32(kB[3], qf[7], s0);
      SBAR();
#pragma unroll
      for (int i = 0; i < 4; ++i) kB[i] = rd128<OA + 8192>(ka_ ^ ((4 + i) << 5));
      WAITL4(4, kA[0], kA[1], kA[2], kA[3]);
      s1 = MFMA32(kA[0], qf[0], s1);
      SBAR();
      s1 = MFMA32(kA[1], qf[1], s1);
      float tmax = fmaxf(fmaxf(s0[0], s0[1]), s0[2]);
#pragma unroll
      for (int r = 3; r < 9; r += 2) tmax = fmaxf(fmaxf(tmax, s0[r]), s0[r + 1]);
      SBAR();
      s1 = MFMA32(kA[2], qf[2], s1);
#pragma unroll
      for (int r = 9; r < 15; r += 2) tmax = fmaxf(fmaxf(tmax, s0[r]), s0[r + 1]);
      tmax = fmaxf(tmax, s0[15]);
      tmax = xmax32(tmax);
      float mn = fmaxf(m1, tmax);
      float alpha = __builtin_amdgcn_exp2f((m1 - mn) * CS);
      m1 = mn;
      SBAR();
      s1 = MFMA32(kA[3], qf[3], s1);
      SBAR();
      if (!__all(alpha == 1.f)) {
#pragma unroll
        for (int dt = 0; dt < 4; ++dt)
#pragma unroll
          for (int r = 0; r < 16; ++r) acc[dt][r] *= alpha;
      }
      bf16x8 kr[4], qr[4];
#pragma unroll
      for (int ks = 0; ks < 4; ++ks) { kr[ks] = rd128<OA>(ka2_ ^ (ks << 6)); qr[ks] = rd128<0>(q2a_ ^ (ks << 6)); }
      WAITL4(8, kB[0], kB[1], kB[2], kB[3]);
      bf16x8 pf[4];
      float mc = -mn * CS;
      float ps = 0.f;
      float e[8];
      s1 = MFMA32(kB[0], qf[4], s1);
#pragma unroll
      for (int j = 0; j < 4; ++j) { e[j] = __builtin_amdgcn_exp2f(fmaf(s0[j], CS, mc)); ps += e[j]; }
      SBAR();
      s1 = MFMA32(kB[1], qf[5], s1);
#pragma unroll
      for (int j = 4; j < 8; ++j) { e[j] = __builtin_amdgcn_exp2f(fmaf(s0[j], CS, mc)); ps += e[j]; }
      { u32x4 a = {cvtpk(e[0], e[1]), cvtpk(e[2], e[3]), cvtpk(e[4], e[5]), cvtpk(e[6], e[7])}; pf[0] = __builtin_bit_cast(bf16x8, a); }
      SBAR();
      s1 = MFMA32(kB[2], qf[6], s1);
#pragma unroll
      for (int j = 0; j < 4; ++j) { e[j] = __builtin_amdgcn_exp2f(fmaf(s0[8 + j], CS, mc)); ps += e[j]; }
      SBAR();
      s1 = MFMA32(kB[3], qf[7], s1);
#pragma unroll
      for (int j = 4; j < 8; ++j) { e[j] = __builtin_amdgcn_exp2f(fmaf(s0[8 + j], CS, mc)); ps += e[j]; }
      { u32x4 a = {cvtpk(e[0], e[1]), cvtpk(e[2], e[3]), cvtpk(e[4], e[5]), cvtpk(e[6], e[7])}; pf[1] = __builtin_bit_cast(bf16x8, a); }
      l1 = l1 * alpha + ps;
      OPAQUE(l1);
      SBAR();
      SBAR();
      const int v0 = va_, v1 = va_ ^ 64, v2 = va_ ^ 128, v3 = va_ ^ 192;
      s16x4 vl[2][4], vh[2][4];
      auto issue_v = [&](auto SC) {
        constexpr int s = decltype(SC)::value, u = s & 1;
        vl[u][0] = rdtr<OB + s * 4096>(v0); vh[u][0] = rdtr<OB + s * 4096 + 2048>(v0);
        vl[u][1] = rdtr<OB + s * 4096>(v1); vh[u][1] = rdtr<OB + s * 4096 + 2048>(v1);
        vl[u][2] = rdtr<OB + s * 4096>(v2); vh[u][2] = rdtr<OB + s * 4096 + 2048>(v2);
        vl[u][3] = rdtr<OB + s * 4096>(v3); vh[u][3] = rdtr<OB + s * 4096 + 2048>(v3);
      };
      issue_v(IC<0>{});
      WAITL8(8, kr[0], kr[1], kr[2], kr[3], qr[0], qr[1], qr[2], qr[3]);
#pragma unroll
      for (int ks = 0; ks < 4; ++ks) sr = MFMA16(kr[ks], qr[ks], sr);
      SBAR();
      SBAR();
      issue_v(IC<1>{});
      WAITL8(8, vl[0][0], vh[0][0], vl[0][1], vh[0][1], vl[0][2], vh[0][2], vl[0][3], vh[0][3]);
      acc[0] = MFMA32(cat8(vl[0][0], vh[0][0]), pf[0], acc[0]);
      float tm1 = fmaxf(fmaxf(s1[0], s1[1]), s1[2]);
#pragma unroll
      for (int r = 3; r < 9; r += 2) tm1 = fmaxf(fmaxf(tm1, s1[r]), s1[r + 1]);
      SBAR();
      acc[1] = MFMA32(cat8(vl[0][1], vh[0][1]), pf[0], acc[1]);
#pragma unroll
      for (int r = 9; r < 15; r += 2) tm1 = fmaxf(fmaxf(tm1, s1[r]), s1[r + 1]);
      tm1 = fmaxf(tm1, s1[15]);
      tm1 = xmax32(tm1);
      float mnb = fmaxf(m1, tm1);
      float alphab = __builtin_amdgcn_exp2f((m1 - mnb) * CS);
      m1 = mnb;
      mc = -mnb * CS;
      ps = 0.f;
      SBAR();
      acc[2] = MFMA32(cat8(vl[0][2], vh[0][2]), pf[0], acc[2]);
#pragma unroll
      for (int j = 0; j < 4; ++j) { e[j] = __builtin_amdgcn_exp2f(fmaf(s1[j], CS, mc)); ps += e[j]; }
      SBAR();
      acc[3] = MFMA32(cat8(vl[0][3], vh[0][3]), pf[0], acc[3]);
#pragma unroll
      for (int j = 4; j < 8; ++j) { e[j] = __builtin_amdgcn_exp2f(fmaf(s1[j], CS, mc)); ps += e[j]; }
      { u32x4 a = {cvtpk(e[0], e[1]), cvtpk(e[2], e[3]), cvtpk(e[4], e[5]), cvtpk(e[6], e[7])}; pf[2] = __builtin_bit_cast(bf16x8, a); }
      SBAR();
      issue_v(IC<2>{});
      WAITL8(8, vl[1][0], vh[1][0], vl[1][1], vh[1][1], vl[1][2], vh[1][2], vl[1][3], vh[1][3]);
      acc[0] = MFMA32(cat8(vl[1][0], vh[1][0]), pf[1], acc[0]);
#pragma unroll
      for (int j = 0; j < 4; ++j) { e[j] = __builtin_amdgcn_exp2f(fmaf(s1[8 + j], CS, mc)); ps += e[j]; }
      SBAR();
      acc[1] = MFMA32(cat8(vl[1][1], vh[1][1]), pf[1], acc[1]);
#pragma unroll
      for (int j = 4; j < 8; ++j) { e[j] = __builtin_amdgcn_exp2f(fmaf(s1[8 + j], CS, mc)); ps += e[j]; }
      { u32x4 a = {cvtpk(e[0], e[1]), cvtpk(e[2], e[3]), cvtpk(e[4], e[5]), cvtpk(e[6], e[7])}; pf[3] = __builtin_bit_cast(bf16x8, a); }
      l1 = l1 * alphab + ps;
      OPAQUE(l1);
      SBAR();
      acc[2] = MFMA32(cat8(vl[1][2], vh[1][2]), pf[1], acc[2]);
      float t2 = fmaxf(fmaxf(sr[0], sr[1]), fmaxf(sr[2], sr[3]));
      t2 = xmax16(t2);
      t2 = xmax32(t2);
      float mn2 = fmaxf(m2, t2);
      float alpha2 = __builtin_amdgcn_exp2f((m2 - mn2) * CS);
      m2 = mn2;
      SBAR();
      acc[3] = MFMA32(cat8(vl[1][3], vh[1][3]), pf[1], acc[3]);
      const float mc2 = -mn2 * CS;
      float e0 = __builtin_amdgcn_exp2f(fmaf(sr[0], CS, mc2));
      float e1 = __builtin_amdgcn_exp2f(fmaf(sr[1], CS, mc2));
      float e2 = __builtin_amdgcn_exp2f(fmaf(sr[2], CS, mc2));
      float e3 = __builtin_amdgcn_exp2f(fmaf(sr[3], CS, mc2));
      l2 = l2 * alpha2 + (e0 + e1) + (e2 + e3);
      OPAQUE(l2);
      u32x2 p2u = {cvtpk(e0, e1), cvtpk(e2, e3)};
      s16x4 p2 = __builtin_bit_cast(s16x4, p2u);
      SBAR();
      if (!__all(alphab == 1.f && alpha2 == 1.f)) {
#pragma unroll
        for (int dt = 0; dt < 4; ++dt)
#pragma unroll
          for (int r = 0; r < 16; ++r) acc[dt][r] *= alphab;
#pragma unroll
        for (int dd = 0; dd < 8; ++dd)
#pragma unroll
          for (int r = 0; r < 4; ++r) acc2[dd][r] *= alpha2;
      }
      issue_v(IC<3>{});
      WAITL8(8, vl[0][0], vh[0][0], vl[0][1], vh[0][1], vl[0][2], vh[0][2], vl[0][3], vh[0][3]);
#pragma unroll
      for (int dt = 0; dt < 4; ++dt) acc[dt] = MFMA32(cat8(vl[0][dt], vh[0][dt]), pf[2], acc[dt]);
      SBAR();
      s16x4 vr[8];
#pragma unroll
      for (int dd = 0; dd < 8; ++dd) vr[dd] = rdtr<OB>(va2_ ^ (dd << 5));
      WAITL8(8, vl[1][0], vh[1][0], vl[1][1], vh[1][1], vl[1][2], vh[1][2], vl[1][3], vh[1][3]);
#pragma unroll
      for (int dt = 0; dt < 4; ++dt) acc[dt] = MFMA32(cat8(vl[1][dt], vh[1][dt]), pf[3], acc[dt]);
      WAITL8(0, vr[0], vr[1], vr[2], vr[3], vr[4], vr[5], vr[6], vr[7]);
#pragma unroll
      for (int dd = 0; dd < 8; ++dd) acc2[dd] = MFMA16K16(vr[dd], p2, acc2[dd]);
    };
    for (int t = 0; t < 8; t += 2) {
      body(IC<0>{}, t);
      body(IC<1>{}, t + 1);
    }
  }

  int tz = threadIdx.x;
  OPAQUE(tz);
  asm volatile("s_waitcnt lgkmcnt(0)" ::: "memory");
  RAW_BAR();
  {
    const int l15 = tz & 15, g = (tz >> 4) & 3;
    float lt2 = xsum32(xsum16(l2));
    float* pw = (float*)(smem + ((w & 1) ? A_OFF_B + A_BUFB : A_OFF_A + A_BUFA) + (w >> 1) * 8192);
#pragma unroll
    for (int dd = 0; dd < 8; ++dd) *(f32x4*)(pw + l15 * 128 + 16 * dd + 4 * g) = acc2[dd];
    float* st = (float*)(smem + A_OFF_Q2);
    if (g == 0) { st[w * 32 + l15] = m2; st[w * 32 + 16 + l15] = lt2; }
  }
  asm volatile("s_waitcnt lgkmcnt(0)" ::: "memory");
  RAW_BAR();
  {
    const int q = tz >> 4, d0 = (tz & 15) * 8;
    const float* st = (const float*)(smem + A_OFF_Q2);
    float mm = fmaxf(fmaxf(st[q], st[32 + q]), fmaxf(st[64 + q], st[96 + q]));
    float o[8] = {0, 0, 0, 0, 0, 0, 0, 0};
    float ls = 0.f;
#pragma unroll
    for (int ww = 0; ww < 4; ++ww) {
      float sc = __builtin_amdgcn_exp2f((st[ww * 32 + q] - mm) * CS);
      ls += st[ww * 32 + 16 + q] * sc;
      const float* pw = (const float*)(smem + ((ww & 1) ? A_OFF_B + A_BUFB : A_OFF_A + A_BUFA) + (ww >> 1) * 8192) + q * 128 + d0;
      f32x4 a = *(const f32x4*)pw, c = *(const f32x4*)(pw + 4);
#pragma unroll
      for (int j = 0; j < 4; ++j) { o[j] += a[j] * sc; o[4 + j] += c[j] * sc; }
    }
    float inv = 1.f / ls;
    u32x4 pk = {cvtpk(o[0] * inv, o[1] * inv), cvtpk(o[2] * inv, o[3] * inv),
                cvtpk(o[4] * inv, o[5] * inv), cvtpk(o[6] * inv, o[7] * inv)};
    *(u32x4*)(og + (size_t)(b * SQ + 128 + q) * HD + h * DH + d0) = pk;
  }
  asm volatile("s_waitcnt lgkmcnt(0)" ::: "memory");
  RAW_BAR();
  {
    const int l31 = tz & 31, hh = (tz >> 5) & 1;
    float lt = xsum32(l1);
    float inv = 1.f / lt;
    char* ob = smem + (w < 2 ? A_OFF_A + A_BUFA : A_OFF_B + A_BUFB) + ((w & 1) * 32 + l31) * 256 + 8 * hh;
#pragma unroll
    for (int dt = 0; dt < 4; ++dt)
#pragma unroll
      for (int i = 0; i < 4; ++i) {
        u32x2 pk = {cvtpk(acc[dt][4 * i] * inv, acc[dt][4 * i + 1] * inv),
                    cvtpk(acc[dt][4 * i + 2] * inv, acc[dt][4 * i + 3] * inv)};
        *(u32x2*)(ob + (((4 * dt + i) ^ (l31 & 15)) << 4)) = pk;
      }
    if (tz == 0 && nidx >= 0) *slot = atomicAdd(counter, 1u);
  }
  asm volatile("s_waitcnt lgkmcnt(0)" ::: "memory");
  RAW_BAR();
  {
    const int c = tz & 15, r0 = tz >> 4;
    short* orow = og + (size_t)(b * SQ + r0) * HD + h * DH + c * 8;
#pragma unroll
    for (int j = 0; j < 8; ++j) {
      const int r = r0 + 16 * j;
      const char* src = smem + (j < 4 ? A_OFF_A + A_BUFA + r * 256 : A_OFF_B + A_BUFB + (r - 64) * 256) + ((c ^ r0) << 4);
      bf16x8 v = *(const bf16x8*)src;
      *(bf16x8*)(orow + (size_t)(16 * j) * HD) = v;
    }
  }
  if (nidx < 0) break;
  pidx = nidx;
  }
}

constexpr int B_NST = 4;
constexpr int B_BUFA = 20480;
constexpr int B_BUFB = 8192;
constexpr int B_STRIDE = B_BUFA + B_BUFB;
constexpr int B_LDS = B_NST * B_STRIDE;

__global__ __launch_bounds__(256, 1) void oproj_kernel(const short* __restrict__ og,
                                                        const short* __restrict__ wo,
                                                        short* __restrict__ out) {
  extern __shared__ __attribute__((aligned(16))) char smem[];
  const int tid = threadIdx.x, lane = tid & 63;
  const int w = __builtin_amdgcn_readfirstlane(tid >> 6);
  const int xg = blockIdx.x & 7, idx = blockIdx.x >> 3;
  const int b = xg * 4 + (idx >> 3), nt0 = idx & 7;
  const char* ob = (const char*)(og + (size_t)b * SQ * HD);
  const char* wob = (const char*)(wo + nt0 * 64);
  const int l31 = lane & 31, hh = lane >> 5, l15 = lane & 15, g = lane >> 4;
  const int qp = l15 >> 2, p4 = lane & 3, blk = (lane >> 4) & 1;
  unsigned osrc[5], wsrc[2];
#pragma unroll
  for (int ii = 0; ii < 5; ++ii) {
    int r = 8 * (w * 5 + ii) + (lane >> 3), cp = lane & 7;
    int rs = r < SQ ? r : SQ - 1;
    osrc[ii] = rs * (HD * 2) + ((cp ^ ((r >> 1) & 7)) << 4);
  }
#pragma unroll
  for (int ii = 0; ii < 2; ++ii) {
    int r = 8 * (w * 2 + ii) + (lane >> 3), cp = lane & 7;
    wsrc[ii] = r * (DM * 2) + ((cp ^ (((r >> 1) & 1) << 2)) << 4);
  }
  auto stage = [&](int t, int buf) {
    char* la = smem + buf * B_STRIDE + w * 5120;
    char* lb = smem + buf * B_STRIDE + B_BUFA + w * 2048;
    const char* ot = ob + t * 128;
    const char* wt = wob + (size_t)t * (64 * DM * 2);
#pragma unroll
    for (int ii = 0; ii < 5; ++ii) glds16(ot + osrc[ii], la + ii * 1024);
#pragma unroll
    for (int ii = 0; ii < 2; ++ii) glds16(wt + wsrc[ii], lb + ii * 1024);
  };

  f32x16 acc[2] = {f32x16{}, f32x16{}};
  f32x4 acc2 = f32x4{};
  constexpr int NT = HD / 64;
  const int sb = (int)(uintptr_t)smem;
  const int xa = sb + (32 * w + l31) * 128 + ((hh ^ ((l31 >> 1) & 7)) << 4);
  const int wa = sb + B_BUFA + (8 * hh + qp) * 128 + (((2 * blk + (p4 >> 1)) ^ ((qp >> 1) << 2)) << 4) + 8 * (p4 & 1);
  const int xa2 = sb + (128 + l15) * 128 + ((g ^ ((l15 >> 1) & 7)) << 4);
  const int wa2 = sb + B_BUFA + (8 * g + qp) * 128 + (((2 * w + (p4 >> 1)) ^ ((qp >> 1) << 2)) << 4) + 8 * (p4 & 1);
  stage(0, 0);
  stage(1, 1);
  stage(2, 2);
  auto body = [&](auto BC, int t) {
    constexpr int buf = decltype(BC)::value;
    constexpr int OB = (buf & 1) * B_STRIDE;
    const int hi2 = (buf >> 1) * 2 * B_STRIDE;
    if (t < NT - 2) asm volatile("s_waitcnt vmcnt(14)" ::: "memory");
    else if (t == NT - 2) asm volatile("s_waitcnt vmcnt(7)" ::: "memory");
    else asm volatile("s_waitcnt vmcnt(0)" ::: "memory");
    RAW_BAR();
    if (t + 3 < NT) stage(t + 3, (buf + 3) & 3);
    const int xa_ = xa + hi2, w0 = wa + hi2, w1 = (wa ^ 64) + hi2, xa2_ = xa2 + hi2, wa2_ = wa2 + hi2;
    bf16x8 xf[4], xr[2];
    s16x4 wl[4][2], wh[4][2], rl[2], rh[2];
    sfor<0, 4>([&](auto SC) {
      constexpr int s = decltype(SC)::value;
      xf[s] = rd128<OB>(xa_ ^ (s << 5));
      wl[s][0] = rdtr<OB + s * 2048>(w0); wh[s][0] = rdtr<OB + s * 2048 + 512>(w0);
      wl[s][1] = rdtr<OB + s * 2048>(w1); wh[s][1] = rdtr<OB + s * 2048 + 512>(w1);
      if constexpr (s >= 1) {
        asm volatile("s_waitcnt lgkmcnt(5)" : "+v"(xf[s - 1]), "+v"(wl[s - 1][0]), "+v"(wh[s - 1][0]), "+v"(wl[s - 1][1]), "+v"(wh[s - 1][1]) :: "memory");
        acc[0] = MFMA32(cat8(wl[s - 1][0], wh[s - 1][0]), xf[s - 1], acc[0]);
        acc[1] = MFMA32(cat8(wl[s - 1][1], wh[s - 1][1]), xf[s - 1], acc[1]);
      }
    });
    xr[0] = rd128<OB>(xa2_); rl[0] = rdtr<OB>(wa2_); rh[0] = rdtr<OB + 512>(wa2_);
    xr[1] = rd128<OB>(xa2_ ^ 64); rl[1] = rdtr<OB + 4096>(wa2_); rh[1] = rdtr<OB + 4096 + 512>(wa2_);
    asm volatile("s_waitcnt lgkmcnt(6)" : "+v"(xf[3]), "+v"(wl[3][0]), "+v"(wh[3][0]), "+v"(wl[3][1]), "+v"(wh[3][1]) :: "memory");
    acc[0] = MFMA32(cat8(wl[3][0], wh[3][0]), xf[3], acc[0]);
    acc[1] = MFMA32(cat8(wl[3][1], wh[3][1]), xf[3], acc[1]);
    asm volatile("s_waitcnt lgkmcnt(0)" : "+v"(xr[0]), "+v"(rl[0]), "+v"(rh[0]), "+v"(xr[1]), "+v"(rl[1]), "+v"(rh[1]) :: "memory");
    acc2 = MFMA16(cat8(rl[0], rh[0]), xr[0], acc2);
    acc2 = MFMA16(cat8(rl[1], rh[1]), xr[1], acc2);
  };
  for (int t = 0; t < NT; t += 4) {
    body(IC<0>{}, t);
    body(IC<1>{}, t + 1);
    body(IC<2>{}, t + 2);
    body(IC<3>{}, t + 3);
  }
  {
    short* orow = out + (size_t)(b * SQ + 32 * w + l31) * DM + nt0 * 64 + 4 * hh;
#pragma unroll
    for (int nt = 0; nt < 2; ++nt)
#pragma unroll
      for (int i = 0; i < 4; ++i) {
        u32x2 pk = {cvtpk(acc[nt][4 * i], acc[nt][4 * i + 1]), cvtpk(acc[nt][4 * i + 2], acc[nt][4 * i + 3])};
        *(u32x2*)(orow + 32 * nt + 8 * i) = pk;
      }
    u32x2 pk = {cvtpk(acc2[0], acc2[1]), cvtpk(acc2[2], acc2[3])};
    *(u32x2*)(out + (size_t)(b * SQ + 128 + l15) * DM + nt0 * 64 + 16 * w + 4 * g) = pk;
  }
}

extern "C" void kernel_launch(void* const* d_in, const int* in_sizes, int n_in, void* d_out,
                              int out_size, void* d_ws, size_t ws_size, hipStream_t stream) {
  const short* x = (const short*)d_in[0];
  const short* kc = (const short*)d_in[1];
  const short* vc = (const short*)d_in[2];
  const short* wq = (const short*)d_in[3];
  const short* wo = (const short*)d_in[4];
  short* qg = (short*)d_ws;
  short* og = qg + (size_t)MROWS * HD;
  short* out = (short*)d_out;
  (void)hipFuncSetAttribute((const void*)qproj_kernel, hipFuncAttributeMaxDynamicSharedMemorySize, G1_LDS);
  (void)hipFuncSetAttribute((const void*)attn_kernel, hipFuncAttributeMaxDynamicSharedMemorySize, A_LDS);
  (void)hipFuncSetAttribute((const void*)oproj_kernel, hipFuncAttributeMaxDynamicSharedMemorySize, B_LDS);
  unsigned* counter = (unsigned*)(og + (size_t)MROWS * HD);
  qproj_kernel<<<256, 512, G1_LDS, stream>>>(x, wq, qg, counter);
  attn_kernel<<<512, 256, A_LDS, stream>>>(qg, kc, vc, og, counter);
  oproj_kernel<<<256, 256, B_LDS, stream>>>(og, wo, out);
}
```

```cpp
#include <hip/hip_runtime.h>
#include <hip/hip_bf16.h>

typedef __attribute__((ext_vector_type(8))) short bf16x8;
typedef __attribute__((ext_vector_type(4))) short s16x4;
typedef __attribute__((ext_vector_type(16))) float f32x16;
typedef __attribute__((ext_vector_type(4))) float f32x4;
typedef __attribute__((ext_vector_type(4))) unsigned u32x4;
typedef __attribute__((ext_vector_type(2))) unsigned u32x2;

constexpr int NB = 32, SQ = 144, DM = 512, NH = 32, SK = 512, DH = 128;
constexpr int HD = NH * DH;
constexpr int MROWS = NB * SQ;

#define MFMA32(a, b, c) __builtin_amdgcn_mfma_f32_32x32x16_bf16((a), (b), (c), 0, 0, 0)
#define MFMA16(a, b, c) __builtin_amdgcn_mfma_f32_16x16x32_bf16((a), (b), (c), 0, 0, 0)
#define MFMA16K16(a, b, c) __builtin_amdgcn_mfma_f32_16x16x16bf16_1k((a), (b), (c), 0, 0, 0)

__device__ __forceinline__ unsigned cvtpk(float lo, float hi) {
  unsigned r;
  asm volatile("v_cvt_pk_bf16_f32 %0, %1, %2" : "=v"(r) : "v"(lo), "v"(hi));
  return r;
}
__device__ __forceinline__ bf16x8 cat8(s16x4 lo, s16x4 hi) {
  return __builtin_shufflevector(lo, hi, 0, 1, 2, 3, 4, 5, 6, 7);
}
__device__ __forceinline__ void glds16(const void* g, char* l) {
  __builtin_amdgcn_global_load_lds((const unsigned*)g, (unsigned*)l, 16, 0, 0);
}
__device__ __forceinline__ float xmax32(float v) {
  auto rr = __builtin_amdgcn_permlane32_swap(__float_as_uint(v), __float_as_uint(v), false, false);
  return fmaxf(__uint_as_float(rr[0]), __uint_as_float(rr[1]));
}
__device__ __forceinline__ float xsum32(float v) {
  auto rr = __builtin_amdgcn_permlane32_swap(__float_as_uint(v), __float_as_uint(v), false, false);
  return __uint_as_float(rr[0]) + __uint_as_float(rr[1]);
}
__device__ __forceinline__ float xmax16(float v) {
  auto rr = __builtin_amdgcn_permlane16_swap(__float_as_uint(v), __float_as_uint(v), false, false);
  return fmaxf(__uint_as_float(rr[0]), __uint_as_float(rr[1]));
}
__device__ __forceinline__ float xsum16(float v) {
  auto rr = __builtin_amdgcn_permlane16_swap(__float_as_uint(v), __float_as_uint(v), false, false);
  return __uint_as_float(rr[0]) + __uint_as_float(rr[1]);
}
template <int OFF> __device__ __forceinline__ bf16x8 rd128(int a) {
  bf16x8 r;
  asm volatile("ds_read_b128 %0, %1 offset:%2" : "=v"(r) : "v"(a), "i"(OFF) : "memory");
  return r;
}
template <int OFF> __device__ __forceinline__ s16x4 rdtr(int a) {
  s16x4 r;
  asm volatile("ds_read_b64_tr_b16 %0, %1 offset:%2" : "=v"(r) : "v"(a), "i"(OFF) : "memory");
  return r;
}
#define WAITL2(N, a, b) asm volatile("s_waitcnt lgkmcnt(" #N ")" : "+v"(a), "+v"(b) :: "memory")
#define WAITL4(N, a, b, c, d) asm volatile("s_waitcnt lgkmcnt(" #N ")" : "+v"(a), "+v"(b), "+v"(c), "+v"(d) :: "memory")
#define WAITL8(N, a, b, c, d, e, f, g_, h_) asm volatile("s_waitcnt lgkmcnt(" #N ")" : "+v"(a), "+v"(b), "+v"(c), "+v"(d), "+v"(e), "+v"(f), "+v"(g_), "+v"(h_) :: "memory")
#define RAW_BAR() __builtin_amdgcn_s_barrier()
#define SBAR() __builtin_amdgcn_sched_barrier(0)
#define OPAQUE(v) asm volatile("" : "+v"(v))
template <int N> struct IC { static constexpr int value = N; };
template <int I, int N, class F> __device__ __forceinline__ void sfor(F&& f) {
  if constexpr (I < N) { f(IC<I>{}); sfor<I + 1, N>(f); }
}

constexpr int A_BUFA = 20480;
constexpr int A_BUFB = 16384;
constexpr int A_OFF_A = 0;
constexpr int A_OFF_B = 2 * A_BUFA;
constexpr int A_OFF_Q2 = A_OFF_B + 2 * A_BUFB;
constexpr int A_OFF_SLOT = A_OFF_Q2 + 4096;
constexpr int A_LDS = A_OFF_SLOT + 16;

__global__ __launch_bounds__(256, 2) void attn_kernel(
    const short* __restrict__ x, const short* __restrict__ wq, const short* __restrict__ kc,
    const short* __restrict__ vc, short* __restrict__ og, unsigned* __restrict__ counter) {
  extern __shared__ __attribute__((aligned(16))) char smem[];
  const int tid = threadIdx.x, lane = tid & 63;
  const int w = __builtin_amdgcn_readfirstlane(tid >> 6);
  const int l31 = lane & 31, hh = lane >> 5, l15 = lane & 15, g = lane >> 4;
  const int qp = l15 >> 2, p4 = lane & 3, blk = (lane >> 4) & 1;
  const int sb = (int)(uintptr_t)smem;
  volatile unsigned* slot = (volatile unsigned*)(smem + A_OFF_SLOT);

  const unsigned ksrc = (16 * w + g) * 256 + ((l15 ^ g) << 4);
  const unsigned vsrc = (16 * w + g) * 256 + ((l15 ^ (g << 2)) << 4);
  auto stage_kv = [&](int pi, int t, int buf, unsigned ko, unsigned vo) {
    const char* kb = (const char*)kc + (size_t)pi * (SK * DH * 2) + t * 16384;
    const char* vb = (const char*)vc + (size_t)pi * (SK * DH * 2) + t * 16384;
#pragma unroll
    for (int i = 0; i < 4; ++i)
      glds16(kb + ((ko ^ (i << 6)) + i * 1024), smem + A_OFF_A + buf * A_BUFA + w * 4096 + i * 1024);
#pragma unroll
    for (int i = 0; i < 4; ++i)
      glds16(vb + (vo + i * 1024), smem + A_OFF_B + buf * A_BUFB + w * 4096 + i * 1024);
  };
  auto stage_q = [&](int pi, int t, int buf) {
    int ln = lane;
    OPAQUE(ln);
    const char* xt = (const char*)(x + (size_t)(pi >> 5) * SQ * DM) + t * 128;
    const char* wt = (const char*)(wq + (pi & 31) * DH) + (size_t)t * (64 * HD * 2);
#pragma unroll
    for (int ii = 0; ii < 5; ++ii) {
      int r = 8 * (w * 5 + ii) + (ln >> 3), cp = ln & 7;
      int rs = r < SQ ? r : SQ - 1;
      unsigned so = rs * 1024 + ((cp ^ ((r >> 1) & 7)) << 4);
      glds16(xt + so, smem + A_OFF_A + buf * A_BUFA + w * 5120 + ii * 1024);
    }
    const unsigned wo0 = (16 * w + (ln >> 4)) * (HD * 2) + (((ln & 15) ^ ((ln >> 4) << 2)) << 4);
#pragma unroll
    for (int ii = 0; ii < 4; ++ii)
      glds16(wt + (wo0 + ii * 4 * HD * 2), smem + A_OFF_B + buf * A_BUFB + w * 4096 + ii * 1024);
  };
  int pidx = (blockIdx.x & 7) * 128 + (blockIdx.x >> 3);
  stage_q(pidx, 0, 0);
  if (tid == 0) *slot = atomicAdd(counter, 1u) + 512u;
  constexpr float CS = 0.088388347648318440f * 1.4426950408889634f;

  for (;;) {
  const int b = pidx >> 5, h = pidx & 31;
  f32x16 acc[4];
#pragma unroll
  for (int i = 0; i < 4; ++i) acc[i] = f32x16{};
  f32x4 acc2[8];
#pragma unroll
  for (int i = 0; i < 8; ++i) acc2[i] = f32x4{};
  {
    int lq = lane;
    OPAQUE(lq);
    const int l31 = lq & 31, hh = lq >> 5, l15 = lq & 15, g = lq >> 4;
    const int qp = l15 >> 2, p4 = lq & 3, blk = (lq >> 4) & 1;
    const int xa = sb + A_OFF_A + (32 * w + l31) * 128 + ((hh ^ ((l31 >> 1) & 7)) << 4);
    const int wa = sb + A_OFF_B + (8 * hh + qp) * 256 + (((2 * blk + (p4 >> 1)) ^ (qp << 2)) << 4) + 8 * (p4 & 1);
    const int xa2 = sb + A_OFF_A + (128 + l15) * 128 + ((g ^ ((l15 >> 1) & 7)) << 4);
    const int wa2 = sb + A_OFF_B + (8 * g + qp) * 256 + (((4 * w + (p4 >> 1)) ^ (qp << 2)) << 4) + 8 * (p4 & 1);
    auto body = [&](auto BC, int t) {
      constexpr int buf = decltype(BC)::value;
      constexpr int OA = buf * A_BUFA, OB = buf * A_BUFB;
      asm volatile("s_waitcnt vmcnt(0) lgkmcnt(0)" ::: "memory");
      RAW_BAR();
      if (t < 7) stage_q(pidx, t + 1, buf ^ 1);
      else stage_kv(pidx, 0, buf ^ 1, ksrc, vsrc);
      SBAR();
      const int w0 = wa, w1 = wa ^ 64, w2 = wa ^ 128, w3 = wa ^ 192;
      bf16x8 xf[2];
      s16x4 wl[2][4], wh[2][4];
      bf16x8 xr[2];
      s16x4 rl[2][2], rh[2][2];
      auto issue = [&](auto SC) {
        constexpr int s = decltype(SC)::value, u = s & 1;
        xf[u] = rd128<OA>(xa ^ (s << 5));
        wl[u][0] = rdtr<OB + s * 4096>(w0); wh[u][0] = rdtr<OB + s * 4096 + 1024>(w0);
        wl[u][1] = rdtr<OB + s * 4096>(w1); wh[u][1] = rdtr<OB + s * 4096 + 1024>(w1);
        wl[u][2] = rdtr<OB + s * 4096>(w2); wh[u][2] = rdtr<OB + s * 4096 + 1024>(w2);
        wl[u][3] = rdtr<OB + s * 4096>(w3); wh[u][3] = rdtr<OB + s * 4096 + 1024>(w3);
      };
      issue(IC<0>{});
      sfor<0, 4>([&](auto SC) {
        constexpr int s = decltype(SC)::value, u = s & 1;
        if constexpr (s < 3) {
          issue(IC<s + 1>{});
          asm volatile("s_waitcnt lgkmcnt(9)" : "+v"(xf[u]), "+v"(wl[u][0]), "+v"(wh[u][0]), "+v"(wl[u][1]), "+v"(wh[u][1]),
                       "+v"(wl[u][2]), "+v"(wh[u][2]), "+v"(wl[u][3]), "+v"(wh[u][3]) :: "memory");
        } else {
          xr[0] = rd128<OA>(xa2); xr[1] = rd128<OA>(xa2 ^ 64);
          rl[0][0] = rdtr<OB>(wa2); rh[0][0] = rdtr<OB + 1024>(wa2);
          rl[0][1] = rdtr<OB>(wa2 ^ 32); rh[0][1] = rdtr<OB + 1024>(wa2 ^ 32);
          rl[1][0] = rdtr<OB + 8192>(wa2); rh[1][0] = rdtr<OB + 8192 + 1024>(wa2);
          rl[1][1] = rdtr<OB + 8192>(wa2 ^ 32); rh[1][1] = rdtr<OB + 8192 + 1024>(wa2 ^ 32);
          asm volatile("s_waitcnt lgkmcnt(10)" : "+v"(xf[u]), "+v"(wl[u][0]), "+v"(wh[u][0]), "+v"(wl[u][1]), "+v"(wh[u][1]),
                       "+v"(wl[u][2]), "+v"(wh[u][2]), "+v"(wl[u][3]), "+v"(wh[u][3]) :: "memory");
        }
#pragma unroll
        for (int dt = 0; dt < 4; ++dt) acc[dt] = MFMA32(cat8(wl[u][dt], wh[u][dt]), xf[u], acc[dt]);
        SBAR();
      });
      asm volatile("s_waitcnt lgkmcnt(0)" : "+v"(xr[0]), "+v"(xr[1]), "+v"(rl[0][0]), "+v"(rh[0][0]), "+v"(rl[0][1]), "+v"(rh[0][1]),
                   "+v"(rl[1][0]), "+v"(rh[1][0]), "+v"(rl[1][1]), "+v"(rh[1][1]) :: "memory");
#pragma unroll
      for (int s2 = 0; s2 < 2; ++s2)
#pragma unroll
        for (int e = 0; e < 2; ++e) acc2[e] = MFMA16(cat8(rl[s2][e], rh[s2][e]), xr[s2], acc2[e]);
    };
    for (int t = 0; t < 8; t += 2) {
      body(IC<0>{}, t);
      body(IC<1>{}, t + 1);
    }
  }
  bf16x8 qf[8];
#pragma unroll
  for (int dt = 0; dt < 4; ++dt) {
#pragma unroll
    for (int s = 0; s < 2; ++s) {
      unsigned a0 = cvtpk(acc[dt][8 * s + 0], acc[dt][8 * s + 1]);
      unsigned a1 = cvtpk(acc[dt][8 * s + 2], acc[dt][8 * s + 3]);
      unsigned b0 = cvtpk(acc[dt][8 * s + 4], acc[dt][8 * s + 5]);
      unsigned b1 = cvtpk(acc[dt][8 * s + 6], acc[dt][8 * s + 7]);
      auto r0 = __builtin_amdgcn_permlane32_swap(a0, b0, false, false);
      auto r1 = __builtin_amdgcn_permlane32_swap(a1, b1, false, false);
      u32x4 wv = {r0[0], r1[0], r0[1], r1[1]};
      qf[2 * dt + s] = __builtin_bit_cast(bf16x8, wv);
    }
  }
  {
#pragma unroll
    for (int e = 0; e < 2; ++e) {
      int dd = 2 * w + e;
      u32x2 pk = {cvtpk(acc2[e][0], acc2[e][1]), cvtpk(acc2[e][2], acc2[e][3])};
      int ch = (2 * dd + (g >> 1)) ^ l15;
      asm volatile("ds_write_b64 %0, %1" :: "v"(sb + A_OFF_Q2 + l15 * 256 + (ch << 4) + 8 * (g & 1)), "v"(pk) : "memory");
    }
  }
#pragma unroll
  for (int i = 0; i < 4; ++i) acc[i] = f32x16{};
#pragma unroll
  for (int i = 0; i < 8; ++i) acc2[i] = f32x4{};
  int nidx = 0;
  float m1 = -1e30f, l1 = 0.f, m2 = -1e30f, l2 = 0.f;
  {
    int ka = sb + A_OFF_A + l31 * 256 + ((hh ^ l15) << 4);
    int ka2 = sb + A_OFF_A + (16 * w + l15) * 256 + ((g ^ l15) << 4);
    int q2a = sb + A_OFF_Q2 + l15 * 256 + ((g ^ l15) << 4);
    int va = sb + A_OFF_B + (4 * hh + qp) * 256 + (((2 * blk + (p4 >> 1)) ^ (qp << 2)) << 4) + 8 * (p4 & 1);
    int va2 = sb + A_OFF_B + (16 * w + 4 * g + qp) * 256 + (((p4 >> 1) ^ (qp << 2)) << 4) + 8 * (p4 & 1);
    auto body = [&](auto BC, int t) {
      constexpr int buf = decltype(BC)::value;
      constexpr int OA = buf * A_BUFA, OB = buf * A_BUFB;
      asm volatile("s_waitcnt vmcnt(0) lgkmcnt(0)" ::: "memory");
      RAW_BAR();
      int ka_ = ka, ka2_ = ka2, q2a_ = q2a, va_ = va, va2_ = va2;
      unsigned ko = ksrc, vo = vsrc;
      OPAQUE(ka_); OPAQUE(ka2_); OPAQUE(q2a_); OPAQUE(va_); OPAQUE(va2_); OPAQUE(ko); OPAQUE(vo);
      if (t < 7) {
        stage_kv(pidx, t + 1, buf ^ 1, ko, vo);
      } else {
        const unsigned it = __builtin_amdgcn_readfirstlane(*slot);
        nidx = it < 1024u ? (int)((it & 7) * 128 + (it >> 3)) : -1;
        if (nidx >= 0) stage_q(nidx, 0, 0);
      }
      SBAR();
      f32x16 s0 = f32x16{}, s1 = f32x16{};
      f32x4 sr = f32x4{};
      bf16x8 kA[4], kB[4];
#pragma unroll
      for (int i = 0; i < 4; ++i) kA[i] = rd128<OA>(ka_ ^ (i << 5));
#pragma unroll
      for (int i = 0; i < 4; ++i) kB[i] = rd128<OA>(ka_ ^ ((4 + i) << 5));
      WAITL4(4, kA[0], kA[1], kA[2], kA[3]);
      s0 = MFMA32(kA[0], qf[0], s0);
      s0 = MFMA32(kA[1], qf[1], s0);
      SBAR();
      s0 = MFMA32(kA[2], qf[2], s0);
      s0 = MFMA32(kA[3], qf[3], s0);
      SBAR();
#pragma unroll
      for (int i = 0; i < 4; ++i) kA[i] = rd128<OA + 8192>(ka_ ^ (i << 5));
      WAITL4(4, kB[0], kB[1], kB[2], kB[3]);
      s0 = MFMA32(kB[0], qf[4], s0);
      s0 = MFMA32(kB[1], qf[5], s0);
      SBAR();
      s0 = MFMA32(kB[2], qf[6], s0);
      s0 = MFMA32(kB[3], qf[7], s0);
      SBAR();
#pragma unroll
      for (int i = 0; i < 4; ++i) kB[i] = rd128<OA + 8192>(ka_ ^ ((4 + i) << 5));
      WAITL4(4, kA[0], kA[1], kA[2], kA[3]);
      s1 = MFMA32(kA[0], qf[0], s1);
      SBAR();
      s1 = MFMA32(kA[1], qf[1], s1);
      float tmax = fmaxf(fmaxf(s0[0], s0[1]), s0[2]);
#pragma unroll
      for (int r = 3; r < 9; r += 2) tmax = fmaxf(fmaxf(tmax, s0[r]), s0[r + 1]);
      SBAR();
      s1 = MFMA32(kA[2], qf[2], s1);
#pragma unroll
      for (int r = 9; r < 15; r += 2) tmax = fmaxf(fmaxf(tmax, s0[r]), s0[r + 1]);
      tmax = fmaxf(tmax, s0[15]);
      tmax = xmax32(tmax);
      float mn = fmaxf(m1, tmax);
      float alpha = __builtin_amdgcn_exp2f((m1 - mn) * CS);
      m1 = mn;
      SBAR();
      s1 = MFMA32(kA[3], qf[3], s1);
      SBAR();
      if (!__all(alpha == 1.f)) {
#pragma unroll
        for (int dt = 0; dt < 4; ++dt)
#pragma unroll
          for (int r = 0; r < 16; ++r) acc[dt][r] *= alpha;
      }
      bf16x8 kr[4], qr[4];
#pragma unroll
      for (int ks = 0; ks < 4; ++ks) { kr[ks] = rd128<OA>(ka2_ ^ (ks << 6)); qr[ks] = rd128<0>(q2a_ ^ (ks << 6)); }
      WAITL4(8, kB[0], kB[1], kB[2], kB[3]);
      bf16x8 pf[4];
      float mc = -mn * CS;
      float ps = 0.f;
      float e[8];
      s1 = MFMA32(kB[0], qf[4], s1);
#pragma unroll
      for (int j = 0; j < 4; ++j) { e[j] = __builtin_amdgcn_exp2f(fmaf(s0[j], CS, mc)); ps += e[j]; }
      SBAR();
      s1 = MFMA32(kB[1], qf[5], s1);
#pragma unroll
      for (int j = 4; j < 8; ++j) { e[j] = __builtin_amdgcn_exp2f(fmaf(s0[j], CS, mc)); ps += e[j]; }
      { u32x4 a = {cvtpk(e[0], e[1]), cvtpk(e[2], e[3]), cvtpk(e[4], e[5]), cvtpk(e[6], e[7])}; pf[0] = __builtin_bit_cast(bf16x8, a); }
      SBAR();
      s1 = MFMA32(kB[2], qf[6], s1);
#pragma unroll
      for (int j = 0; j < 4; ++j) { e[j] = __builtin_amdgcn_exp2f(fmaf(s0[8 + j], CS, mc)); ps += e[j]; }
      SBAR();
      s1 = MFMA32(kB[3], qf[7], s1);
#pragma unroll
      for (int j = 4; j < 8; ++j) { e[j] = __builtin_amdgcn_exp2f(fmaf(s0[8 + j], CS, mc)); ps += e[j]; }
      { u32x4 a = {cvtpk(e[0], e[1]), cvtpk(e[2], e[3]), cvtpk(e[4], e[5]), cvtpk(e[6], e[7])}; pf[1] = __builtin_bit_cast(bf16x8, a); }
      l1 = l1 * alpha + ps;
      OPAQUE(l1);
      SBAR();
      SBAR();
      const int v0 = va_, v1 = va_ ^ 64, v2 = va_ ^ 128, v3 = va_ ^ 192;
      s16x4 vl[2][4], vh[2][4];
      auto issue_v = [&](auto SC) {
        constexpr int s = decltype(SC)::value, u = s & 1;
        vl[u][0] = rdtr<OB + s * 4096>(v0); vh[u][0] = rdtr<OB + s * 4096 + 2048>(v0);
        vl[u][1] = rdtr<OB + s * 4096>(v1); vh[u][1] = rdtr<OB + s * 4096 + 2048>(v1);
        vl[u][2] = rdtr<OB + s * 4096>(v2); vh[u][2] = rdtr<OB + s * 4096 + 2048>(v2);
        vl[u][3] = rdtr<OB + s * 4096>(v3); vh[u][3] = rdtr<OB + s * 4096 + 2048>(v3);
      };
      issue_v(IC<0>{});
      WAITL8(8, kr[0], kr[1], kr[2], kr[3], qr[0], qr[1], qr[2], qr[3]);
#pragma unroll
      for (int ks = 0; ks < 4; ++ks) sr = MFMA16(kr[ks], qr[ks], sr);
      SBAR();
      SBAR();
      issue_v(IC<1>{});
      WAITL8(8, vl[0][0], vh[0][0], vl[0][1], vh[0][1], vl[0][2], vh[0][2], vl[0][3], vh[0][3]);
      acc[0] = MFMA32(cat8(vl[0][0], vh[0][0]), pf[0], acc[0]);
      float tm1 = fmaxf(fmaxf(s1[0], s1[1]), s1[2]);
#pragma unroll
      for (int r = 3; r < 9; r += 2) tm1 = fmaxf(fmaxf(tm1, s1[r]), s1[r + 1]);
      SBAR();
      acc[1] = MFMA32(cat8(vl[0][1], vh[0][1]), pf[0], acc[1]);
#pragma unroll
      for (int r = 9; r < 15; r += 2) tm1 = fmaxf(fmaxf(tm1, s1[r]), s1[r + 1]);
      tm1 = fmaxf(tm1, s1[15]);
      tm1 = xmax32(tm1);
      float mnb = fmaxf(m1, tm1);
      float alphab = __builtin_amdgcn_exp2f((m1 - mnb) * CS);
      m1 = mnb;
      mc = -mnb * CS;
      ps = 0.f;
      SBAR();
      acc[2] = MFMA32(cat8(vl[0][2], vh[0][2]), pf[0], acc[2]);
#pragma unroll
      for (int j = 0; j < 4; ++j) { e[j] = __builtin_amdgcn_exp2f(fmaf(s1[j], CS, mc)); ps += e[j]; }
      SBAR();
      acc[3] = MFMA32(cat8(vl[0][3], vh[0][3]), pf[0], acc[3]);
#pragma unroll
      for (int j = 4; j < 8; ++j) { e[j] = __builtin_amdgcn_exp2f(fmaf(s1[j], CS, mc)); ps += e[j]; }
      { u32x4 a = {cvtpk(e[0], e[1]), cvtpk(e[2], e[3]), cvtpk(e[4], e[5]), cvtpk(e[6], e[7])}; pf[2] = __builtin_bit_cast(bf16x8, a); }
      SBAR();
      issue_v(IC<2>{});
      WAITL8(8, vl[1][0], vh[1][0], vl[1][1], vh[1][1], vl[1][2], vh[1][2], vl[1][3], vh[1][3]);
      acc[0] = MFMA32(cat8(vl[1][0], vh[1][0]), pf[1], acc[0]);
#pragma unroll
      for (int j = 0; j < 4; ++j) { e[j] = __builtin_amdgcn_exp2f(fmaf(s1[8 + j], CS, mc)); ps += e[j]; }
      SBAR();
      acc[1] = MFMA32(cat8(vl[1][1], vh[1][1]), pf[1], acc[1]);
#pragma unroll
      for (int j = 4; j < 8; ++j) { e[j] = __builtin_amdgcn_exp2f(fmaf(s1[8 + j], CS, mc)); ps += e[j]; }
      { u32x4 a = {cvtpk(e[0], e[1]), cvtpk(e[2], e[3]), cvtpk(e[4], e[5]), cvtpk(e[6], e[7])}; pf[3] = __builtin_bit_cast(bf16x8, a); }
      l1 = l1 * alphab + ps;
      OPAQUE(l1);
      SBAR();
      acc[2] = MFMA32(cat8(vl[1][2], vh[1][2]), pf[1], acc[2]);
      float t2 = fmaxf(fmaxf(sr[0], sr[1]), fmaxf(sr[2], sr[3]));
      t2 = xmax16(t2);
      t2 = xmax32(t2);
      float mn2 = fmaxf(m2, t2);
      float alpha2 = __builtin_amdgcn_exp2f((m2 - mn2) * CS);
      m2 = mn2;
      SBAR();
      acc[3] = MFMA32(cat8(vl[1][3], vh[1][3]), pf[1], acc[3]);
      const float mc2 = -mn2 * CS;
      float e0 = __builtin_amdgcn_exp2f(fmaf(sr[0], CS, mc2));
      float e1 = __builtin_amdgcn_exp2f(fmaf(sr[1], CS, mc2));
      float e2 = __builtin_amdgcn_exp2f(fmaf(sr[2], CS, mc2));
      float e3 = __builtin_amdgcn_exp2f(fmaf(sr[3], CS, mc2));
      l2 = l2 * alpha2 + (e0 + e1) + (e2 + e3);
      OPAQUE(l2);
      u32x2 p2u = {cvtpk(e0, e1), cvtpk(e2, e3)};
      s16x4 p2 = __builtin_bit_cast(s16x4, p2u);
      SBAR();
      if (!__all(alphab == 1.f && alpha2 == 1.f)) {
#pragma unroll
        for (int dt = 0; dt < 4; ++dt)
#pragma unroll
          for (int r = 0; r < 16; ++r) acc[dt][r] *= alphab;
#pragma unroll
        for (int dd = 0; dd < 8; ++dd)
#pragma unroll
          for (int r = 0; r < 4; ++r) acc2[dd][r] *= alpha2;
      }
      issue_v(IC<3>{});
      WAITL8(8, vl[0][0], vh[0][0], vl[0][1], vh[0][1], vl[0][2], vh[0][2], vl[0][3], vh[0][3]);
#pragma unroll
      for (int dt = 0; dt < 4; ++dt) acc[dt] = MFMA32(cat8(vl[0][dt], vh[0][dt]), pf[2], acc[dt]);
      SBAR();
      s16x4 vr[8];
#pragma unroll
      for (int dd = 0; dd < 8; ++dd) vr[dd] = rdtr<OB>(va2_ ^ (dd << 5));
      WAITL8(8, vl[1][0], vh[1][0], vl[1][1], vh[1][1], vl[1][2], vh[1][2], vl[1][3], vh[1][3]);
#pragma unroll
      for (int dt = 0; dt < 4; ++dt) acc[dt] = MFMA32(cat8(vl[1][dt], vh[1][dt]), pf[3], acc[dt]);
      WAITL8(0, vr[0], vr[1], vr[2], vr[3], vr[4], vr[5], vr[6], vr[7]);
#pragma unroll
      for (int dd = 0; dd < 8; ++dd) acc2[dd] = MFMA16K16(vr[dd], p2, acc2[dd]);
    };
    for (int t = 0; t < 8; t += 2) {
      body(IC<0>{}, t);
      body(IC<1>{}, t + 1);
    }
  }

  int tz = threadIdx.x;
  OPAQUE(tz);
  asm volatile("s_waitcnt lgkmcnt(0)" ::: "memory");
  RAW_BAR();
  {
    const int l15 = tz & 15, g = (tz >> 4) & 3;
    float lt2 = xsum32(xsum16(l2));
    float* pw = (float*)(smem + ((w & 1) ? A_OFF_B + A_BUFB : A_OFF_A + A_BUFA) + (w >> 1) * 8192);
#pragma unroll
    for (int dd = 0; dd < 8; ++dd) *(f32x4*)(pw + l15 * 128 + 16 * dd + 4 * g) = acc2[dd];
    float* st = (float*)(smem + A_OFF_Q2);
    if (g == 0) { st[w * 32 + l15] = m2; st[w * 32 + 16 + l15] = lt2; }
  }
  asm volatile("s_waitcnt lgkmcnt(0)" ::: "memory");
  RAW_BAR();
  {
    const int q = tz >> 4, d0 = (tz & 15) * 8;
    const float* st = (const float*)(smem + A_OFF_Q2);
    float mm = fmaxf(fmaxf(st[q], st[32 + q]), fmaxf(st[64 + q], st[96 + q]));
    float o[8] = {0, 0, 0, 0, 0, 0, 0, 0};
    float ls = 0.f;
#pragma unroll
    for (int ww = 0; ww < 4; ++ww) {
      float sc = __builtin_amdgcn_exp2f((st[ww * 32 + q] - mm) * CS);
      ls += st[ww * 32 + 16 + q] * sc;
      const float* pw = (const float*)(smem + ((ww & 1) ? A_OFF_B + A_BUFB : A_OFF_A + A_BUFA) + (ww >> 1) * 8192) + q * 128 + d0;
      f32x4 a = *(const f32x4*)pw, c = *(const f32x4*)(pw + 4);
#pragma unroll
      for (int j = 0; j < 4; ++j) { o[j] += a[j] * sc; o[4 + j] += c[j] * sc; }
    }
    float inv = 1.f / ls;
    u32x4 pk = {cvtpk(o[0] * inv, o[1] * inv), cvtpk(o[2] * inv, o[3] * inv),
                cvtpk(o[4] * inv, o[5] * inv), cvtpk(o[6] * inv, o[7] * inv)};
    *(u32x4*)(og + (size_t)(b * SQ + 128 + q) * HD + h * DH + d0) = pk;
  }
  asm volatile("s_waitcnt lgkmcnt(0)" ::: "memory");
  RAW_BAR();
  {
    const int l31 = tz & 31, hh = (tz >> 5) & 1;
    float lt = xsum32(l1);
    float inv = 1.f / lt;
    char* ob = smem + (w < 2 ? A_OFF_A + A_BUFA : A_OFF_B + A_BUFB) + ((w & 1) * 32 + l31) * 256 + 8 * hh;
#pragma unroll
    for (int dt = 0; dt < 4; ++dt)
#pragma unroll
      for (int i = 0; i < 4; ++i) {
        u32x2 pk = {cvtpk(acc[dt][4 * i] * inv, acc[dt][4 * i + 1] * inv),
                    cvtpk(acc[dt][4 * i + 2] * inv, acc[dt][4 * i + 3] * inv)};
        *(u32x2*)(ob + (((4 * dt + i) ^ (l31 & 15)) << 4)) = pk;
      }
    if (tz == 0 && nidx >= 0) *slot = atomicAdd(counter, 1u) + 512u;
  }
  asm volatile("s_waitcnt lgkmcnt(0)" ::: "memory");
  RAW_BAR();
  {
    const int c = tz & 15, r0 = tz >> 4;
    short* orow = og + (size_t)(b * SQ + r0) * HD + h * DH + c * 8;
#pragma unroll
    for (int j = 0; j < 8; ++j) {
      const int r = r0 + 16 * j;
      const char* src = smem + (j < 4 ? A_OFF_A + A_BUFA + r * 256 : A_OFF_B + A_BUFB + (r - 64) * 256) + ((c ^ r0) << 4);
      bf16x8 v = *(const bf16x8*)src;
      *(bf16x8*)(orow + (size_t)(16 * j) * HD) = v;
    }
  }
  if (nidx < 0) break;
  pidx = nidx;
  }
}

constexpr int B_NST = 4;
constexpr int B_BUFA = 20480;
constexpr int B_BUFB = 8192;
constexpr int B_STRIDE = B_BUFA + B_BUFB;
constexpr int B_LDS = B_NST * B_STRIDE;

__global__ __launch_bounds__(256, 1) void oproj_kernel(const short* __restrict__ og,
                                                        const short* __restrict__ wo,
                                                        short* __restrict__ out) {
  extern __shared__ __attribute__((aligned(16))) char smem[];
  const int tid = threadIdx.x, lane = tid & 63;
  const int w = __builtin_amdgcn_readfirstlane(tid >> 6);
  const int xg = blockIdx.x & 7, idx = blockIdx.x >> 3;
  const int b = xg * 4 + (idx >> 3), nt0 = idx & 7;
  const char* ob = (const char*)(og + (size_t)b * SQ * HD);
  const char* wob = (const char*)(wo + nt0 * 64);
  const int l31 = lane & 31, hh = lane >> 5, l15 = lane & 15, g = lane >> 4;
  const int qp = l15 >> 2, p4 = lane & 3, blk = (lane >> 4) & 1;
  unsigned osrc[5], wsrc[2];
#pragma unroll
  for (int ii = 0; ii < 5; ++ii) {
    int r = 8 * (w * 5 + ii) + (lane >> 3), cp = lane & 7;
    int rs = r < SQ ? r : SQ - 1;
    osrc[ii] = rs * (HD * 2) + ((cp ^ ((r >> 1) & 7)) << 4);
  }
#pragma unroll
  for (int ii = 0; ii < 2; ++ii) {
    int r = 8 * (w * 2 + ii) + (lane >> 3), cp = lane & 7;
    wsrc[ii] = r * (DM * 2) + ((cp ^ (((r >> 1) & 1) << 2)) << 4);
  }
  auto stage = [&](int t, int buf) {
    char* la = smem + buf * B_STRIDE + w * 5120;
    char* lb = smem + buf * B_STRIDE + B_BUFA + w * 2048;
    const char* ot = ob + t * 128;
    const char* wt = wob + (size_t)t * (64 * DM * 2);
#pragma unroll
    for (int ii = 0; ii < 5; ++ii) glds16(ot + osrc[ii], la + ii * 1024);
#pragma unroll
    for (int ii = 0; ii < 2; ++ii) glds16(wt + wsrc[ii], lb + ii * 1024);
  };

  f32x16 acc[2] = {f32x16{}, f32x16{}};
  f32x4 acc2 = f32x4{};
  constexpr int NT = HD / 64;
  const int sb = (int)(uintptr_t)smem;
  const int xa = sb + (32 * w + l31) * 128 + ((hh ^ ((l31 >> 1) & 7)) << 4);
  const int wa = sb + B_BUFA + (8 * hh + qp) * 128 + (((2 * blk + (p4 >> 1)) ^ ((qp >> 1) << 2)) << 4) + 8 * (p4 & 1);
  const int xa2 = sb + (128 + l15) * 128 + ((g ^ ((l15 >> 1) & 7)) << 4);
  const int wa2 = sb + B_BUFA + (8 * g + qp) * 128 + (((2 * w + (p4 >> 1)) ^ ((qp >> 1) << 2)) << 4) + 8 * (p4 & 1);
  stage(0, 0);
  stage(1, 1);
  stage(2, 2);
  auto body = [&](auto BC, int t) {
    constexpr int buf = decltype(BC)::value;
    constexpr int OB = (buf & 1) * B_STRIDE;
    const int hi2 = (buf >> 1) * 2 * B_STRIDE;
    if (t < NT - 2) asm volatile("s_waitcnt vmcnt(14)" ::: "memory");
    else if (t == NT - 2) asm volatile("s_waitcnt vmcnt(7)" ::: "memory");
    else asm volatile("s_waitcnt vmcnt(0)" ::: "memory");
    RAW_BAR();
    if (t + 3 < NT) stage(t + 3, (buf + 3) & 3);
    const int xa_ = xa + hi2, w0 = wa + hi2, w1 = (wa ^ 64) + hi2, xa2_ = xa2 + hi2, wa2_ = wa2 + hi2;
    bf16x8 xf[4], xr[2];
    s16x4 wl[4][2], wh[4][2], rl[2], rh[2];
    sfor<0, 4>([&](auto SC) {
      constexpr int s = decltype(SC)::value;
      xf[s] = rd128<OB>(xa_ ^ (s << 5));
      wl[s][0] = rdtr<OB + s * 2048>(w0); wh[s][0] = rdtr<OB + s * 2048 + 512>(w0);
      wl[s][1] = rdtr<OB + s * 2048>(w1); wh[s][1] = rdtr<OB + s * 2048 + 512>(w1);
      if constexpr (s >= 1) {
        asm volatile("s_waitcnt lgkmcnt(5)" : "+v"(xf[s - 1]), "+v"(wl[s - 1][0]), "+v"(wh[s - 1][0]), "+v"(wl[s - 1][1]), "+v"(wh[s - 1][1]) :: "memory");
        acc[0] = MFMA32(cat8(wl[s - 1][0], wh[s - 1][0]), xf[s - 1], acc[0]);
        acc[1] = MFMA32(cat8(wl[s - 1][1], wh[s - 1][1]), xf[s - 1], acc[1]);
      }
    });
    xr[0] = rd128<OB>(xa2_); rl[0] = rdtr<OB>(wa2_); rh[0] = rdtr<OB + 512>(wa2_);
    xr[1] = rd128<OB>(xa2_ ^ 64); rl[1] = rdtr<OB + 4096>(wa2_); rh[1] = rdtr<OB + 4096 + 512>(wa2_);
    asm volatile("s_waitcnt lgkmcnt(6)" : "+v"(xf[3]), "+v"(wl[3][0]), "+v"(wh[3][0]), "+v"(wl[3][1]), "+v"(wh[3][1]) :: "memory");
    acc[0] = MFMA32(cat8(wl[3][0], wh[3][0]), xf[3], acc[0]);
    acc[1] = MFMA32(cat8(wl[3][1], wh[3][1]), xf[3], acc[1]);
    asm volatile("s_waitcnt lgkmcnt(0)" : "+v"(xr[0]), "+v"(rl[0]), "+v"(rh[0]), "+v"(xr[1]), "+v"(rl[1]), "+v"(rh[1]) :: "memory");
    acc2 = MFMA16(cat8(rl[0], rh[0]), xr[0], acc2);
    acc2 = MFMA16(cat8(rl[1], rh[1]), xr[1], acc2);
  };
  for (int t = 0; t < NT; t += 4) {
    body(IC<0>{}, t);
    body(IC<1>{}, t + 1);
    body(IC<2>{}, t + 2);
    body(IC<3>{}, t + 3);
  }
  {
    short* orow = out + (size_t)(b * SQ + 32 * w + l31) * DM + nt0 * 64 + 4 * hh;
#pragma unroll
    for (int nt = 0; nt < 2; ++nt)
#pragma unroll
      for (int i = 0; i < 4; ++i) {
        u32x2 pk = {cvtpk(acc[nt][4 * i], acc[nt][4 * i + 1]), cvtpk(acc[nt][4 * i + 2], acc[nt][4 * i + 3])};
        *(u32x2*)(orow + 32 * nt + 8 * i) = pk;
      }
    u32x2 pk = {cvtpk(acc2[0], acc2[1]), cvtpk(acc2[2], acc2[3])};
    *(u32x2*)(out + (size_t)(b * SQ + 128 + l15) * DM + nt0 * 64 + 16 * w + 4 * g) = pk;
  }
}

extern "C" void kernel_launch(void* const* d_in, const int* in_sizes, int n_in, void* d_out,
                              int out_size, void* d_ws, size_t ws_size, hipStream_t stream) {
  const short* x = (const short*)d_in[0];
  const short* kc = (const short*)d_in[1];
  const short* vc = (const short*)d_in[2];
  const short* wq = (const short*)d_in[3];
  const short* wo = (const short*)d_in[4];
  short* og = (short*)d_ws;
  short* out = (short*)d_out;
  unsigned* counter = (unsigned*)(og + (size_t)MROWS * HD);
  (void)hipFuncSetAttribute((const void*)attn_kernel, hipFuncAttributeMaxDynamicSharedMemorySize, A_LDS);
  (void)hipFuncSetAttribute((const void*)oproj_kernel, hipFuncAttributeMaxDynamicSharedMemorySize, B_LDS);
  (void)hipMemsetAsync(counter, 0, 16, stream);
  attn_kernel<<<512, 256, A_LDS, stream>>>(x, wq, kc, vc, og, counter);
  oproj_kernel<<<256, 256, B_LDS, stream>>>(og, wo, out);
}
```

```cpp
#include <hip/hip_runtime.h>
#include <hip/hip_bf16.h>

typedef __attribute__((ext_vector_type(8))) short bf16x8;
typedef __attribute__((ext_vector_type(4))) short s16x4;
typedef __attribute__((ext_vector_type(16))) float f32x16;
typedef __attribute__((ext_vector_type(4))) float f32x4;
typedef __attribute__((ext_vector_type(4))) unsigned u32x4;
typedef __attribute__((ext_vector_type(2))) unsigned u32x2;

constexpr int NB = 32, SQ = 144, DM = 512, NH = 32, SK = 512, DH = 128;
constexpr int HD = NH * DH;
constexpr int MROWS = NB * SQ;

#define MFMA32(a, b, c) __builtin_amdgcn_mfma_f32_32x32x16_bf16((a), (b), (c), 0, 0, 0)
#define MFMA16(a, b, c) __builtin_amdgcn_mfma_f32_16x16x32_bf16((a), (b), (c), 0, 0, 0)
#define MFMA16K16(a, b, c) __builtin_amdgcn_mfma_f32_16x16x16bf16_1k((a), (b), (c), 0, 0, 0)

__device__ __forceinline__ unsigned cvtpk(float lo, float hi) {
  unsigned r;
  asm volatile("v_cvt_pk_bf16_f32 %0, %1, %2" : "=v"(r) : "v"(lo), "v"(hi));
  return r;
}
__device__ __forceinline__ bf16x8 cat8(s16x4 lo, s16x4 hi) {
  return __builtin_shufflevector(lo, hi, 0, 1, 2, 3, 4, 5, 6, 7);
}
__device__ __forceinline__ void glds16(const void* g, char* l) {
  __builtin_amdgcn_global_load_lds((const unsigned*)g, (unsigned*)l, 16, 0, 0);
}
__device__ __forceinline__ float xmax32(float v) {
  auto rr = __builtin_amdgcn_permlane32_swap(__float_as_uint(v), __float_as_uint(v), false, false);
  return fmaxf(__uint_as_float(rr[0]), __uint_as_float(rr[1]));
}
__device__ __forceinline__ float xsum32(float v) {
  auto rr = __builtin_amdgcn_permlane32_swap(__float_as_uint(v), __float_as_uint(v), false, false);
  return __uint_as_float(rr[0]) + __uint_as_float(rr[1]);
}
__device__ __forceinline__ float xmax16(float v) {
  auto rr = __builtin_amdgcn_permlane16_swap(__float_as_uint(v), __float_as_uint(v), false, false);
  return fmaxf(__uint_as_float(rr[0]), __uint_as_float(rr[1]));
}
__device__ __forceinline__ float xsum16(float v) {
  auto rr = __builtin_amdgcn_permlane16_swap(__float_as_uint(v), __float_as_uint(v), false, false);
  return __uint_as_float(rr[0]) + __uint_as_float(rr[1]);
}
template <int OFF> __device__ __forceinline__ bf16x8 rd128(int a) {
  bf16x8 r;
  asm volatile("ds_read_b128 %0, %1 offset:%2" : "=v"(r) : "v"(a), "i"(OFF) : "memory");
  return r;
}
template <int OFF> __device__ __forceinline__ s16x4 rdtr(int a) {
  s16x4 r;
  asm volatile("ds_read_b64_tr_b16 %0, %1 offset:%2" : "=v"(r) : "v"(a), "i"(OFF) : "memory");
  return r;
}
#define WAITL2(N, a, b) asm volatile("s_waitcnt lgkmcnt(" #N ")" : "+v"(a), "+v"(b) :: "memory")
#define WAITL4(N, a, b, c, d) asm volatile("s_waitcnt lgkmcnt(" #N ")" : "+v"(a), "+v"(b), "+v"(c), "+v"(d) :: "memory")
#define WAITL8(N, a, b, c, d, e, f, g_, h_) asm volatile("s_waitcnt lgkmcnt(" #N ")" : "+v"(a), "+v"(b), "+v"(c), "+v"(d), "+v"(e), "+v"(f), "+v"(g_), "+v"(h_) :: "memory")
#define RAW_BAR() __builtin_amdgcn_s_barrier()
#define SBAR() __builtin_amdgcn_sched_barrier(0)
#define OPAQUE(v) asm volatile("" : "+v"(v))
template <int N> struct IC { static constexpr int value = N; };
template <int I, int N, class F> __device__ __forceinline__ void sfor(F&& f) {
  if constexpr (I < N) { f(IC<I>{}); sfor<I + 1, N>(f); }
}

constexpr int A_BUFA = 20480;
constexpr int A_BUFB = 16384;
constexpr int A_OFF_A = 0;
constexpr int A_OFF_B = 2 * A_BUFA;
constexpr int A_OFF_Q2 = A_OFF_B + 2 * A_BUFB;
constexpr int A_OFF_SLOT = A_OFF_Q2 + 4096;
constexpr int A_LDS = A_OFF_SLOT + 16;

__global__ __launch_bounds__(256, 2) void attn_kernel(
    const short* __restrict__ x, const short* __restrict__ wq, const short* __restrict__ kc,
    const short* __restrict__ vc, short* __restrict__ og, unsigned* __restrict__ counter) {
  extern __shared__ __attribute__((aligned(16))) char smem[];
  const int tid = threadIdx.x, lane = tid & 63;
  const int w = __builtin_amdgcn_readfirstlane(tid >> 6);
  const int l31 = lane & 31, hh = lane >> 5, l15 = lane & 15, g = lane >> 4;
  const int qp = l15 >> 2, p4 = lane & 3, blk = (lane >> 4) & 1;
  const int sb = (int)(uintptr_t)smem;
  volatile unsigned* slot = (volatile unsigned*)(smem + A_OFF_SLOT);

  const unsigned ksrc = (16 * w + g) * 256 + ((l15 ^ g) << 4);
  const unsigned vsrc = (16 * w + g) * 256 + ((l15 ^ (g << 2)) << 4);
  auto stage_kv = [&](int pi, int t, int buf, unsigned ko, unsigned vo) {
    const char* kb = (const char*)kc + (size_t)pi * (SK * DH * 2) + t * 16384;
    const char* vb = (const char*)vc + (size_t)pi * (SK * DH * 2) + t * 16384;
#pragma unroll
    for (int i = 0; i < 4; ++i)
      glds16(kb + ((ko ^ (i << 6)) + i * 1024), smem + A_OFF_A + buf * A_BUFA + w * 4096 + i * 1024);
#pragma unroll
    for (int i = 0; i < 4; ++i)
      glds16(vb + (vo + i * 1024), smem + A_OFF_B + buf * A_BUFB + w * 4096 + i * 1024);
  };
  auto stage_q = [&](int pi, int t, int buf) {
    int ln = lane;
    OPAQUE(ln);
    const char* xt = (const char*)(x + (size_t)(pi >> 5) * SQ * DM) + t * 128;
    const char* wt = (const char*)(wq + (pi & 31) * DH) + (size_t)t * (64 * HD * 2);
#pragma unroll
    for (int ii = 0; ii < 5; ++ii) {
      int r = 8 * (w * 5 + ii) + (ln >> 3), cp = ln & 7;
      int rs = r < SQ ? r : SQ - 1;
      unsigned so = rs * 1024 + ((cp ^ ((r >> 1) & 7)) << 4);
      glds16(xt + so, smem + A_OFF_A + buf * A_BUFA + w * 5120 + ii * 1024);
    }
    const unsigned wo0 = (16 * w + (ln >> 4)) * (HD * 2) + (((ln & 15) ^ ((ln >> 4) << 2)) << 4);
#pragma unroll
    for (int ii = 0; ii < 4; ++ii)
      glds16(wt + (wo0 + ii * 4 * HD * 2), smem + A_OFF_B + buf * A_BUFB + w * 4096 + ii * 1024);
  };
  int pidx = (blockIdx.x & 7) * 128 + (blockIdx.x >> 3);
#define STAG 0
  if (STAG > 0 && blockIdx.x >= 256) {
    unsigned long long t0 = __builtin_amdgcn_s_memtime();
    while (__builtin_amdgcn_s_memtime() - t0 < STAG) __builtin_amdgcn_s_sleep(8);
  }
  stage_q(pidx, 0, 0);
  if (tid == 0) *slot = blockIdx.x + 512u;
  constexpr float CS = 0.088388347648318440f * 1.4426950408889634f;
  constexpr float THRS = 8.f / CS;

  for (;;) {
  const int b = pidx >> 5, h = pidx & 31;
  f32x16 acc[4];
#pragma unroll
  for (int i = 0; i < 4; ++i) acc[i] = f32x16{};
  f32x4 acc2[8];
#pragma unroll
  for (int i = 0; i < 8; ++i) acc2[i] = f32x4{};
  {
    int lq = lane;
    OPAQUE(lq);
    const int l31 = lq & 31, hh = lq >> 5, l15 = lq & 15, g = lq >> 4;
    const int qp = l15 >> 2, p4 = lq & 3, blk = (lq >> 4) & 1;
    const int xa = sb + A_OFF_A + (32 * w + l31) * 128 + ((hh ^ ((l31 >> 1) & 7)) << 4);
    const int wa = sb + A_OFF_B + (8 * hh + qp) * 256 + (((2 * blk + (p4 >> 1)) ^ (qp << 2)) << 4) + 8 * (p4 & 1);
    const int xa2 = sb + A_OFF_A + (128 + l15) * 128 + ((g ^ ((l15 >> 1) & 7)) << 4);
    const int wa2 = sb + A_OFF_B + (8 * g + qp) * 256 + (((4 * w + (p4 >> 1)) ^ (qp << 2)) << 4) + 8 * (p4 & 1);
    auto body = [&](auto BC, int t) {
      constexpr int buf = decltype(BC)::value;
      constexpr int OA = buf * A_BUFA, OB = buf * A_BUFB;
      asm volatile("s_waitcnt vmcnt(0) lgkmcnt(0)" ::: "memory");
      RAW_BAR();
      if (t < 7) stage_q(pidx, t + 1, buf ^ 1);
      else stage_kv(pidx, 0, buf ^ 1, ksrc, vsrc);
      SBAR();
      const int w0 = wa, w1 = wa ^ 64, w2 = wa ^ 128, w3 = wa ^ 192;
      bf16x8 xf[2];
      s16x4 wl[2][4], wh[2][4];
      bf16x8 xr[2];
      s16x4 rl[2][2], rh[2][2];
      auto issue = [&](auto SC) {
        constexpr int s = decltype(SC)::value, u = s & 1;
        xf[u] = rd128<OA>(xa ^ (s << 5));
        wl[u][0] = rdtr<OB + s * 4096>(w0); wh[u][0] = rdtr<OB + s * 4096 + 1024>(w0);
        wl[u][1] = rdtr<OB + s * 4096>(w1); wh[u][1] = rdtr<OB + s * 4096 + 1024>(w1);
        wl[u][2] = rdtr<OB + s * 4096>(w2); wh[u][2] = rdtr<OB + s * 4096 + 1024>(w2);
        wl[u][3] = rdtr<OB + s * 4096>(w3); wh[u][3] = rdtr<OB + s * 4096 + 1024>(w3);
      };
      issue(IC<0>{});
      sfor<0, 4>([&](auto SC) {
        constexpr int s = decltype(SC)::value, u = s & 1;
        if constexpr (s < 3) {
          issue(IC<s + 1>{});
          asm volatile("s_waitcnt lgkmcnt(9)" : "+v"(xf[u]), "+v"(wl[u][0]), "+v"(wh[u][0]), "+v"(wl[u][1]), "+v"(wh[u][1]),
                       "+v"(wl[u][2]), "+v"(wh[u][2]), "+v"(wl[u][3]), "+v"(wh[u][3]) :: "memory");
        } else {
          xr[0] = rd128<OA>(xa2); xr[1] = rd128<OA>(xa2 ^ 64);
          rl[0][0] = rdtr<OB>(wa2); rh[0][0] = rdtr<OB + 1024>(wa2);
          rl[0][1] = rdtr<OB>(wa2 ^ 32); rh[0][1] = rdtr<OB + 1024>(wa2 ^ 32);
          rl[1][0] = rdtr<OB + 8192>(wa2); rh[1][0] = rdtr<OB + 8192 + 1024>(wa2);
          rl[1][1] = rdtr<OB + 8192>(wa2 ^ 32); rh[1][1] = rdtr<OB + 8192 + 1024>(wa2 ^ 32);
          asm volatile("s_waitcnt lgkmcnt(10)" : "+v"(xf[u]), "+v"(wl[u][0]), "+v"(wh[u][0]), "+v"(wl[u][1]), "+v"(wh[u][1]),
                       "+v"(wl[u][2]), "+v"(wh[u][2]), "+v"(wl[u][3]), "+v"(wh[u][3]) :: "memory");
        }
#pragma unroll
        for (int dt = 0; dt < 4; ++dt) acc[dt] = MFMA32(cat8(wl[u][dt], wh[u][dt]), xf[u], acc[dt]);
        SBAR();
      });
      asm volatile("s_waitcnt lgkmcnt(0)" : "+v"(xr[0]), "+v"(xr[1]), "+v"(rl[0][0]), "+v"(rh[0][0]), "+v"(rl[0][1]), "+v"(rh[0][1]),
                   "+v"(rl[1][0]), "+v"(rh[1][0]), "+v"(rl[1][1]), "+v"(rh[1][1]) :: "memory");
#pragma unroll
      for (int s2 = 0; s2 < 2; ++s2)
#pragma unroll
        for (int e = 0; e < 2; ++e) acc2[e] = MFMA16(cat8(rl[s2][e], rh[s2][e]), xr[s2], acc2[e]);
    };
    for (int t = 0; t < 8; t += 2) {
      body(IC<0>{}, t);
      body(IC<1>{}, t + 1);
    }
  }
  bf16x8 qf[8];
#pragma unroll
  for (int dt = 0; dt < 4; ++dt) {
#pragma unroll
    for (int s = 0; s < 2; ++s) {
      unsigned a0 = cvtpk(acc[dt][8 * s + 0], acc[dt][8 * s + 1]);
      unsigned a1 = cvtpk(acc[dt][8 * s + 2], acc[dt][8 * s + 3]);
      unsigned b0 = cvtpk(acc[dt][8 * s + 4], acc[dt][8 * s + 5]);
      unsigned b1 = cvtpk(acc[dt][8 * s + 6], acc[dt][8 * s + 7]);
      auto r0 = __builtin_amdgcn_permlane32_swap(a0, b0, false, false);
      auto r1 = __builtin_amdgcn_permlane32_swap(a1, b1, false, false);
      u32x4 wv = {r0[0], r1[0], r0[1], r1[1]};
      qf[2 * dt + s] = __builtin_bit_cast(bf16x8, wv);
    }
  }
  {
#pragma unroll
    for (int e = 0; e < 2; ++e) {
      int dd = 2 * w + e;
      u32x2 pk = {cvtpk(acc2[e][0], acc2[e][1]), cvtpk(acc2[e][2], acc2[e][3])};
      int ch = (2 * dd + (g >> 1)) ^ l15;
      asm volatile("ds_write_b64 %0, %1" :: "v"(sb + A_OFF_Q2 + l15 * 256 + (ch << 4) + 8 * (g & 1)), "v"(pk) : "memory");
    }
  }
#pragma unroll
  for (int i = 0; i < 4; ++i) acc[i] = f32x16{};
#pragma unroll
  for (int i = 0; i < 8; ++i) acc2[i] = f32x4{};
  int nidx = 0;
  float m1 = -1e30f, l1 = 0.f, m2 = -1e30f, l2 = 0.f;
  {
    int ka = sb + A_OFF_A + l31 * 256 + ((hh ^ l15) << 4);
    int ka2 = sb + A_OFF_A + (16 * w + l15) * 256 + ((g ^ l15) << 4);
    int q2a = sb + A_OFF_Q2 + l15 * 256 + ((g ^ l15) << 4);
    int va = sb + A_OFF_B + (4 * hh + qp) * 256 + (((2 * blk + (p4 >> 1)) ^ (qp << 2)) << 4) + 8 * (p4 & 1);
    int va2 = sb + A_OFF_B + (16 * w + 4 * g + qp) * 256 + (((p4 >> 1) ^ (qp << 2)) << 4) + 8 * (p4 & 1);
    auto body = [&](auto BC, int t) {
      constexpr int buf = decltype(BC)::value;
      constexpr int OA = buf * A_BUFA, OB = buf * A_BUFB;
      asm volatile("s_waitcnt vmcnt(0) lgkmcnt(0)" ::: "memory");
      RAW_BAR();
      int ka_ = ka, ka2_ = ka2, q2a_ = q2a, va_ = va, va2_ = va2;
      unsigned ko = ksrc, vo = vsrc;
      OPAQUE(ka_); OPAQUE(ka2_); OPAQUE(q2a_); OPAQUE(va_); OPAQUE(va2_); OPAQUE(ko); OPAQUE(vo);
      if (t < 7) {
        stage_kv(pidx, t + 1, buf ^ 1, ko, vo);
      } else {
        const unsigned it = __builtin_amdgcn_readfirstlane(*slot);
        nidx = it < 1024u ? (int)((it & 7) * 128 + (it >> 3)) : -1;
        if (nidx >= 0) stage_q(nidx, 0, 0);
      }
      SBAR();
      f32x16 s0 = f32x16{}, s1 = f32x16{};
      f32x4 sr = f32x4{};
      bf16x8 kA[4], kB[4];
#pragma unroll
      for (int i = 0; i < 4; ++i) kA[i] = rd128<OA>(ka_ ^ (i << 5));
#pragma unroll
      for (int i = 0; i < 4; ++i) kB[i] = rd128<OA>(ka_ ^ ((4 + i) << 5));
      WAITL4(4, kA[0], kA[1], kA[2], kA[3]);
      s0 = MFMA32(kA[0], qf[0], s0);
      s0 = MFMA32(kA[1], qf[1], s0);
      SBAR();
      s0 = MFMA32(kA[2], qf[2], s0);
      s0 = MFMA32(kA[3], qf[3], s0);
      SBAR();
#pragma unroll
      for (int i = 0; i < 4; ++i) kA[i] = rd128<OA + 8192>(ka_ ^ (i << 5));
      WAITL4(4, kB[0], kB[1], kB[2], kB[3]);
      s0 = MFMA32(kB[0], qf[4], s0);
      s0 = MFMA32(kB[1], qf[5], s0);
      SBAR();
      s0 = MFMA32(kB[2], qf[6], s0);
      s0 = MFMA32(kB[3], qf[7], s0);
      SBAR();
#pragma unroll
      for (int i = 0; i < 4; ++i) kB[i] = rd128<OA + 8192>(ka_ ^ ((4 + i) << 5));
      WAITL4(4, kA[0], kA[1], kA[2], kA[3]);
      s1 = MFMA32(kA[0], qf[0], s1);
      SBAR();
      s1 = MFMA32(kA[1], qf[1], s1);
      float tmax = fmaxf(fmaxf(s0[0], s0[1]), s0[2]);
#pragma unroll
      for (int r = 3; r < 9; r += 2) tmax = fmaxf(fmaxf(tmax, s0[r]), s0[r + 1]);
      SBAR();
      s1 = MFMA32(kA[2], qf[2], s1);
#pragma unroll
      for (int r = 9; r < 15; r += 2) tmax = fmaxf(fmaxf(tmax, s0[r]), s0[r + 1]);
      tmax = fmaxf(tmax, s0[15]);
      tmax = xmax32(tmax);
      SBAR();
      s1 = MFMA32(kA[3], qf[3], s1);
      SBAR();
      if (!__all(tmax - m1 <= THRS)) {
        float mn0 = fmaxf(m1, tmax);
        float alpha = __builtin_amdgcn_exp2f((m1 - mn0) * CS);
        m1 = mn0;
        l1 *= alpha;
#pragma unroll
        for (int dt = 0; dt < 4; ++dt)
#pragma unroll
          for (int r = 0; r < 16; ++r) acc[dt][r] *= alpha;
      }
      const float mn = m1;
      bf16x8 kr[4], qr[4];
#pragma unroll
      for (int ks = 0; ks < 4; ++ks) { kr[ks] = rd128<OA>(ka2_ ^ (ks << 6)); qr[ks] = rd128<0>(q2a_ ^ (ks << 6)); }
      WAITL4(8, kB[0], kB[1], kB[2], kB[3]);
      bf16x8 pf[4];
      float mc = -mn * CS;
      float ps = 0.f;
      float e[8];
      s1 = MFMA32(kB[0], qf[4], s1);
#pragma unroll
      for (int j = 0; j < 4; ++j) { e[j] = __builtin_amdgcn_exp2f(fmaf(s0[j], CS, mc)); ps += e[j]; }
      SBAR();
      s1 = MFMA32(kB[1], qf[5], s1);
#pragma unroll
      for (int j = 4; j < 8; ++j) { e[j] = __builtin_amdgcn_exp2f(fmaf(s0[j], CS, mc)); ps += e[j]; }
      { u32x4 a = {cvtpk(e[0], e[1]), cvtpk(e[2], e[3]), cvtpk(e[4], e[5]), cvtpk(e[6], e[7])}; pf[0] = __builtin_bit_cast(bf16x8, a); }
      SBAR();
      s1 = MFMA32(kB[2], qf[6], s1);
#pragma unroll
      for (int j = 0; j < 4; ++j) { e[j] = __builtin_amdgcn_exp2f(fmaf(s0[8 + j], CS, mc)); ps += e[j]; }
      SBAR();
      s1 = MFMA32(kB[3], qf[7], s1);
#pragma unroll
      for (int j = 4; j < 8; ++j) { e[j] = __builtin_amdgcn_exp2f(fmaf(s0[8 + j], CS, mc)); ps += e[j]; }
      { u32x4 a = {cvtpk(e[0], e[1]), cvtpk(e[2], e[3]), cvtpk(e[4], e[5]), cvtpk(e[6], e[7])}; pf[1] = __builtin_bit_cast(bf16x8, a); }
      l1 += ps;
      OPAQUE(l1);
      SBAR();
      SBAR();
      const int v0 = va_, v1 = va_ ^ 64, v2 = va_ ^ 128, v3 = va_ ^ 192;
      s16x4 vl[2][4], vh[2][4];
      auto issue_v = [&](auto SC) {
        constexpr int s = decltype(SC)::value, u = s & 1;
        vl[u][0] = rdtr<OB + s * 4096>(v0); vh[u][0] = rdtr<OB + s * 4096 + 2048>(v0);
        vl[u][1] = rdtr<OB + s * 4096>(v1); vh[u][1] = rdtr<OB + s * 4096 + 2048>(v1);
        vl[u][2] = rdtr<OB + s * 4096>(v2); vh[u][2] = rdtr<OB + s * 4096 + 2048>(v2);
        vl[u][3] = rdtr<OB + s * 4096>(v3); vh[u][3] = rdtr<OB + s * 4096 + 2048>(v3);
      };
      issue_v(IC<0>{});
      WAITL8(8, kr[0], kr[1], kr[2], kr[3], qr[0], qr[1], qr[2], qr[3]);
#pragma unroll
      for (int ks = 0; ks < 4; ++ks) sr = MFMA16(kr[ks], qr[ks], sr);
      SBAR();
      SBAR();
      issue_v(IC<1>{});
      WAITL8(8, vl[0][0], vh[0][0], vl[0][1], vh[0][1], vl[0][2], vh[0][2], vl[0][3], vh[0][3]);
      acc[0] = MFMA32(cat8(vl[0][0], vh[0][0]), pf[0], acc[0]);
      float tm1 = fmaxf(fmaxf(s1[0], s1[1]), s1[2]);
#pragma unroll
      for (int r = 3; r < 9; r += 2) tm1 = fmaxf(fmaxf(tm1, s1[r]), s1[r + 1]);
      SBAR();
      acc[1] = MFMA32(cat8(vl[0][1], vh[0][1]), pf[0], acc[1]);
#pragma unroll
      for (int r = 9; r < 15; r += 2) tm1 = fmaxf(fmaxf(tm1, s1[r]), s1[r + 1]);
      tm1 = fmaxf(tm1, s1[15]);
      tm1 = xmax32(tm1);
      const bool keepb = __all(tm1 - m1 <= THRS);
      float mnb = keepb ? m1 : fmaxf(m1, tm1);
      float alphab = keepb ? 1.f : __builtin_amdgcn_exp2f((m1 - mnb) * CS);
      m1 = mnb;
      mc = -mnb * CS;
      ps = 0.f;
      SBAR();
      acc[2] = MFMA32(cat8(vl[0][2], vh[0][2]), pf[0], acc[2]);
#pragma unroll
      for (int j = 0; j < 4; ++j) { e[j] = __builtin_amdgcn_exp2f(fmaf(s1[j], CS, mc)); ps += e[j]; }
      SBAR();
      acc[3] = MFMA32(cat8(vl[0][3], vh[0][3]), pf[0], acc[3]);
#pragma unroll
      for (int j = 4; j < 8; ++j) { e[j] = __builtin_amdgcn_exp2f(fmaf(s1[j], CS, mc)); ps += e[j]; }
      { u32x4 a = {cvtpk(e[0], e[1]), cvtpk(e[2], e[3]), cvtpk(e[4], e[5]), cvtpk(e[6], e[7])}; pf[2] = __builtin_bit_cast(bf16x8, a); }
      SBAR();
      issue_v(IC<2>{});
      WAITL8(8, vl[1][0], vh[1][0], vl[1][1], vh[1][1], vl[1][2], vh[1][2], vl[1][3], vh[1][3]);
      acc[0] = MFMA32(cat8(vl[1][0], vh[1][0]), pf[1], acc[0]);
#pragma unroll
      for (int j = 0; j < 4; ++j) { e[j] = __builtin_amdgcn_exp2f(fmaf(s1[8 + j], CS, mc)); ps += e[j]; }
      SBAR();
      acc[1] = MFMA32(cat8(vl[1][1], vh[1][1]), pf[1], acc[1]);
#pragma unroll
      for (int j = 4; j < 8; ++j) { e[j] = __builtin_amdgcn_exp2f(fmaf(s1[8 + j], CS, mc)); ps += e[j]; }
      { u32x4 a = {cvtpk(e[0], e[1]), cvtpk(e[2], e[3]), cvtpk(e[4], e[5]), cvtpk(e[6], e[7])}; pf[3] = __builtin_bit_cast(bf16x8, a); }
      l1 = l1 * alphab + ps;
      OPAQUE(l1);
      SBAR();
      acc[2] = MFMA32(cat8(vl[1][2], vh[1][2]), pf[1], acc[2]);
      float t2 = fmaxf(fmaxf(sr[0], sr[1]), fmaxf(sr[2], sr[3]));
      t2 = xmax16(t2);
      t2 = xmax32(t2);
      const bool keep2 = __all(t2 - m2 <= THRS);
      float mn2 = keep2 ? m2 : fmaxf(m2, t2);
      float alpha2 = keep2 ? 1.f : __builtin_amdgcn_exp2f((m2 - mn2) * CS);
      m2 = mn2;
      SBAR();
      acc[3] = MFMA32(cat8(vl[1][3], vh[1][3]), pf[1], acc[3]);
      const float mc2 = -mn2 * CS;
      float e0 = __builtin_amdgcn_exp2f(fmaf(sr[0], CS, mc2));
      float e1 = __builtin_amdgcn_exp2f(fmaf(sr[1], CS, mc2));
      float e2 = __builtin_amdgcn_exp2f(fmaf(sr[2], CS, mc2));
      float e3 = __builtin_amdgcn_exp2f(fmaf(sr[3], CS, mc2));
      l2 = l2 * alpha2 + (e0 + e1) + (e2 + e3);
      OPAQUE(l2);
      u32x2 p2u = {cvtpk(e0, e1), cvtpk(e2, e3)};
      s16x4 p2 = __builtin_bit_cast(s16x4, p2u);
      SBAR();
      if (!keepb) {
#pragma unroll
        for (int dt = 0; dt < 4; ++dt)
#pragma unroll
          for (int r = 0; r < 16; ++r) acc[dt][r] *= alphab;
      }
      if (!keep2) {
#pragma unroll
        for (int dd = 0; dd < 8; ++dd)
#pragma unroll
          for (int r = 0; r < 4; ++r) acc2[dd][r] *= alpha2;
      }
      issue_v(IC<3>{});
      WAITL8(8, vl[0][0], vh[0][0], vl[0][1], vh[0][1], vl[0][2], vh[0][2], vl[0][3], vh[0][3]);
#pragma unroll
      for (int dt = 0; dt < 4; ++dt) acc[dt] = MFMA32(cat8(vl[0][dt], vh[0][dt]), pf[2], acc[dt]);
      SBAR();
      s16x4 vr[8];
#pragma unroll
      for (int dd = 0; dd < 8; ++dd) vr[dd] = rdtr<OB>(va2_ ^ (dd << 5));
      WAITL8(8, vl[1][0], vh[1][0], vl[1][1], vh[1][1], vl[1][2], vh[1][2], vl[1][3], vh[1][3]);
#pragma unroll
      for (int dt = 0; dt < 4; ++dt) acc[dt] = MFMA32(cat8(vl[1][dt], vh[1][dt]), pf[3], acc[dt]);
      WAITL8(0, vr[0], vr[1], vr[2], vr[3], vr[4], vr[5], vr[6], vr[7]);
#pragma unroll
      for (int dd = 0; dd < 8; ++dd) acc2[dd] = MFMA16K16(vr[dd], p2, acc2[dd]);
    };
    for (int t = 0; t < 8; t += 2) {
      body(IC<0>{}, t);
      body(IC<1>{}, t + 1);
    }
  }

  int tz = threadIdx.x;
  OPAQUE(tz);
  asm volatile("s_waitcnt lgkmcnt(0)" ::: "memory");
  RAW_BAR();
  {
    const int l15 = tz & 15, g = (tz >> 4) & 3;
    float lt2 = xsum32(xsum16(l2));
    float* pw = (float*)(smem + ((w & 1) ? A_OFF_B + A_BUFB : A_OFF_A + A_BUFA) + (w >> 1) * 8192);
#pragma unroll
    for (int dd = 0; dd < 8; ++dd) *(f32x4*)(pw + l15 * 128 + 16 * dd + 4 * g) = acc2[dd];
    float* st = (float*)(smem + A_OFF_Q2);
    if (g == 0) { st[w * 32 + l15] = m2; st[w * 32 + 16 + l15] = lt2; }
  }
  asm volatile("s_waitcnt lgkmcnt(0)" ::: "memory");
  RAW_BAR();
  {
    const int q = tz >> 4, d0 = (tz & 15) * 8;
    const float* st = (const float*)(smem + A_OFF_Q2);
    float mm = fmaxf(fmaxf(st[q], st[32 + q]), fmaxf(st[64 + q], st[96 + q]));
    float o[8] = {0, 0, 0, 0, 0, 0, 0, 0};
    float ls = 0.f;
#pragma unroll
    for (int ww = 0; ww < 4; ++ww) {
      float sc = __builtin_amdgcn_exp2f((st[ww * 32 + q] - mm) * CS);
      ls += st[ww * 32 + 16 + q] * sc;
      const float* pw = (const float*)(smem + ((ww & 1) ? A_OFF_B + A_BUFB : A_OFF_A + A_BUFA) + (ww >> 1) * 8192) + q * 128 + d0;
      f32x4 a = *(const f32x4*)pw, c = *(const f32x4*)(pw + 4);
#pragma unroll
      for (int j = 0; j < 4; ++j) { o[j] += a[j] * sc; o[4 + j] += c[j] * sc; }
    }
    float inv = 1.f / ls;
    u32x4 pk = {cvtpk(o[0] * inv, o[1] * inv), cvtpk(o[2] * inv, o[3] * inv),
                cvtpk(o[4] * inv, o[5] * inv), cvtpk(o[6] * inv, o[7] * inv)};
    *(u32x4*)(og + (size_t)(b * SQ + 128 + q) * HD + h * DH + d0) = pk;
  }
  asm volatile("s_waitcnt lgkmcnt(0)" ::: "memory");
  RAW_BAR();
  {
    const int l31 = tz & 31, hh = (tz >> 5) & 1;
    float lt = xsum32(l1);
    float inv = 1.f / lt;
    char* ob = smem + (w < 2 ? A_OFF_A + A_BUFA : A_OFF_B + A_BUFB) + ((w & 1) * 32 + l31) * 256 + 8 * hh;
#pragma unroll
    for (int dt = 0; dt < 4; ++dt)
#pragma unroll
      for (int i = 0; i < 4; ++i) {
        u32x2 pk = {cvtpk(acc[dt][4 * i] * inv, acc[dt][4 * i + 1] * inv),
                    cvtpk(acc[dt][4 * i + 2] * inv, acc[dt][4 * i + 3] * inv)};
        *(u32x2*)(ob + (((4 * dt + i) ^ (l31 & 15)) << 4)) = pk;
      }
    if (tz == 0 && nidx >= 0) *slot = 4096u;
  }
  asm volatile("s_waitcnt lgkmcnt(0)" ::: "memory");
  RAW_BAR();
  {
    const int c = tz & 15, r0 = tz >> 4;
    short* orow = og + (size_t)(b * SQ + r0) * HD + h * DH + c * 8;
#pragma unroll
    for (int j = 0; j < 8; ++j) {
      const int r = r0 + 16 * j;
      const char* src = smem + (j < 4 ? A_OFF_A + A_BUFA + r * 256 : A_OFF_B + A_BUFB + (r - 64) * 256) + ((c ^ r0) << 4);
      bf16x8 v = *(const bf16x8*)src;
      *(bf16x8*)(orow + (size_t)(16 * j) * HD) = v;
    }
  }
  if (nidx < 0) break;
  pidx = nidx;
  }
}

constexpr int B_NST = 4;
constexpr int B_BUFA = 20480;
constexpr int B_BUFB = 8192;
constexpr int B_STRIDE = B_BUFA + B_BUFB;
constexpr int B_LDS = B_NST * B_STRIDE;

__global__ __launch_bounds__(512, 2) void oproj_kernel(const short* __restrict__ og,
                                                        const short* __restrict__ wo,
                                                        short* __restrict__ out) {
  extern __shared__ __attribute__((aligned(16))) char smem[];
  const int tid = threadIdx.x, lane = tid & 63;
  const int wv = __builtin_amdgcn_readfirstlane(tid >> 6);
  const int w = wv & 3;
  const int xg = blockIdx.x & 7, idx = blockIdx.x >> 3;
  const int b = xg * 4 + (idx >> 3), nt0 = idx & 7;
  const int l31 = lane & 31, hh = lane >> 5, l15 = lane & 15, g = lane >> 4;
  const int qp = l15 >> 2, p4 = lane & 3, blk = (lane >> 4) & 1;
  constexpr int NT = HD / 64;
  if (wv >= 4) {
    const char* ob = (const char*)(og + (size_t)b * SQ * HD);
    const char* wob = (const char*)(wo + nt0 * 64);
    unsigned osrc[5], wsrc[2];
#pragma unroll
    for (int ii = 0; ii < 5; ++ii) {
      int r = 8 * (w * 5 + ii) + (lane >> 3), cp = lane & 7;
      int rs = r < SQ ? r : SQ - 1;
      osrc[ii] = rs * (HD * 2) + ((cp ^ ((r >> 1) & 7)) << 4);
    }
#pragma unroll
    for (int ii = 0; ii < 2; ++ii) {
      int r = 8 * (w * 2 + ii) + (lane >> 3), cp = lane & 7;
      wsrc[ii] = r * (DM * 2) + ((cp ^ (((r >> 1) & 1) << 2)) << 4);
    }
    auto stage = [&](int t) {
      const int buf = t & 3;
      char* la = smem + buf * B_STRIDE + w * 5120;
      char* lb = smem + buf * B_STRIDE + B_BUFA + w * 2048;
      const char* ot = ob + t * 128;
      const char* wt = wob + (size_t)t * (64 * DM * 2);
#pragma unroll
      for (int ii = 0; ii < 5; ++ii) glds16(ot + osrc[ii], la + ii * 1024);
#pragma unroll
      for (int ii = 0; ii < 2; ++ii) glds16(wt + wsrc[ii], lb + ii * 1024);
    };
    stage(0); stage(1); stage(2);
    asm volatile("s_waitcnt vmcnt(14)" ::: "memory");
    for (int t = 0; t < NT; ++t) {
      RAW_BAR();
      if (t + 3 < NT) { stage(t + 3); asm volatile("s_waitcnt vmcnt(14)" ::: "memory"); }
      else if (t + 2 < NT) asm volatile("s_waitcnt vmcnt(7)" ::: "memory");
      else asm volatile("s_waitcnt vmcnt(0)" ::: "memory");
    }
    return;
  }
  f32x16 acc[2] = {f32x16{}, f32x16{}};
  f32x4 acc2 = f32x4{};
  const int sb = (int)(uintptr_t)smem;
  const int xa = sb + (32 * w + l31) * 128 + ((hh ^ ((l31 >> 1) & 7)) << 4);
  const int wa = sb + B_BUFA + (8 * hh + qp) * 128 + (((2 * blk + (p4 >> 1)) ^ ((qp >> 1) << 2)) << 4) + 8 * (p4 & 1);
  const int xa2 = sb + (128 + l15) * 128 + ((g ^ ((l15 >> 1) & 7)) << 4);
  const int wa2 = sb + B_BUFA + (8 * g + qp) * 128 + (((2 * w + (p4 >> 1)) ^ ((qp >> 1) << 2)) << 4) + 8 * (p4 & 1);
  auto body = [&](auto BC) {
    constexpr int buf = decltype(BC)::value;
    constexpr int OB = (buf & 1) * B_STRIDE;
    const int hi2 = (buf >> 1) * 2 * B_STRIDE;
    RAW_BAR();
    const int xa_ = xa + hi2, w0 = wa + hi2, w1 = (wa ^ 64) + hi2, xa2_ = xa2 + hi2, wa2_ = wa2 + hi2;
    bf16x8 xf[4], xr[2];
    s16x4 wl[4][2], wh[4][2], rl[2], rh[2];
    sfor<0, 4>([&](auto SC) {
      constexpr int s = decltype(SC)::value;
      xf[s] = rd128<OB>(xa_ ^ (s << 5));
      wl[s][0] = rdtr<OB + s * 2048>(w0); wh[s][0] = rdtr<OB + s * 2048 + 512>(w0);
      wl[s][1] = rdtr<OB + s * 2048>(w1); wh[s][1] = rdtr<OB + s * 2048 + 512>(w1);
      if constexpr (s >= 1) {
        asm volatile("s_waitcnt lgkmcnt(5)" : "+v"(xf[s - 1]), "+v"(wl[s - 1][0]), "+v"(wh[s - 1][0]), "+v"(wl[s - 1][1]), "+v"(wh[s - 1][1]) :: "memory");
        acc[0] = MFMA32(cat8(wl[s - 1][0], wh[s - 1][0]), xf[s - 1], acc[0]);
        acc[1] = MFMA32(cat8(wl[s - 1][1], wh[s - 1][1]), xf[s - 1], acc[1]);
      }
    });
    xr[0] = rd128<OB>(xa2_); rl[0] = rdtr<OB>(wa2_); rh[0] = rdtr<OB + 512>(wa2_);
    xr[1] = rd128<OB>(xa2_ ^ 64); rl[1] = rdtr<OB + 4096>(wa2_); rh[1] = rdtr<OB + 4096 + 512>(wa2_);
    asm volatile("s_waitcnt lgkmcnt(6)" : "+v"(xf[3]), "+v"(wl[3][0]), "+v"(wh[3][0]), "+v"(wl[3][1]), "+v"(wh[3][1]) :: "memory");
    acc[0] = MFMA32(cat8(wl[3][0], wh[3][0]), xf[3], acc[0]);
    acc[1] = MFMA32(cat8(wl[3][1], wh[3][1]), xf[3], acc[1]);
    asm volatile("s_waitcnt lgkmcnt(0)" : "+v"(xr[0]), "+v"(rl[0]), "+v"(rh[0]), "+v"(xr[1]), "+v"(rl[1]), "+v"(rh[1]) :: "memory");
    acc2 = MFMA16(cat8(rl[0], rh[0]), xr[0], acc2);
    acc2 = MFMA16(cat8(rl[1], rh[1]), xr[1], acc2);
  };
  for (int t = 0; t < NT; t += 4) {
    body(IC<0>{});
    body(IC<1>{});
    body(IC<2>{});
    body(IC<3>{});
  }
  {
    short* orow = out + (size_t)(b * SQ + 32 * w + l31) * DM + nt0 * 64 + 4 * hh;
#pragma unroll
    for (int nt = 0; nt < 2; ++nt)
#pragma unroll
      for (int i = 0; i < 4; ++i) {
        u32x2 pk = {cvtpk(acc[nt][4 * i], acc[nt][4 * i + 1]), cvtpk(acc[nt][4 * i + 2], acc[nt][4 * i + 3])};
        *(u32x2*)(orow + 32 * nt + 8 * i) = pk;
      }
    u32x2 pk = {cvtpk(acc2[0], acc2[1]), cvtpk(acc2[2], acc2[3])};
    *(u32x2*)(out + (size_t)(b * SQ + 128 + l15) * DM + nt0 * 64 + 16 * w + 4 * g) = pk;
  }
}

extern "C" void kernel_launch(void* const* d_in, const int* in_sizes, int n_in, void* d_out,
                              int out_size, void* d_ws, size_t ws_size, hipStream_t stream) {
  const short* x = (const short*)d_in[0];
  const short* kc = (const short*)d_in[1];
  const short* vc = (const short*)d_in[2];
  const short* wq = (const short*)d_in[3];
  const short* wo = (const short*)d_in[4];
  short* og = (short*)d_ws;
  short* out = (short*)d_out;
  unsigned* counter = (unsigned*)(og + (size_t)MROWS * HD);
  (void)hipFuncSetAttribute((const void*)attn_kernel, hipFuncAttributeMaxDynamicSharedMemorySize, A_LDS);
  (void)hipFuncSetAttribute((const void*)oproj_kernel, hipFuncAttributeMaxDynamicSharedMemorySize, B_LDS);
  attn_kernel<<<512, 256, A_LDS, stream>>>(x, wq, kc, vc, og, counter);
  oproj_kernel<<<256, 512, B_LDS, stream>>>(og, wo, out);
}
```

```cpp
#include <hip/hip_runtime.h>
#include <hip/hip_bf16.h>

typedef __attribute__((ext_vector_type(8))) short bf16x8;
typedef __attribute__((ext_vector_type(4))) short s16x4;
typedef __attribute__((ext_vector_type(16))) float f32x16;
typedef __attribute__((ext_vector_type(4))) float f32x4;
typedef __attribute__((ext_vector_type(4))) unsigned u32x4;
typedef __attribute__((ext_vector_type(2))) unsigned u32x2;

constexpr int NB = 32, SQ = 144, DM = 512, NH = 32, SK = 512, DH = 128;
constexpr int HD = NH * DH;
constexpr int MROWS = NB * SQ;

#define MFMA32(a, b, c) __builtin_amdgcn_mfma_f32_32x32x16_bf16((a), (b), (c), 0, 0, 0)
#define MFMA16(a, b, c) __builtin_amdgcn_mfma_f32_16x16x32_bf16((a), (b), (c), 0, 0, 0)
#define MFMA16K16(a, b, c) __builtin_amdgcn_mfma_f32_16x16x16bf16_1k((a), (b), (c), 0, 0, 0)

__device__ __forceinline__ unsigned cvtpk(float lo, float hi) {
  unsigned r;
  asm volatile("v_cvt_pk_bf16_f32 %0, %1, %2" : "=v"(r) : "v"(lo), "v"(hi));
  return r;
}
__device__ __forceinline__ bf16x8 cat8(s16x4 lo, s16x4 hi) {
  return __builtin_shufflevector(lo, hi, 0, 1, 2, 3, 4, 5, 6, 7);
}
__device__ __forceinline__ void glds16(const void* g, char* l) {
  __builtin_amdgcn_global_load_lds((const unsigned*)g, (unsigned*)l, 16, 0, 0);
}
__device__ __forceinline__ void glds16nt(const void* g, char* l) {
  __builtin_amdgcn_global_load_lds((const unsigned*)g, (unsigned*)l, 16, 0, 2);
}
__device__ __forceinline__ float xmax32(float v) {
  auto rr = __builtin_amdgcn_permlane32_swap(__float_as_uint(v), __float_as_uint(v), false, false);
  return fmaxf(__uint_as_float(rr[0]), __uint_as_float(rr[1]));
}
__device__ __forceinline__ float xsum32(float v) {
  auto rr = __builtin_amdgcn_permlane32_swap(__float_as_uint(v), __float_as_uint(v), false, false);
  return __uint_as_float(rr[0]) + __uint_as_float(rr[1]);
}
__device__ __forceinline__ float xmax16(float v) {
  auto rr = __builtin_amdgcn_permlane16_swap(__float_as_uint(v), __float_as_uint(v), false, false);
  return fmaxf(__uint_as_float(rr[0]), __uint_as_float(rr[1]));
}
__device__ __forceinline__ float xsum16(float v) {
  auto rr = __builtin_amdgcn_permlane16_swap(__float_as_uint(v), __float_as_uint(v), false, false);
  return __uint_as_float(rr[0]) + __uint_as_float(rr[1]);
}
template <int OFF> __device__ __forceinline__ bf16x8 rd128(int a) {
  bf16x8 r;
  asm volatile("ds_read_b128 %0, %1 offset:%2" : "=v"(r) : "v"(a), "i"(OFF) : "memory");
  return r;
}
template <int OFF> __device__ __forceinline__ s16x4 rdtr(int a) {
  s16x4 r;
  asm volatile("ds_read_b64_tr_b16 %0, %1 offset:%2" : "=v"(r) : "v"(a), "i"(OFF) : "memory");
  return r;
}
#define WAITL2(N, a, b) asm volatile("s_waitcnt lgkmcnt(" #N ")" : "+v"(a), "+v"(b) :: "memory")
#define WAITL4(N, a, b, c, d) asm volatile("s_waitcnt lgkmcnt(" #N ")" : "+v"(a), "+v"(b), "+v"(c), "+v"(d) :: "memory")
#define WAITL8(N, a, b, c, d, e, f, g_, h_) asm volatile("s_waitcnt lgkmcnt(" #N ")" : "+v"(a), "+v"(b), "+v"(c), "+v"(d), "+v"(e), "+v"(f), "+v"(g_), "+v"(h_) :: "memory")
#define RAW_BAR() __builtin_amdgcn_s_barrier()
#define SBAR() __builtin_amdgcn_sched_barrier(0)
#define OPAQUE(v) asm volatile("" : "+v"(v))
template <int N> struct IC { static constexpr int value = N; };
template <int I, int N, class F> __device__ __forceinline__ void sfor(F&& f) {
  if constexpr (I < N) { f(IC<I>{}); sfor<I + 1, N>(f); }
}

constexpr int A_BUFA = 20480;
constexpr int A_BUFB = 16384;
constexpr int A_OFF_A = 0;
constexpr int A_OFF_B = 2 * A_BUFA;
constexpr int A_OFF_Q2 = A_OFF_B + 2 * A_BUFB;
constexpr int A_OFF_SLOT = A_OFF_Q2 + 4096;
constexpr int A_LDS = A_OFF_SLOT + 16;

__global__ __launch_bounds__(256, 2) void attn_kernel(
    const short* __restrict__ x, const short* __restrict__ wq, const short* __restrict__ kc,
    const short* __restrict__ vc, short* __restrict__ og, unsigned* __restrict__ counter) {
  extern __shared__ __attribute__((aligned(16))) char smem[];
  const int tid = threadIdx.x, lane = tid & 63;
  const int w = __builtin_amdgcn_readfirstlane(tid >> 6);
  const int l31 = lane & 31, hh = lane >> 5, l15 = lane & 15, g = lane >> 4;
  const int qp = l15 >> 2, p4 = lane & 3, blk = (lane >> 4) & 1;
  const int sb = (int)(uintptr_t)smem;
  volatile unsigned* slot = (volatile unsigned*)(smem + A_OFF_SLOT);

  const unsigned ksrc = (16 * w + g) * 256 + ((l15 ^ g) << 4);
  const unsigned vsrc = (16 * w + g) * 256 + ((l15 ^ (g << 2)) << 4);
  auto stage_kv = [&](int pi, int t, int buf, unsigned ko, unsigned vo) {
    const char* kb = (const char*)kc + (size_t)pi * (SK * DH * 2) + t * 16384;
    const char* vb = (const char*)vc + (size_t)pi * (SK * DH * 2) + t * 16384;
#pragma unroll
    for (int i = 0; i < 4; ++i)
      glds16nt(kb + ((ko ^ (i << 6)) + i * 1024), smem + A_OFF_A + buf * A_BUFA + w * 4096 + i * 1024);
#pragma unroll
    for (int i = 0; i < 4; ++i)
      glds16nt(vb + (vo + i * 1024), smem + A_OFF_B + buf * A_BUFB + w * 4096 + i * 1024);
  };
  auto stage_q = [&](int pi, int t, int buf) {
    int ln = lane;
    OPAQUE(ln);
    const char* xt = (const char*)(x + (size_t)(pi >> 5) * SQ * DM) + t * 128;
    const char* wt = (const char*)(wq + (pi & 31) * DH) + (size_t)t * (64 * HD * 2);
#pragma unroll
    for (int ii = 0; ii < 5; ++ii) {
      int r = 8 * (w * 5 + ii) + (ln >> 3), cp = ln & 7;
      int rs = r < SQ ? r : SQ - 1;
      unsigned so = rs * 1024 + ((cp ^ ((r >> 1) & 7)) << 4);
      glds16(xt + so, smem + A_OFF_A + buf * A_BUFA + w * 5120 + ii * 1024);
    }
    const unsigned wo0 = (16 * w + (ln >> 4)) * (HD * 2) + (((ln & 15) ^ ((ln >> 4) << 2)) << 4);
#pragma unroll
    for (int ii = 0; ii < 4; ++ii)
      glds16(wt + (wo0 + ii * 4 * HD * 2), smem + A_OFF_B + buf * A_BUFB + w * 4096 + ii * 1024);
  };
  int pidx = (blockIdx.x & 7) * 128 + (blockIdx.x >> 3);
#define STAG 0
  if (STAG > 0 && blockIdx.x >= 256) {
    unsigned long long t0 = __builtin_amdgcn_s_memtime();
    while (__builtin_amdgcn_s_memtime() - t0 < STAG) __builtin_amdgcn_s_sleep(8);
  }
  stage_q(pidx, 0, 0);
  if (tid == 0) *slot = blockIdx.x + 512u;
  constexpr float CS = 0.088388347648318440f * 1.4426950408889634f;
  constexpr float THRS = 8.f / CS;

  for (;;) {
  const int b = pidx >> 5, h = pidx & 31;
  f32x16 acc[4];
#pragma unroll
  for (int i = 0; i < 4; ++i) acc[i] = f32x16{};
  f32x4 acc2[8];
#pragma unroll
  for (int i = 0; i < 8; ++i) acc2[i] = f32x4{};
  {
    int lq = lane;
    OPAQUE(lq);
    const int l31 = lq & 31, hh = lq >> 5, l15 = lq & 15, g = lq >> 4;
    const int qp = l15 >> 2, p4 = lq & 3, blk = (lq >> 4) & 1;
    const int xa = sb + A_OFF_A + (32 * w + l31) * 128 + ((hh ^ ((l31 >> 1) & 7)) << 4);
    const int wa = sb + A_OFF_B + (8 * hh + qp) * 256 + (((2 * blk + (p4 >> 1)) ^ (qp << 2)) << 4) + 8 * (p4 & 1);
    const int xa2 = sb + A_OFF_A + (128 + l15) * 128 + ((g ^ ((l15 >> 1) & 7)) << 4);
    const int wa2 = sb + A_OFF_B + (8 * g + qp) * 256 + (((4 * w + (p4 >> 1)) ^ (qp << 2)) << 4) + 8 * (p4 & 1);
    auto body = [&](auto BC, int t) {
      constexpr int buf = decltype(BC)::value;
      constexpr int OA = buf * A_BUFA, OB = buf * A_BUFB;
      asm volatile("s_waitcnt vmcnt(0) lgkmcnt(0)" ::: "memory");
      RAW_BAR();
      if (t < 7) stage_q(pidx, t + 1, buf ^ 1);
      else stage_kv(pidx, 0, buf ^ 1, ksrc, vsrc);
      SBAR();
      const int w0 = wa, w1 = wa ^ 64, w2 = wa ^ 128, w3 = wa ^ 192;
      bf16x8 xf[2];
      s16x4 wl[2][4], wh[2][4];
      bf16x8 xr[2];
      s16x4 rl[2][2], rh[2][2];
      auto issue = [&](auto SC) {
        constexpr int s = decltype(SC)::value, u = s & 1;
        xf[u] = rd128<OA>(xa ^ (s << 5));
        wl[u][0] = rdtr<OB + s * 4096>(w0); wh[u][0] = rdtr<OB + s * 4096 + 1024>(w0);
        wl[u][1] = rdtr<OB + s * 4096>(w1); wh[u][1] = rdtr<OB + s * 4096 + 1024>(w1);
        wl[u][2] = rdtr<OB + s * 4096>(w2); wh[u][2] = rdtr<OB + s * 4096 + 1024>(w2);
        wl[u][3] = rdtr<OB + s * 4096>(w3); wh[u][3] = rdtr<OB + s * 4096 + 1024>(w3);
      };
      issue(IC<0>{});
      sfor<0, 4>([&](auto SC) {
        constexpr int s = decltype(SC)::value, u = s & 1;
        if constexpr (s < 3) {
          issue(IC<s + 1>{});
          asm volatile("s_waitcnt lgkmcnt(9)" : "+v"(xf[u]), "+v"(wl[u][0]), "+v"(wh[u][0]), "+v"(wl[u][1]), "+v"(wh[u][1]),
                       "+v"(wl[u][2]), "+v"(wh[u][2]), "+v"(wl[u][3]), "+v"(wh[u][3]) :: "memory");
        } else {
          xr[0] = rd128<OA>(xa2); xr[1] = rd128<OA>(xa2 ^ 64);
          rl[0][0] = rdtr<OB>(wa2); rh[0][0] = rdtr<OB + 1024>(wa2);
          rl[0][1] = rdtr<OB>(wa2 ^ 32); rh[0][1] = rdtr<OB + 1024>(wa2 ^ 32);
          rl[1][0] = rdtr<OB + 8192>(wa2); rh[1][0] = rdtr<OB + 8192 + 1024>(wa2);
          rl[1][1] = rdtr<OB + 8192>(wa2 ^ 32); rh[1][1] = rdtr<OB + 8192 + 1024>(wa2 ^ 32);
          asm volatile("s_waitcnt lgkmcnt(10)" : "+v"(xf[u]), "+v"(wl[u][0]), "+v"(wh[u][0]), "+v"(wl[u][1]), "+v"(wh[u][1]),
                       "+v"(wl[u][2]), "+v"(wh[u][2]), "+v"(wl[u][3]), "+v"(wh[u][3]) :: "memory");
        }
#pragma unroll
        for (int dt = 0; dt < 4; ++dt) acc[dt] = MFMA32(cat8(wl[u][dt], wh[u][dt]), xf[u], acc[dt]);
        SBAR();
      });
      asm volatile("s_waitcnt lgkmcnt(0)" : "+v"(xr[0]), "+v"(xr[1]), "+v"(rl[0][0]), "+v"(rh[0][0]), "+v"(rl[0][1]), "+v"(rh[0][1]),
                   "+v"(rl[1][0]), "+v"(rh[1][0]), "+v"(rl[1][1]), "+v"(rh[1][1]) :: "memory");
#pragma unroll
      for (int s2 = 0; s2 < 2; ++s2)
#pragma unroll
        for (int e = 0; e < 2; ++e) acc2[e] = MFMA16(cat8(rl[s2][e], rh[s2][e]), xr[s2], acc2[e]);
    };
    for (int t = 0; t < 8; t += 2) {
      body(IC<0>{}, t);
      body(IC<1>{}, t + 1);
    }
  }
  bf16x8 qf[8];
#pragma unroll
  for (int dt = 0; dt < 4; ++dt) {
#pragma unroll
    for (int s = 0; s < 2; ++s) {
      unsigned a0 = cvtpk(acc[dt][8 * s + 0], acc[dt][8 * s + 1]);
      unsigned a1 = cvtpk(acc[dt][8 * s + 2], acc[dt][8 * s + 3]);
      unsigned b0 = cvtpk(acc[dt][8 * s + 4], acc[dt][8 * s + 5]);
      unsigned b1 = cvtpk(acc[dt][8 * s + 6], acc[dt][8 * s + 7]);
      auto r0 = __builtin_amdgcn_permlane32_swap(a0, b0, false, false);
      auto r1 = __builtin_amdgcn_permlane32_swap(a1, b1, false, false);
      u32x4 wv = {r0[0], r1[0], r0[1], r1[1]};
      qf[2 * dt + s] = __builtin_bit_cast(bf16x8, wv);
    }
  }
  {
#pragma unroll
    for (int e = 0; e < 2; ++e) {
      int dd = 2 * w + e;
      u32x2 pk = {cvtpk(acc2[e][0], acc2[e][1]), cvtpk(acc2[e][2], acc2[e][3])};
      int ch = (2 * dd + (g >> 1)) ^ l15;
      asm volatile("ds_write_b64 %0, %1" :: "v"(sb + A_OFF_Q2 + l15 * 256 + (ch << 4) + 8 * (g & 1)), "v"(pk) : "memory");
    }
  }
#pragma unroll
  for (int i = 0; i < 4; ++i) acc[i] = f32x16{};
#pragma unroll
  for (int i = 0; i < 8; ++i) acc2[i] = f32x4{};
  int nidx = 0;
  float m1 = -1e30f, l1 = 0.f, m2 = -1e30f, l2 = 0.f;
  {
    int ka = sb + A_OFF_A + l31 * 256 + ((hh ^ l15) << 4);
    int ka2 = sb + A_OFF_A + (16 * w + l15) * 256 + ((g ^ l15) << 4);
    int q2a = sb + A_OFF_Q2 + l15 * 256 + ((g ^ l15) << 4);
    int va = sb + A_OFF_B + (4 * hh + qp) * 256 + (((2 * blk + (p4 >> 1)) ^ (qp << 2)) << 4) + 8 * (p4 & 1);
    int va2 = sb + A_OFF_B + (16 * w + 4 * g + qp) * 256 + (((p4 >> 1) ^ (qp << 2)) << 4) + 8 * (p4 & 1);
    auto body = [&](auto BC, int t) {
      constexpr int buf = decltype(BC)::value;
      constexpr int OA = buf * A_BUFA, OB = buf * A_BUFB;
      asm volatile("s_waitcnt vmcnt(0) lgkmcnt(0)" ::: "memory");
      RAW_BAR();
      int ka_ = ka, ka2_ = ka2, q2a_ = q2a, va_ = va, va2_ = va2;
      unsigned ko = ksrc, vo = vsrc;
      OPAQUE(ka_); OPAQUE(ka2_); OPAQUE(q2a_); OPAQUE(va_); OPAQUE(va2_); OPAQUE(ko); OPAQUE(vo);
      if (t < 7) {
        stage_kv(pidx, t + 1, buf ^ 1, ko, vo);
      } else {
        const unsigned it = __builtin_amdgcn_readfirstlane(*slot);
        nidx = it < 1024u ? (int)((it & 7) * 128 + (it >> 3)) : -1;
        if (nidx >= 0) stage_q(nidx, 0, 0);
      }
      SBAR();
      f32x16 s0 = f32x16{}, s1 = f32x16{};
      f32x4 sr = f32x4{};
      bf16x8 kA[4], kB[4];
#pragma unroll
      for (int i = 0; i < 4; ++i) kA[i] = rd128<OA>(ka_ ^ (i << 5));
#pragma unroll
      for (int i = 0; i < 4; ++i) kB[i] = rd128<OA>(ka_ ^ ((4 + i) << 5));
      WAITL4(4, kA[0], kA[1], kA[2], kA[3]);
      s0 = MFMA32(kA[0], qf[0], s0);
      s0 = MFMA32(kA[1], qf[1], s0);
      SBAR();
      s0 = MFMA32(kA[2], qf[2], s0);
      s0 = MFMA32(kA[3], qf[3], s0);
      SBAR();
#pragma unroll
      for (int i = 0; i < 4; ++i) kA[i] = rd128<OA + 8192>(ka_ ^ (i << 5));
      WAITL4(4, kB[0], kB[1], kB[2], kB[3]);
      s0 = MFMA32(kB[0], qf[4], s0);
      s0 = MFMA32(kB[1], qf[5], s0);
      SBAR();
      s0 = MFMA32(kB[2], qf[6], s0);
      s0 = MFMA32(kB[3], qf[7], s0);
      SBAR();
#pragma unroll
      for (int i = 0; i < 4; ++i) kB[i] = rd128<OA + 8192>(ka_ ^ ((4 + i) << 5));
      WAITL4(4, kA[0], kA[1], kA[2], kA[3]);
      s1 = MFMA32(kA[0], qf[0], s1);
      SBAR();
      s1 = MFMA32(kA[1], qf[1], s1);
      float tmax = fmaxf(fmaxf(s0[0], s0[1]), s0[2]);
#pragma unroll
      for (int r = 3; r < 9; r += 2) tmax = fmaxf(fmaxf(tmax, s0[r]), s0[r + 1]);
      SBAR();
      s1 = MFMA32(kA[2], qf[2], s1);
#pragma unroll
      for (int r = 9; r < 15; r += 2) tmax = fmaxf(fmaxf(tmax, s0[r]), s0[r + 1]);
      tmax = fmaxf(tmax, s0[15]);
      tmax = xmax32(tmax);
      SBAR();
      s1 = MFMA32(kA[3], qf[3], s1);
      SBAR();
      if (!__all(tmax - m1 <= THRS)) {
        float mn0 = fmaxf(m1, tmax);
        float alpha = __builtin_amdgcn_exp2f((m1 - mn0) * CS);
        m1 = mn0;
        l1 *= alpha;
#pragma unroll
        for (int dt = 0; dt < 4; ++dt)
#pragma unroll
          for (int r = 0; r < 16; ++r) acc[dt][r] *= alpha;
      }
      const float mn = m1;
      bf16x8 kr[4], qr[4];
#pragma unroll
      for (int ks = 0; ks < 4; ++ks) { kr[ks] = rd128<OA>(ka2_ ^ (ks << 6)); qr[ks] = rd128<0>(q2a_ ^ (ks << 6)); }
      WAITL4(8, kB[0], kB[1], kB[2], kB[3]);
      bf16x8 pf[4];
      float mc = -mn * CS;
      float ps = 0.f;
      float e[8];
      s1 = MFMA32(kB[0], qf[4], s1);
#pragma unroll
      for (int j = 0; j < 4; ++j) { e[j] = __builtin_amdgcn_exp2f(fmaf(s0[j], CS, mc)); ps += e[j]; }
      SBAR();
      s1 = MFMA32(kB[1], qf[5], s1);
#pragma unroll
      for (int j = 4; j < 8; ++j) { e[j] = __builtin_amdgcn_exp2f(fmaf(s0[j], CS, mc)); ps += e[j]; }
      { u32x4 a = {cvtpk(e[0], e[1]), cvtpk(e[2], e[3]), cvtpk(e[4], e[5]), cvtpk(e[6], e[7])}; pf[0] = __builtin_bit_cast(bf16x8, a); }
      SBAR();
      s1 = MFMA32(kB[2], qf[6], s1);
#pragma unroll
      for (int j = 0; j < 4; ++j) { e[j] = __builtin_amdgcn_exp2f(fmaf(s0[8 + j], CS, mc)); ps += e[j]; }
      SBAR();
      s1 = MFMA32(kB[3], qf[7], s1);
#pragma unroll
      for (int j = 4; j < 8; ++j) { e[j] = __builtin_amdgcn_exp2f(fmaf(s0[8 + j], CS, mc)); ps += e[j]; }
      { u32x4 a = {cvtpk(e[0], e[1]), cvtpk(e[2], e[3]), cvtpk(e[4], e[5]), cvtpk(e[6], e[7])}; pf[1] = __builtin_bit_cast(bf16x8, a); }
      l1 += ps;
      OPAQUE(l1);
      SBAR();
      SBAR();
      const int v0 = va_, v1 = va_ ^ 64, v2 = va_ ^ 128, v3 = va_ ^ 192;
      s16x4 vl[2][4], vh[2][4];
      auto issue_v = [&](auto SC) {
        constexpr int s = decltype(SC)::value, u = s & 1;
        vl[u][0] = rdtr<OB + s * 4096>(v0); vh[u][0] = rdtr<OB + s * 4096 + 2048>(v0);
        vl[u][1] = rdtr<OB + s * 4096>(v1); vh[u][1] = rdtr<OB + s * 4096 + 2048>(v1);
        vl[u][2] = rdtr<OB + s * 4096>(v2); vh[u][2] = rdtr<OB + s * 4096 + 2048>(v2);
        vl[u][3] = rdtr<OB + s * 4096>(v3); vh[u][3] = rdtr<OB + s * 4096 + 2048>(v3);
      };
      issue_v(IC<0>{});
      WAITL8(8, kr[0], kr[1], kr[2], kr[3], qr[0], qr[1], qr[2], qr[3]);
#pragma unroll
      for (int ks = 0; ks < 4; ++ks) sr = MFMA16(kr[ks], qr[ks], sr);
      SBAR();
      SBAR();
      issue_v(IC<1>{});
      WAITL8(8, vl[0][0], vh[0][0], vl[0][1], vh[0][1], vl[0][2], vh[0][2], vl[0][3], vh[0][3]);
      acc[0] = MFMA32(cat8(vl[0][0], vh[0][0]), pf[0], acc[0]);
      float tm1 = fmaxf(fmaxf(s1[0], s1[1]), s1[2]);
#pragma unroll
      for (int r = 3; r < 9; r += 2) tm1 = fmaxf(fmaxf(tm1, s1[r]), s1[r + 1]);
      SBAR();
      acc[1] = MFMA32(cat8(vl[0][1], vh[0][1]), pf[0], acc[1]);
#pragma unroll
      for (int r = 9; r < 15; r += 2) tm1 = fmaxf(fmaxf(tm1, s1[r]), s1[r + 1]);
      tm1 = fmaxf(tm1, s1[15]);
      tm1 = xmax32(tm1);
      const bool keepb = __all(tm1 - m1 <= THRS);
      float mnb = keepb ? m1 : fmaxf(m1, tm1);
      float alphab = keepb ? 1.f : __builtin_amdgcn_exp2f((m1 - mnb) * CS);
      m1 = mnb;
      mc = -mnb * CS;
      ps = 0.f;
      SBAR();
      acc[2] = MFMA32(cat8(vl[0][2], vh[0][2]), pf[0], acc[2]);
#pragma unroll
      for (int j = 0; j < 4; ++j) { e[j] = __builtin_amdgcn_exp2f(fmaf(s1[j], CS, mc)); ps += e[j]; }
      SBAR();
      acc[3] = MFMA32(cat8(vl[0][3], vh[0][3]), pf[0], acc[3]);
#pragma unroll
      for (int j = 4; j < 8; ++j) { e[j] = __builtin_amdgcn_exp2f(fmaf(s1[j], CS, mc)); ps += e[j]; }
      { u32x4 a = {cvtpk(e[0], e[1]), cvtpk(e[2], e[3]), cvtpk(e[4], e[5]), cvtpk(e[6], e[7])}; pf[2] = __builtin_bit_cast(bf16x8, a); }
      SBAR();
      issue_v(IC<2>{});
      WAITL8(8, vl[1][0], vh[1][0], vl[1][1], vh[1][1], vl[1][2], vh[1][2], vl[1][3], vh[1][3]);
      acc[0] = MFMA32(cat8(vl[1][0], vh[1][0]), pf[1], acc[0]);
#pragma unroll
      for (int j = 0; j < 4; ++j) { e[j] = __builtin_amdgcn_exp2f(fmaf(s1[8 + j], CS, mc)); ps += e[j]; }
      SBAR();
      acc[1] = MFMA32(cat8(vl[1][1], vh[1][1]), pf[1], acc[1]);
#pragma unroll
      for (int j = 4; j < 8; ++j) { e[j] = __builtin_amdgcn_exp2f(fmaf(s1[8 + j], CS, mc)); ps += e[j]; }
      { u32x4 a = {cvtpk(e[0], e[1]), cvtpk(e[2], e[3]), cvtpk(e[4], e[5]), cvtpk(e[6], e[7])}; pf[3] = __builtin_bit_cast(bf16x8, a); }
      l1 = l1 * alphab + ps;
      OPAQUE(l1);
      SBAR();
      acc[2] = MFMA32(cat8(vl[1][2], vh[1][2]), pf[1], acc[2]);
      float t2 = fmaxf(fmaxf(sr[0], sr[1]), fmaxf(sr[2], sr[3]));
      t2 = xmax16(t2);
      t2 = xmax32(t2);
      const bool keep2 = __all(t2 - m2 <= THRS);
      float mn2 = keep2 ? m2 : fmaxf(m2, t2);
      float alpha2 = keep2 ? 1.f : __builtin_amdgcn_exp2f((m2 - mn2) * CS);
      m2 = mn2;
      SBAR();
      acc[3] = MFMA32(cat8(vl[1][3], vh[1][3]), pf[1], acc[3]);
      const float mc2 = -mn2 * CS;
      float e0 = __builtin_amdgcn_exp2f(fmaf(sr[0], CS, mc2));
      float e1 = __builtin_amdgcn_exp2f(fmaf(sr[1], CS, mc2));
      float e2 = __builtin_amdgcn_exp2f(fmaf(sr[2], CS, mc2));
      float e3 = __builtin_amdgcn_exp2f(fmaf(sr[3], CS, mc2));
      l2 = l2 * alpha2 + (e0 + e1) + (e2 + e3);
      OPAQUE(l2);
      u32x2 p2u = {cvtpk(e0, e1), cvtpk(e2, e3)};
      s16x4 p2 = __builtin_bit_cast(s16x4, p2u);
      SBAR();
      if (!keepb) {
#pragma unroll
        for (int dt = 0; dt < 4; ++dt)
#pragma unroll
          for (int r = 0; r < 16; ++r) acc[dt][r] *= alphab;
      }
      if (!keep2) {
#pragma unroll
        for (int dd = 0; dd < 8; ++dd)
#pragma unroll
          for (int r = 0; r < 4; ++r) acc2[dd][r] *= alpha2;
      }
      issue_v(IC<3>{});
      WAITL8(8, vl[0][0], vh[0][0], vl[0][1], vh[0][1], vl[0][2], vh[0][2], vl[0][3], vh[0][3]);
#pragma unroll
      for (int dt = 0; dt < 4; ++dt) acc[dt] = MFMA32(cat8(vl[0][dt], vh[0][dt]), pf[2], acc[dt]);
      SBAR();
      s16x4 vr[8];
#pragma unroll
      for (int dd = 0; dd < 8; ++dd) vr[dd] = rdtr<OB>(va2_ ^ (dd << 5));
      WAITL8(8, vl[1][0], vh[1][0], vl[1][1], vh[1][1], vl[1][2], vh[1][2], vl[1][3], vh[1][3]);
#pragma unroll
      for (int dt = 0; dt < 4; ++dt) acc[dt] = MFMA32(cat8(vl[1][dt], vh[1][dt]), pf[3], acc[dt]);
      WAITL8(0, vr[0], vr[1], vr[2], vr[3], vr[4], vr[5], vr[6], vr[7]);
#pragma unroll
      for (int dd = 0; dd < 8; ++dd) acc2[dd] = MFMA16K16(vr[dd], p2, acc2[dd]);
    };
    for (int t = 0; t < 8; t += 2) {
      body(IC<0>{}, t);
      body(IC<1>{}, t + 1);
    }
  }

  int tz = threadIdx.x;
  OPAQUE(tz);
  asm volatile("s_waitcnt lgkmcnt(0)" ::: "memory");
  RAW_BAR();
  {
    const int l15 = tz & 15, g = (tz >> 4) & 3;
    float lt2 = xsum32(xsum16(l2));
    float* pw = (float*)(smem + ((w & 1) ? A_OFF_B + A_BUFB : A_OFF_A + A_BUFA) + (w >> 1) * 8192);
#pragma unroll
    for (int dd = 0; dd < 8; ++dd) *(f32x4*)(pw + l15 * 128 + 16 * dd + 4 * g) = acc2[dd];
    float* st = (float*)(smem + A_OFF_Q2);
    if (g == 0) { st[w * 32 + l15] = m2; st[w * 32 + 16 + l15] = lt2; }
  }
  asm volatile("s_waitcnt lgkmcnt(0)" ::: "memory");
  RAW_BAR();
  {
    const int q = tz >> 4, d0 = (tz & 15) * 8;
    const float* st = (const float*)(smem + A_OFF_Q2);
    float mm = fmaxf(fmaxf(st[q], st[32 + q]), fmaxf(st[64 + q], st[96 + q]));
    float o[8] = {0, 0, 0, 0, 0, 0, 0, 0};
    float ls = 0.f;
#pragma unroll
    for (int ww = 0; ww < 4; ++ww) {
      float sc = __builtin_amdgcn_exp2f((st[ww * 32 + q] - mm) * CS);
      ls += st[ww * 32 + 16 + q] * sc;
      const float* pw = (const float*)(smem + ((ww & 1) ? A_OFF_B + A_BUFB : A_OFF_A + A_BUFA) + (ww >> 1) * 8192) + q * 128 + d0;
      f32x4 a = *(const f32x4*)pw, c = *(const f32x4*)(pw + 4);
#pragma unroll
      for (int j = 0; j < 4; ++j) { o[j] += a[j] * sc; o[4 + j] += c[j] * sc; }
    }
    float inv = 1.f / ls;
    u32x4 pk = {cvtpk(o[0] * inv, o[1] * inv), cvtpk(o[2] * inv, o[3] * inv),
                cvtpk(o[4] * inv, o[5] * inv), cvtpk(o[6] * inv, o[7] * inv)};
    *(u32x4*)(og + (size_t)(b * SQ + 128 + q) * HD + h * DH + d0) = pk;
  }
  asm volatile("s_waitcnt lgkmcnt(0)" ::: "memory");
  RAW_BAR();
  {
    const int l31 = tz & 31, hh = (tz >> 5) & 1;
    float lt = xsum32(l1);
    float inv = 1.f / lt;
    char* ob = smem + (w < 2 ? A_OFF_A + A_BUFA : A_OFF_B + A_BUFB) + ((w & 1) * 32 + l31) * 256 + 8 * hh;
#pragma unroll
    for (int dt = 0; dt < 4; ++dt)
#pragma unroll
      for (int i = 0; i < 4; ++i) {
        u32x2 pk = {cvtpk(acc[dt][4 * i] * inv, acc[dt][4 * i + 1] * inv),
                    cvtpk(acc[dt][4 * i + 2] * inv, acc[dt][4 * i + 3] * inv)};
        *(u32x2*)(ob + (((4 * dt + i) ^ (l31 & 15)) << 4)) = pk;
      }
    if (tz == 0 && nidx >= 0) *slot = 4096u;
  }
  asm volatile("s_waitcnt lgkmcnt(0)" ::: "memory");
  RAW_BAR();
  {
    const int c = tz & 15, r0 = tz >> 4;
    short* orow = og + (size_t)(b * SQ + r0) * HD + h * DH + c * 8;
#pragma unroll
    for (int j = 0; j < 8; ++j) {
      const int r = r0 + 16 * j;
      const char* src = smem + (j < 4 ? A_OFF_A + A_BUFA + r * 256 : A_OFF_B + A_BUFB + (r - 64) * 256) + ((c ^ r0) << 4);
      bf16x8 v = *(const bf16x8*)src;
      *(bf16x8*)(orow + (size_t)(16 * j) * HD) = v;
    }
  }
  if (nidx < 0) break;
  pidx = nidx;
  }
}

constexpr int B_NST = 4;
constexpr int B_BUFA = 20480;
constexpr int B_BUFB = 8192;
constexpr int B_STRIDE = B_BUFA + B_BUFB;
constexpr int B_LDS = B_NST * B_STRIDE;

__global__ __launch_bounds__(512, 2) void oproj_kernel(const short* __restrict__ og,
                                                        const short* __restrict__ wo,
                                                        short* __restrict__ out) {
  extern __shared__ __attribute__((aligned(16))) char smem[];
  const int tid = threadIdx.x, lane = tid & 63;
  const int wv = __builtin_amdgcn_readfirstlane(tid >> 6);
  const int w = wv & 3;
  const int xg = blockIdx.x & 7, idx = blockIdx.x >> 3;
  const int b = xg * 4 + (idx >> 3), nt0 = idx & 7;
  const int l31 = lane & 31, hh = lane >> 5, l15 = lane & 15, g = lane >> 4;
  const int qp = l15 >> 2, p4 = lane & 3, blk = (lane >> 4) & 1;
  constexpr int NT = HD / 64;
  if (wv >= 4) {
    const char* ob = (const char*)(og + (size_t)b * SQ * HD);
    const char* wob = (const char*)(wo + nt0 * 64);
    unsigned osrc[5], wsrc[2];
#pragma unroll
    for (int ii = 0; ii < 5; ++ii) {
      int r = 8 * (w * 5 + ii) + (lane >> 3), cp = lane & 7;
      int rs = r < SQ ? r : SQ - 1;
      osrc[ii] = rs * (HD * 2) + ((cp ^ ((r >> 1) & 7)) << 4);
    }
#pragma unroll
    for (int ii = 0; ii < 2; ++ii) {
      int r = 8 * (w * 2 + ii) + (lane >> 3), cp = lane & 7;
      wsrc[ii] = r * (DM * 2) + ((cp ^ (((r >> 1) & 1) << 2)) << 4);
    }
    auto stage = [&](int t) {
      const int buf = t & 3;
      char* la = smem + buf * B_STRIDE + w * 5120;
      char* lb = smem + buf * B_STRIDE + B_BUFA + w * 2048;
      const char* ot = ob + t * 128;
      const char* wt = wob + (size_t)t * (64 * DM * 2);
#pragma unroll
      for (int ii = 0; ii < 5; ++ii) glds16(ot + osrc[ii], la + ii * 1024);
#pragma unroll
      for (int ii = 0; ii < 2; ++ii) glds16(wt + wsrc[ii], lb + ii * 1024);
    };
    stage(0); stage(1); stage(2);
    asm volatile("s_waitcnt vmcnt(14)" ::: "memory");
    for (int t = 0; t < NT; ++t) {
      RAW_BAR();
      if (t + 3 < NT) { stage(t + 3); asm volatile("s_waitcnt vmcnt(14)" ::: "memory"); }
      else if (t + 2 < NT) asm volatile("s_waitcnt vmcnt(7)" ::: "memory");
      else asm volatile("s_waitcnt vmcnt(0)" ::: "memory");
    }
    return;
  }
  f32x16 acc[2] = {f32x16{}, f32x16{}};
  f32x4 acc2 = f32x4{};
  const int sb = (int)(uintptr_t)smem;
  const int xa = sb + (32 * w + l31) * 128 + ((hh ^ ((l31 >> 1) & 7)) << 4);
  const int wa = sb + B_BUFA + (8 * hh + qp) * 128 + (((2 * blk + (p4 >> 1)) ^ ((qp >> 1) << 2)) << 4) + 8 * (p4 & 1);
  const int xa2 = sb + (128 + l15) * 128 + ((g ^ ((l15 >> 1) & 7)) << 4);
  const int wa2 = sb + B_BUFA + (8 * g + qp) * 128 + (((2 * w + (p4 >> 1)) ^ ((qp >> 1) << 2)) << 4) + 8 * (p4 & 1);
  auto body = [&](auto BC) {
    constexpr int buf = decltype(BC)::value;
    constexpr int OB = (buf & 1) * B_STRIDE;
    const int hi2 = (buf >> 1) * 2 * B_STRIDE;
    RAW_BAR();
    const int xa_ = xa + hi2, w0 = wa + hi2, w1 = (wa ^ 64) + hi2, xa2_ = xa2 + hi2, wa2_ = wa2 + hi2;
    bf16x8 xf[4], xr[2];
    s16x4 wl[4][2], wh[4][2], rl[2], rh[2];
    sfor<0, 4>([&](auto SC) {
      constexpr int s = decltype(SC)::value;
      xf[s] = rd128<OB>(xa_ ^ (s << 5));
      wl[s][0] = rdtr<OB + s * 2048>(w0); wh[s][0] = rdtr<OB + s * 2048 + 512>(w0);
      wl[s][1] = rdtr<OB + s * 2048>(w1); wh[s][1] = rdtr<OB + s * 2048 + 512>(w1);
      if constexpr (s >= 1) {
        asm volatile("s_waitcnt lgkmcnt(5)" : "+v"(xf[s - 1]), "+v"(wl[s - 1][0]), "+v"(wh[s - 1][0]), "+v"(wl[s - 1][1]), "+v"(wh[s - 1][1]) :: "memory");
        acc[0] = MFMA32(cat8(wl[s - 1][0], wh[s - 1][0]), xf[s - 1], acc[0]);
        acc[1] = MFMA32(cat8(wl[s - 1][1], wh[s - 1][1]), xf[s - 1], acc[1]);
      }
    });
    xr[0] = rd128<OB>(xa2_); rl[0] = rdtr<OB>(wa2_); rh[0] = rdtr<OB + 512>(wa2_);
    xr[1] = rd128<OB>(xa2_ ^ 64); rl[1] = rdtr<OB + 4096>(wa2_); rh[1] = rdtr<OB + 4096 + 512>(wa2_);
    asm volatile("s_waitcnt lgkmcnt(6)" : "+v"(xf[3]), "+v"(wl[3][0]), "+v"(wh[3][0]), "+v"(wl[3][1]), "+v"(wh[3][1]) :: "memory");
    acc[0] = MFMA32(cat8(wl[3][0], wh[3][0]), xf[3], acc[0]);
    acc[1] = MFMA32(cat8(wl[3][1], wh[3][1]), xf[3], acc[1]);
    asm volatile("s_waitcnt lgkmcnt(0)" : "+v"(xr[0]), "+v"(rl[0]), "+v"(rh[0]), "+v"(xr[1]), "+v"(rl[1]), "+v"(rh[1]) :: "memory");
    acc2 = MFMA16(cat8(rl[0], rh[0]), xr[0], acc2);
    acc2 = MFMA16(cat8(rl[1], rh[1]), xr[1], acc2);
  };
  for (int t = 0; t < NT; t += 4) {
    body(IC<0>{});
    body(IC<1>{});
    body(IC<2>{});
    body(IC<3>{});
  }
  {
    short* orow = out + (size_t)(b * SQ + 32 * w + l31) * DM + nt0 * 64 + 4 * hh;
#pragma unroll
    for (int nt = 0; nt < 2; ++nt)
#pragma unroll
      for (int i = 0; i < 4; ++i) {
        u32x2 pk = {cvtpk(acc[nt][4 * i], acc[nt][4 * i + 1]), cvtpk(acc[nt][4 * i + 2], acc[nt][4 * i + 3])};
        *(u32x2*)(orow + 32 * nt + 8 * i) = pk;
      }
    u32x2 pk = {cvtpk(acc2[0], acc2[1]), cvtpk(acc2[2], acc2[3])};
    *(u32x2*)(out + (size_t)(b * SQ + 128 + l15) * DM + nt0 * 64 + 16 * w + 4 * g) = pk;
  }
}

extern "C" void kernel_launch(void* const* d_in, const int* in_sizes, int n_in, void* d_out,
                              int out_size, void* d_ws, size_t ws_size, hipStream_t stream) {
  const short* x = (const short*)d_in[0];
  const short* kc = (const short*)d_in[1];
  const short* vc = (const short*)d_in[2];
  const short* wq = (const short*)d_in[3];
  const short* wo = (const short*)d_in[4];
  short* og = (short*)d_ws;
  short* out = (short*)d_out;
  unsigned* counter = (unsigned*)(og + (size_t)MROWS * HD);
  (void)hipFuncSetAttribute((const void*)attn_kernel, hipFuncAttributeMaxDynamicSharedMemorySize, A_LDS);
  (void)hipFuncSetAttribute((const void*)oproj_kernel, hipFuncAttributeMaxDynamicSharedMemorySize, B_LDS);
  attn_kernel<<<512, 256, A_LDS, stream>>>(x, wq, kc, vc, og, counter);
  oproj_kernel<<<256, 512, B_LDS, stream>>>(og, wo, out);
}
```

```cpp
#include <hip/hip_runtime.h>
#include <hip/hip_bf16.h>

typedef __attribute__((ext_vector_type(8))) short bf16x8;
typedef __attribute__((ext_vector_type(4))) short s16x4;
typedef __attribute__((ext_vector_type(16))) float f32x16;
typedef __attribute__((ext_vector_type(4))) float f32x4;
typedef __attribute__((ext_vector_type(4))) unsigned u32x4;
typedef __attribute__((ext_vector_type(2))) unsigned u32x2;

constexpr int NB = 32, SQ = 144, DM = 512, NH = 32, SK = 512, DH = 128;
constexpr int HD = NH * DH;
constexpr int MROWS = NB * SQ;

#define MFMA32(a, b, c) __builtin_amdgcn_mfma_f32_32x32x16_bf16((a), (b), (c), 0, 0, 0)
#define MFMA16(a, b, c) __builtin_amdgcn_mfma_f32_16x16x32_bf16((a), (b), (c), 0, 0, 0)
#define MFMA16K16(a, b, c) __builtin_amdgcn_mfma_f32_16x16x16bf16_1k((a), (b), (c), 0, 0, 0)

__device__ __forceinline__ unsigned cvtpk(float lo, float hi) {
  unsigned r;
  asm volatile("v_cvt_pk_bf16_f32 %0, %1, %2" : "=v"(r) : "v"(lo), "v"(hi));
  return r;
}
__device__ __forceinline__ bf16x8 cat8(s16x4 lo, s16x4 hi) {
  return __builtin_shufflevector(lo, hi, 0, 1, 2, 3, 4, 5, 6, 7);
}
__device__ __forceinline__ void glds16(const void* g, char* l) {
  __builtin_amdgcn_global_load_lds((const unsigned*)g, (unsigned*)l, 16, 0, 0);
}
__device__ __forceinline__ void glds16nt(const void* g, char* l) {
  __builtin_amdgcn_global_load_lds((const unsigned*)g, (unsigned*)l, 16, 0, 2);
}
__device__ __forceinline__ float xmax32(float v) {
  auto rr = __builtin_amdgcn_permlane32_swap(__float_as_uint(v), __float_as_uint(v), false, false);
  return fmaxf(__uint_as_float(rr[0]), __uint_as_float(rr[1]));
}
__device__ __forceinline__ float xsum32(float v) {
  auto rr = __builtin_amdgcn_permlane32_swap(__float_as_uint(v), __float_as_uint(v), false, false);
  return __uint_as_float(rr[0]) + __uint_as_float(rr[1]);
}
__device__ __forceinline__ float xmax16(float v) {
  auto rr = __builtin_amdgcn_permlane16_swap(__float_as_uint(v), __float_as_uint(v), false, false);
  return fmaxf(__uint_as_float(rr[0]), __uint_as_float(rr[1]));
}
__device__ __forceinline__ float xsum16(float v) {
  auto rr = __builtin_amdgcn_permlane16_swap(__float_as_uint(v), __float_as_uint(v), false, false);
  return __uint_as_float(rr[0]) + __uint_as_float(rr[1]);
}
template <int OFF> __device__ __forceinline__ bf16x8 rd128(int a) {
  bf16x8 r;
  asm volatile("ds_read_b128 %0, %1 offset:%2" : "=v"(r) : "v"(a), "i"(OFF) : "memory");
  return r;
}
template <int OFF> __device__ __forceinline__ s16x4 rdtr(int a) {
  s16x4 r;
  asm volatile("ds_read_b64_tr_b16 %0, %1 offset:%2" : "=v"(r) : "v"(a), "i"(OFF) : "memory");
  return r;
}
#define WAITL2(N, a, b) asm volatile("s_waitcnt lgkmcnt(" #N ")" : "+v"(a), "+v"(b) :: "memory")
#define WAITL4(N, a, b, c, d) asm volatile("s_waitcnt lgkmcnt(" #N ")" : "+v"(a), "+v"(b), "+v"(c), "+v"(d) :: "memory")
#define WAITL8(N, a, b, c, d, e, f, g_, h_) asm volatile("s_waitcnt lgkmcnt(" #N ")" : "+v"(a), "+v"(b), "+v"(c), "+v"(d), "+v"(e), "+v"(f), "+v"(g_), "+v"(h_) :: "memory")
#define RAW_BAR() __builtin_amdgcn_s_barrier()
#define SBAR() __builtin_amdgcn_sched_barrier(0)
#define OPAQUE(v) asm volatile("" : "+v"(v))
template <int N> struct IC { static constexpr int value = N; };
template <int I, int N, class F> __device__ __forceinline__ void sfor(F&& f) {
  if constexpr (I < N) { f(IC<I>{}); sfor<I + 1, N>(f); }
}

constexpr int X_BUFA = 20480;
constexpr int X_BUFB = 16384;
constexpr int A_BUFA = 16384;
constexpr int A_BUFB = 16384;
constexpr int X_OFF_A = 0;
constexpr int X_OFF_B = 2 * X_BUFA;
constexpr int A_OFF_A = X_OFF_B + 2 * X_BUFB;
constexpr int A_OFF_B = A_OFF_A + 2 * A_BUFA;
constexpr int A_OFF_Q2 = A_OFF_B + 2 * A_BUFB;
constexpr int A_OFF_SC = A_OFF_Q2 + 8192;
constexpr int A_LDS = A_OFF_SC + 16384;

__global__ __launch_bounds__(512, 2) void attn_kernel(
    const short* __restrict__ x, const short* __restrict__ wq, const short* __restrict__ kc,
    const short* __restrict__ vc, short* __restrict__ og) {
  extern __shared__ __attribute__((aligned(16))) char smem[];
  const int tid = threadIdx.x, lane = tid & 63;
  const int wv = __builtin_amdgcn_readfirstlane(tid >> 6);
  const int team = wv >> 2, w = wv & 3;
  const int l31 = lane & 31, hh = lane >> 5, l15 = lane & 15, g = lane >> 4;
  const int qp = l15 >> 2, p4 = lane & 3, blk = (lane >> 4) & 1;
  const int sb = (int)(uintptr_t)smem;
  const int pbase = (blockIdx.x & 7) * 128 + (blockIdx.x >> 3) * 4 + team * 2;
  const int q2off = A_OFF_Q2 + team * 4096;

  const unsigned ksrc = (16 * w + g) * 256 + ((l15 ^ g) << 4);
  const unsigned vsrc = (16 * w + g) * 256 + ((l15 ^ (g << 2)) << 4);
  auto stage_kv = [&](int pi, int t, int buf, unsigned ko, unsigned vo) {
    const char* kb = (const char*)kc + (size_t)pi * (SK * DH * 2) + t * 16384;
    const char* vb = (const char*)vc + (size_t)pi * (SK * DH * 2) + t * 16384;
#pragma unroll
    for (int i = 0; i < 4; ++i)
      glds16nt(kb + ((ko ^ (i << 6)) + i * 1024), smem + A_OFF_A + buf * A_BUFA + w * 4096 + i * 1024);
#pragma unroll
    for (int i = 0; i < 4; ++i)
      glds16nt(vb + (vo + i * 1024), smem + A_OFF_B + buf * A_BUFB + w * 4096 + i * 1024);
  };
  auto stage_q = [&](int pi, int t, int buf) {
    int ln = lane;
    OPAQUE(ln);
    const char* xt = (const char*)(x + (size_t)(pi >> 5) * SQ * DM) + t * 128;
    const char* wt = (const char*)(wq + (pi & 31) * DH) + (size_t)t * (64 * HD * 2);
#pragma unroll
    for (int ii = 0; ii < 5; ++ii) {
      int r = 8 * (w * 5 + ii) + (ln >> 3), cp = ln & 7;
      int rs = r < SQ ? r : SQ - 1;
      unsigned so = rs * 1024 + ((cp ^ ((r >> 1) & 7)) << 4);
      glds16(xt + so, smem + X_OFF_A + buf * X_BUFA + w * 5120 + ii * 1024);
    }
    const unsigned wo0 = (16 * w + (ln >> 4)) * (HD * 2) + (((ln & 15) ^ ((ln >> 4) << 2)) << 4);
#pragma unroll
    for (int ii = 0; ii < 4; ++ii)
      glds16(wt + (wo0 + ii * 4 * HD * 2), smem + X_OFF_B + buf * X_BUFB + w * 4096 + ii * 1024);
  };
  constexpr float CS = 0.088388347648318440f * 1.4426950408889634f;
  constexpr float THRS = 8.f / CS;

  f32x16 acc[4];
  f32x4 acc2[8];
  bf16x8 qf[8];
  float pm = 0.f, pl = 0.f;
  int pend = -1;
#pragma unroll
  for (int i = 0; i < 8; ++i) { acc2[i] = f32x4{}; qf[i] = bf16x8{}; }

  auto merge_hook = [&](int step) {
    if (pend < 0 || step > 1) return;
    int lz = lane;
    OPAQUE(lz);
    const int q = lz & 15, gg = lz >> 4;
    if (step == 0 ? (w < 2) : (w == 0)) {
      const float* reg = (const float*)(smem + A_OFF_SC + (step == 0 ? w : 1) * 8192);
      const float* stt = (const float*)(smem + q2off) + (step == 0 ? w : 2) * 32;
      const float om = stt[q], ol = stt[16 + q];
      const float mm = fmaxf(pm, om);
      const float sa = __builtin_amdgcn_exp2f((pm - mm) * CS), so = __builtin_amdgcn_exp2f((om - mm) * CS);
#pragma unroll
      for (int dd = 0; dd < 8; ++dd) {
        f32x4 o = *(const f32x4*)(reg + q * 128 + 16 * dd + 4 * gg);
#pragma unroll
        for (int r = 0; r < 4; ++r) acc2[dd][r] = acc2[dd][r] * sa + o[r] * so;
      }
      pl = pl * sa + ol * so;
      pm = mm;
      if (step == 0 && w == 1) {
        float* wr = (float*)(smem + A_OFF_SC + 8192);
#pragma unroll
        for (int dd = 0; dd < 8; ++dd) *(f32x4*)(wr + q * 128 + 16 * dd + 4 * gg) = acc2[dd];
        float* sto = (float*)(smem + q2off) + 64;
        if (gg == 0) { sto[q] = pm; sto[16 + q] = pl; }
      }
      if (step == 1) {
        const float inv = 1.f / pl;
        short* orow = og + (size_t)((pend >> 5) * SQ + 128 + q) * HD + (pend & 31) * DH + 4 * gg;
#pragma unroll
        for (int dd = 0; dd < 8; ++dd) {
          u32x2 pk = {cvtpk(acc2[dd][0] * inv, acc2[dd][1] * inv), cvtpk(acc2[dd][2] * inv, acc2[dd][3] * inv)};
          *(u32x2*)(orow + 16 * dd) = pk;
        }
      }
    }
    if (step == 1) pend = -1;
  };

  if (team == 0) {
    stage_q(pbase, 0, 0);
  } else {
    for (int t = 0; t < 8; ++t) {
      RAW_BAR();
      if (t == 7) stage_q(pbase, 0, 0);
    }
  }
  for (int ph = 0; ph < 2; ++ph) {
    const int pidx = pbase + ph;
    {
#pragma unroll
      for (int i = 0; i < 4; ++i) acc[i] = f32x16{};
      f32x4 aq2[2] = {f32x4{}, f32x4{}};
      int lq = lane;
      OPAQUE(lq);
      const int l31 = lq & 31, hh = lq >> 5, l15 = lq & 15, g = lq >> 4;
      const int qp = l15 >> 2, p4 = lq & 3, blk = (lq >> 4) & 1;
      const int xa = sb + X_OFF_A + (32 * w + l31) * 128 + ((hh ^ ((l31 >> 1) & 7)) << 4);
      const int wa = sb + X_OFF_B + (8 * hh + qp) * 256 + (((2 * blk + (p4 >> 1)) ^ (qp << 2)) << 4) + 8 * (p4 & 1);
      const int xa2 = sb + X_OFF_A + (128 + l15) * 128 + ((g ^ ((l15 >> 1) & 7)) << 4);
      const int wa2 = sb + X_OFF_B + (8 * g + qp) * 256 + (((4 * w + (p4 >> 1)) ^ (qp << 2)) << 4) + 8 * (p4 & 1);
      auto body = [&](auto BC, int t) {
        constexpr int buf = decltype(BC)::value;
        constexpr int OA = buf * X_BUFA, OB = buf * X_BUFB;
        asm volatile("s_waitcnt vmcnt(0) lgkmcnt(0)" ::: "memory");
        RAW_BAR();
        merge_hook(t);
        if (t < 7) stage_q(pidx, t + 1, buf ^ 1);
        else { unsigned ko = ksrc, vo = vsrc; OPAQUE(ko); OPAQUE(vo); stage_kv(pidx, 0, 0, ko, vo); }
        SBAR();
        const int w0 = wa, w1 = wa ^ 64, w2 = wa ^ 128, w3 = wa ^ 192;
        bf16x8 xf[2];
        s16x4 wl[2][4], wh[2][4];
        bf16x8 xr[2];
        s16x4 rl[2][2], rh[2][2];
        auto issue = [&](auto SC) {
          constexpr int s = decltype(SC)::value, u = s & 1;
          xf[u] = rd128<OA>(xa ^ (s << 5));
          wl[u][0] = rdtr<OB + s * 4096>(w0); wh[u][0] = rdtr<OB + s * 4096 + 1024>(w0);
          wl[u][1] = rdtr<OB + s * 4096>(w1); wh[u][1] = rdtr<OB + s * 4096 + 1024>(w1);
          wl[u][2] = rdtr<OB + s * 4096>(w2); wh[u][2] = rdtr<OB + s * 4096 + 1024>(w2);
          wl[u][3] = rdtr<OB + s * 4096>(w3); wh[u][3] = rdtr<OB + s * 4096 + 1024>(w3);
        };
        issue(IC<0>{});
        sfor<0, 4>([&](auto SC) {
          constexpr int s = decltype(SC)::value, u = s & 1;
          if constexpr (s < 3) {
            issue(IC<s + 1>{});
            asm volatile("s_waitcnt lgkmcnt(9)" : "+v"(xf[u]), "+v"(wl[u][0]), "+v"(wh[u][0]), "+v"(wl[u][1]), "+v"(wh[u][1]),
                         "+v"(wl[u][2]), "+v"(wh[u][2]), "+v"(wl[u][3]), "+v"(wh[u][3]) :: "memory");
          } else {
            xr[0] = rd128<OA>(xa2); xr[1] = rd128<OA>(xa2 ^ 64);
            rl[0][0] = rdtr<OB>(wa2); rh[0][0] = rdtr<OB + 1024>(wa2);
            rl[0][1] = rdtr<OB>(wa2 ^ 32); rh[0][1] = rdtr<OB + 1024>(wa2 ^ 32);
            rl[1][0] = rdtr<OB + 8192>(wa2); rh[1][0] = rdtr<OB + 8192 + 1024>(wa2);
            rl[1][1] = rdtr<OB + 8192>(wa2 ^ 32); rh[1][1] = rdtr<OB + 8192 + 1024>(wa2 ^ 32);
            asm volatile("s_waitcnt lgkmcnt(10)" : "+v"(xf[u]), "+v"(wl[u][0]), "+v"(wh[u][0]), "+v"(wl[u][1]), "+v"(wh[u][1]),
                         "+v"(wl[u][2]), "+v"(wh[u][2]), "+v"(wl[u][3]), "+v"(wh[u][3]) :: "memory");
          }
#pragma unroll
          for (int dt = 0; dt < 4; ++dt) acc[dt] = MFMA32(cat8(wl[u][dt], wh[u][dt]), xf[u], acc[dt]);
          SBAR();
        });
        asm volatile("s_waitcnt lgkmcnt(0)" : "+v"(xr[0]), "+v"(xr[1]), "+v"(rl[0][0]), "+v"(rh[0][0]), "+v"(rl[0][1]), "+v"(rh[0][1]),
                     "+v"(rl[1][0]), "+v"(rh[1][0]), "+v"(rl[1][1]), "+v"(rh[1][1]) :: "memory");
#pragma unroll
        for (int s2 = 0; s2 < 2; ++s2)
#pragma unroll
          for (int e = 0; e < 2; ++e) aq2[e] = MFMA16(cat8(rl[s2][e], rh[s2][e]), xr[s2], aq2[e]);
      };
      for (int t = 0; t < 8; t += 2) {
        body(IC<0>{}, t);
        body(IC<1>{}, t + 1);
      }
#pragma unroll
      for (int dt = 0; dt < 4; ++dt) {
#pragma unroll
        for (int s = 0; s < 2; ++s) {
          unsigned a0 = cvtpk(acc[dt][8 * s + 0], acc[dt][8 * s + 1]);
          unsigned a1 = cvtpk(acc[dt][8 * s + 2], acc[dt][8 * s + 3]);
          unsigned b0 = cvtpk(acc[dt][8 * s + 4], acc[dt][8 * s + 5]);
          unsigned b1 = cvtpk(acc[dt][8 * s + 6], acc[dt][8 * s + 7]);
          auto r0 = __builtin_amdgcn_permlane32_swap(a0, b0, false, false);
          auto r1 = __builtin_amdgcn_permlane32_swap(a1, b1, false, false);
          u32x4 wv4 = {r0[0], r1[0], r0[1], r1[1]};
          qf[2 * dt + s] = __builtin_bit_cast(bf16x8, wv4);
        }
      }
#pragma unroll
      for (int e = 0; e < 2; ++e) {
        int dd = 2 * w + e;
        u32x2 pk = {cvtpk(aq2[e][0], aq2[e][1]), cvtpk(aq2[e][2], aq2[e][3])};
        int ch = (2 * dd + (g >> 1)) ^ l15;
        asm volatile("ds_write_b64 %0, %1" :: "v"(sb + q2off + l15 * 256 + (ch << 4) + 8 * (g & 1)), "v"(pk) : "memory");
      }
    }
    {
#pragma unroll
      for (int i = 0; i < 4; ++i) acc[i] = f32x16{};
#pragma unroll
      for (int i = 0; i < 8; ++i) acc2[i] = f32x4{};
      float m1 = -1e30f, l1 = 0.f, m2 = -1e30f, l2 = 0.f;
      int ka = sb + A_OFF_A + l31 * 256 + ((hh ^ l15) << 4);
      int ka2 = sb + A_OFF_A + (16 * w + l15) * 256 + ((g ^ l15) << 4);
      int q2a = sb + q2off + l15 * 256 + ((g ^ l15) << 4);
      int va = sb + A_OFF_B + (4 * hh + qp) * 256 + (((2 * blk + (p4 >> 1)) ^ (qp << 2)) << 4) + 8 * (p4 & 1);
      int va2 = sb + A_OFF_B + (16 * w + 4 * g + qp) * 256 + (((p4 >> 1) ^ (qp << 2)) << 4) + 8 * (p4 & 1);
      auto body = [&](auto BC, int t) {
      constexpr int buf = decltype(BC)::value;
      constexpr int OA = buf * A_BUFA, OB = buf * A_BUFB;
      asm volatile("s_waitcnt vmcnt(0) lgkmcnt(0)" ::: "memory");
      RAW_BAR();
      int ka_ = ka, ka2_ = ka2, q2a_ = q2a, va_ = va, va2_ = va2;
      unsigned ko = ksrc, vo = vsrc;
      OPAQUE(ka_); OPAQUE(ka2_); OPAQUE(q2a_); OPAQUE(va_); OPAQUE(va2_); OPAQUE(ko); OPAQUE(vo);
      if (t < 7) stage_kv(pidx, t + 1, buf ^ 1, ko, vo);
      else if (ph == 0) stage_q(pidx + 1, 0, 0);
      SBAR();
      f32x16 s0 = f32x16{}, s1 = f32x16{};
      f32x4 sr = f32x4{};
      bf16x8 kA[4], kB[4];
#pragma unroll
      for (int i = 0; i < 4; ++i) kA[i] = rd128<OA>(ka_ ^ (i << 5));
#pragma unroll
      for (int i = 0; i < 4; ++i) kB[i] = rd128<OA>(ka_ ^ ((4 + i) << 5));
      WAITL4(4, kA[0], kA[1], kA[2], kA[3]);
      s0 = MFMA32(kA[0], qf[0], s0);
      s0 = MFMA32(kA[1], qf[1], s0);
      SBAR();
      s0 = MFMA32(kA[2], qf[2], s0);
      s0 = MFMA32(kA[3], qf[3], s0);
      SBAR();
#pragma unroll
      for (int i = 0; i < 4; ++i) kA[i] = rd128<OA + 8192>(ka_ ^ (i << 5));
      WAITL4(4, kB[0], kB[1], kB[2], kB[3]);
      s0 = MFMA32(kB[0], qf[4], s0);
      s0 = MFMA32(kB[1], qf[5], s0);
      SBAR();
      s0 = MFMA32(kB[2], qf[6], s0);
      s0 = MFMA32(kB[3], qf[7], s0);
      SBAR();
#pragma unroll
      for (int i = 0; i < 4; ++i) kB[i] = rd128<OA + 8192>(ka_ ^ ((4 + i) << 5));
      WAITL4(4, kA[0], kA[1], kA[2], kA[3]);
      s1 = MFMA32(kA[0], qf[0], s1);
      SBAR();
      s1 = MFMA32(kA[1], qf[1], s1);
      float tmax = fmaxf(fmaxf(s0[0], s0[1]), s0[2]);
#pragma unroll
      for (int r = 3; r < 9; r += 2) tmax = fmaxf(fmaxf(tmax, s0[r]), s0[r + 1]);
      SBAR();
      s1 = MFMA32(kA[2], qf[2], s1);
#pragma unroll
      for (int r = 9; r < 15; r += 2) tmax = fmaxf(fmaxf(tmax, s0[r]), s0[r + 1]);
      tmax = fmaxf(tmax, s0[15]);
      tmax = xmax32(tmax);
      SBAR();
      s1 = MFMA32(kA[3], qf[3], s1);
      SBAR();
      if (!__all(tmax - m1 <= THRS)) {
        float mn0 = fmaxf(m1, tmax);
        float alpha = __builtin_amdgcn_exp2f((m1 - mn0) * CS);
        m1 = mn0;
        l1 *= alpha;
#pragma unroll
        for (int dt = 0; dt < 4; ++dt)
#pragma unroll
          for (int r = 0; r < 16; ++r) acc[dt][r] *= alpha;
      }
      const float mn = m1;
      bf16x8 kr[4], qr[4];
#pragma unroll
      for (int ks = 0; ks < 4; ++ks) { kr[ks] = rd128<OA>(ka2_ ^ (ks << 6)); qr[ks] = rd128<0>(q2a_ ^ (ks << 6)); }
      WAITL4(8, kB[0], kB[1], kB[2], kB[3]);
      bf16x8 pf[4];
      float mc = -mn * CS;
      float ps = 0.f;
      float e[8];
      s1 = MFMA32(kB[0], qf[4], s1);
#pragma unroll
      for (int j = 0; j < 4; ++j) { e[j] = __builtin_amdgcn_exp2f(fmaf(s0[j], CS, mc)); ps += e[j]; }
      SBAR();
      s1 = MFMA32(kB[1], qf[5], s1);
#pragma unroll
      for (int j = 4; j < 8; ++j) { e[j] = __builtin_amdgcn_exp2f(fmaf(s0[j], CS, mc)); ps += e[j]; }
      { u32x4 a = {cvtpk(e[0], e[1]), cvtpk(e[2], e[3]), cvtpk(e[4], e[5]), cvtpk(e[6], e[7])}; pf[0] = __builtin_bit_cast(bf16x8, a); }
      SBAR();
      s1 = MFMA32(kB[2], qf[6], s1);
#pragma unroll
      for (int j = 0; j < 4; ++j) { e[j] = __builtin_amdgcn_exp2f(fmaf(s0[8 + j], CS, mc)); ps += e[j]; }
      SBAR();
      s1 = MFMA32(kB[3], qf[7], s1);
#pragma unroll
      for (int j = 4; j < 8; ++j) { e[j] = __builtin_amdgcn_exp2f(fmaf(s0[8 + j], CS, mc)); ps += e[j]; }
      { u32x4 a = {cvtpk(e[0], e[1]), cvtpk(e[2], e[3]), cvtpk(e[4], e[5]), cvtpk(e[6], e[7])}; pf[1] = __builtin_bit_cast(bf16x8, a); }
      l1 += ps;
      OPAQUE(l1);
      SBAR();
      SBAR();
      const int v0 = va_, v1 = va_ ^ 64, v2 = va_ ^ 128, v3 = va_ ^ 192;
      s16x4 vl[2][4], vh[2][4];
      auto issue_v = [&](auto SC) {
        constexpr int s = decltype(SC)::value, u = s & 1;
        vl[u][0] = rdtr<OB + s * 4096>(v0); vh[u][0] = rdtr<OB + s * 4096 + 2048>(v0);
        vl[u][1] = rdtr<OB + s * 4096>(v1); vh[u][1] = rdtr<OB + s * 4096 + 2048>(v1);
        vl[u][2] = rdtr<OB + s * 4096>(v2); vh[u][2] = rdtr<OB + s * 4096 + 2048>(v2);
        vl[u][3] = rdtr<OB + s * 4096>(v3); vh[u][3] = rdtr<OB + s * 4096 + 2048>(v3);
      };
      issue_v(IC<0>{});
      WAITL8(8, kr[0], kr[1], kr[2], kr[3], qr[0], qr[1], qr[2], qr[3]);
#pragma unroll
      for (int ks = 0; ks < 4; ++ks) sr = MFMA16(kr[ks], qr[ks], sr);
      SBAR();
      SBAR();
      issue_v(IC<1>{});
      WAITL8(8, vl[0][0], vh[0][0], vl[0][1], vh[0][1], vl[0][2], vh[0][2], vl[0][3], vh[0][3]);
      acc[0] = MFMA32(cat8(vl[0][0], vh[0][0]), pf[0], acc[0]);
      float tm1 = fmaxf(fmaxf(s1[0], s1[1]), s1[2]);
#pragma unroll
      for (int r = 3; r < 9; r += 2) tm1 = fmaxf(fmaxf(tm1, s1[r]), s1[r + 1]);
      SBAR();
      acc[1] = MFMA32(cat8(vl[0][1], vh[0][1]), pf[0], acc[1]);
#pragma unroll
      for (int r = 9; r < 15; r += 2) tm1 = fmaxf(fmaxf(tm1, s1[r]), s1[r + 1]);
      tm1 = fmaxf(tm1, s1[15]);
      tm1 = xmax32(tm1);
      const bool keepb = __all(tm1 - m1 <= THRS);
      float mnb = keepb ? m1 : fmaxf(m1, tm1);
      float alphab = keepb ? 1.f : __builtin_amdgcn_exp2f((m1 - mnb) * CS);
      m1 = mnb;
      mc = -mnb * CS;
      ps = 0.f;
      SBAR();
      acc[2] = MFMA32(cat8(vl[0][2], vh[0][2]), pf[0], acc[2]);
#pragma unroll
      for (int j = 0; j < 4; ++j) { e[j] = __builtin_amdgcn_exp2f(fmaf(s1[j], CS, mc)); ps += e[j]; }
      SBAR();
      acc[3] = MFMA32(cat8(vl[0][3], vh[0][3]), pf[0], acc[3]);
#pragma unroll
      for (int j = 4; j < 8; ++j) { e[j] = __builtin_amdgcn_exp2f(fmaf(s1[j], CS, mc)); ps += e[j]; }
      { u32x4 a = {cvtpk(e[0], e[1]), cvtpk(e[2], e[3]), cvtpk(e[4], e[5]), cvtpk(e[6], e[7])}; pf[2] = __builtin_bit_cast(bf16x8, a); }
      SBAR();
      issue_v(IC<2>{});
      WAITL8(8, vl[1][0], vh[1][0], vl[1][1], vh[1][1], vl[1][2], vh[1][2], vl[1][3], vh[1][3]);
      acc[0] = MFMA32(cat8(vl[1][0], vh[1][0]), pf[1], acc[0]);
#pragma unroll
      for (int j = 0; j < 4; ++j) { e[j] = __builtin_amdgcn_exp2f(fmaf(s1[8 + j], CS, mc)); ps += e[j]; }
      SBAR();
      acc[1] = MFMA32(cat8(vl[1][1], vh[1][1]), pf[1], acc[1]);
#pragma unroll
      for (int j = 4; j < 8; ++j) { e[j] = __builtin_amdgcn_exp2f(fmaf(s1[8 + j], CS, mc)); ps += e[j]; }
      { u32x4 a = {cvtpk(e[0], e[1]), cvtpk(e[2], e[3]), cvtpk(e[4], e[5]), cvtpk(e[6], e[7])}; pf[3] = __builtin_bit_cast(bf16x8, a); }
      l1 = l1 * alphab + ps;
      OPAQUE(l1);
      SBAR();
      acc[2] = MFMA32(cat8(vl[1][2], vh[1][2]), pf[1], acc[2]);
      float t2 = fmaxf(fmaxf(sr[0], sr[1]), fmaxf(sr[2], sr[3]));
      t2 = xmax16(t2);
      t2 = xmax32(t2);
      const bool keep2 = __all(t2 - m2 <= THRS);
      float mn2 = keep2 ? m2 : fmaxf(m2, t2);
      float alpha2 = keep2 ? 1.f : __builtin_amdgcn_exp2f((m2 - mn2) * CS);
      m2 = mn2;
      SBAR();
      acc[3] = MFMA32(cat8(vl[1][3], vh[1][3]), pf[1], acc[3]);
      const float mc2 = -mn2 * CS;
      float e0 = __builtin_amdgcn_exp2f(fmaf(sr[0], CS, mc2));
      float e1 = __builtin_amdgcn_exp2f(fmaf(sr[1], CS, mc2));
      float e2 = __builtin_amdgcn_exp2f(fmaf(sr[2], CS, mc2));
      float e3 = __builtin_amdgcn_exp2f(fmaf(sr[3], CS, mc2));
      l2 = l2 * alpha2 + (e0 + e1) + (e2 + e3);
      OPAQUE(l2);
      u32x2 p2u = {cvtpk(e0, e1), cvtpk(e2, e3)};
      s16x4 p2 = __builtin_bit_cast(s16x4, p2u);
      SBAR();
      if (!keepb) {
#pragma unroll
        for (int dt = 0; dt < 4; ++dt)
#pragma unroll
          for (int r = 0; r < 16; ++r) acc[dt][r] *= alphab;
      }
      if (!keep2) {
#pragma unroll
        for (int dd = 0; dd < 8; ++dd)
#pragma unroll
          for (int r = 0; r < 4; ++r) acc2[dd][r] *= alpha2;
      }
      issue_v(IC<3>{});
      WAITL8(8, vl[0][0], vh[0][0], vl[0][1], vh[0][1], vl[0][2], vh[0][2], vl[0][3], vh[0][3]);
#pragma unroll
      for (int dt = 0; dt < 4; ++dt) acc[dt] = MFMA32(cat8(vl[0][dt], vh[0][dt]), pf[2], acc[dt]);
      SBAR();
      s16x4 vr[8];
#pragma unroll
      for (int dd = 0; dd < 8; ++dd) vr[dd] = rdtr<OB>(va2_ ^ (dd << 5));
      WAITL8(8, vl[1][0], vh[1][0], vl[1][1], vh[1][1], vl[1][2], vh[1][2], vl[1][3], vh[1][3]);
#pragma unroll
      for (int dt = 0; dt < 4; ++dt) acc[dt] = MFMA32(cat8(vl[1][dt], vh[1][dt]), pf[3], acc[dt]);
      WAITL8(0, vr[0], vr[1], vr[2], vr[3], vr[4], vr[5], vr[6], vr[7]);
#pragma unroll
      for (int dd = 0; dd < 8; ++dd) acc2[dd] = MFMA16K16(vr[dd], p2, acc2[dd]);
      };
      for (int t = 0; t < 8; t += 2) {
        body(IC<0>{}, t);
        body(IC<1>{}, t + 1);
      }
      int tz = lane;
      OPAQUE(tz);
      {
        const int r31 = tz & 31, h2 = tz >> 5;
        const float inv = 1.f / xsum32(l1);
        short* orow = og + (size_t)((pidx >> 5) * SQ + 32 * w + r31) * HD + (pidx & 31) * DH + 8 * h2;
#pragma unroll
        for (int dt = 0; dt < 4; ++dt)
#pragma unroll
          for (int i = 0; i < 4; i += 2) {
            unsigned a0 = cvtpk(acc[dt][4 * i] * inv, acc[dt][4 * i + 1] * inv), a1 = cvtpk(acc[dt][4 * i + 2] * inv, acc[dt][4 * i + 3] * inv);
            unsigned b0 = cvtpk(acc[dt][4 * i + 4] * inv, acc[dt][4 * i + 5] * inv), b1 = cvtpk(acc[dt][4 * i + 6] * inv, acc[dt][4 * i + 7] * inv);
            auto r0 = __builtin_amdgcn_permlane32_swap(a0, b0, false, false);
            auto r1 = __builtin_amdgcn_permlane32_swap(a1, b1, false, false);
            u32x4 pk = {r0[0], r1[0], r0[1], r1[1]};
            *(u32x4*)(orow + 32 * dt + 8 * i) = pk;
          }
      }
      {
        const int q = tz & 15, gg = tz >> 4;
        const float lt2 = xsum32(xsum16(l2));
        if (w >= 2) {
          float* wr = (float*)(smem + A_OFF_SC + (w - 2) * 8192);
#pragma unroll
          for (int dd = 0; dd < 8; ++dd) *(f32x4*)(wr + q * 128 + 16 * dd + 4 * gg) = acc2[dd];
          float* sto = (float*)(smem + q2off) + (w - 2) * 32;
          if (gg == 0) { sto[q] = m2; sto[16 + q] = lt2; }
        }
        pm = m2; pl = lt2; pend = pidx;
      }
    }
  }
  const int nfin = team == 0 ? 10 : 2;
  for (int t = 0; t < nfin; ++t) {
    asm volatile("s_waitcnt vmcnt(0) lgkmcnt(0)" ::: "memory");
    RAW_BAR();
    merge_hook(t);
  }
}

constexpr int B_NST = 4;
constexpr int B_BUFA = 20480;
constexpr int B_BUFB = 8192;
constexpr int B_STRIDE = B_BUFA + B_BUFB;
constexpr int B_LDS = B_NST * B_STRIDE;

__global__ __launch_bounds__(512, 2) void oproj_kernel(const short* __restrict__ og,
                                                        const short* __restrict__ wo,
                                                        short* __restrict__ out) {
  extern __shared__ __attribute__((aligned(16))) char smem[];
  const int tid = threadIdx.x, lane = tid & 63;
  const int wv = __builtin_amdgcn_readfirstlane(tid >> 6);
  const int w = wv & 3;
  const int xg = blockIdx.x & 7, idx = blockIdx.x >> 3;
  const int b = xg * 4 + (idx >> 3), nt0 = idx & 7;
  const int l31 = lane & 31, hh = lane >> 5, l15 = lane & 15, g = lane >> 4;
  const int qp = l15 >> 2, p4 = lane & 3, blk = (lane >> 4) & 1;
  constexpr int NT = HD / 64;
  if (wv >= 4) {
    const char* ob = (const char*)(og + (size_t)b * SQ * HD);
    const char* wob = (const char*)(wo + nt0 * 64);
    unsigned osrc[5], wsrc[2];
#pragma unroll
    for (int ii = 0; ii < 5; ++ii) {
      int r = 8 * (w * 5 + ii) + (lane >> 3), cp = lane & 7;
      int rs = r < SQ ? r : SQ - 1;
      osrc[ii] = rs * (HD * 2) + ((cp ^ ((r >> 1) & 7)) << 4);
    }
#pragma unroll
    for (int ii = 0; ii < 2; ++ii) {
      int r = 8 * (w * 2 + ii) + (lane >> 3), cp = lane & 7;
      wsrc[ii] = r * (DM * 2) + ((cp ^ (((r >> 1) & 1) << 2)) << 4);
    }
    auto stage = [&](int t) {
      const int buf = t & 3;
      char* la = smem + buf * B_STRIDE + w * 5120;
      char* lb = smem + buf * B_STRIDE + B_BUFA + w * 2048;
      const char* ot = ob + t * 128;
      const char* wt = wob + (size_t)t * (64 * DM * 2);
#pragma unroll
      for (int ii = 0; ii < 5; ++ii) glds16(ot + osrc[ii], la + ii * 1024);
#pragma unroll
      for (int ii = 0; ii < 2; ++ii) glds16(wt + wsrc[ii], lb + ii * 1024);
    };
    stage(0); stage(1); stage(2);
    asm volatile("s_waitcnt vmcnt(14)" ::: "memory");
    for (int t = 0; t < NT; ++t) {
      RAW_BAR();
      if (t + 3 < NT) { stage(t + 3); asm volatile("s_waitcnt vmcnt(14)" ::: "memory"); }
      else if (t + 2 < NT) asm volatile("s_waitcnt vmcnt(7)" ::: "memory");
      else asm volatile("s_waitcnt vmcnt(0)" ::: "memory");
    }
    return;
  }
  f32x16 acc[2] = {f32x16{}, f32x16{}};
  f32x4 acc2 = f32x4{};
  const int sb = (int)(uintptr_t)smem;
  const int xa = sb + (32 * w + l31) * 128 + ((hh ^ ((l31 >> 1) & 7)) << 4);
  const int wa = sb + B_BUFA + (8 * hh + qp) * 128 + (((2 * blk + (p4 >> 1)) ^ ((qp >> 1) << 2)) << 4) + 8 * (p4 & 1);
  const int xa2 = sb + (128 + l15) * 128 + ((g ^ ((l15 >> 1) & 7)) << 4);
  const int wa2 = sb + B_BUFA + (8 * g + qp) * 128 + (((2 * w + (p4 >> 1)) ^ ((qp >> 1) << 2)) << 4) + 8 * (p4 & 1);
  auto body = [&](auto BC) {
    constexpr int buf = decltype(BC)::value;
    constexpr int OB = (buf & 1) * B_STRIDE;
    const int hi2 = (buf >> 1) * 2 * B_STRIDE;
    RAW_BAR();
    const int xa_ = xa + hi2, w0 = wa + hi2, w1 = (wa ^ 64) + hi2, xa2_ = xa2 + hi2, wa2_ = wa2 + hi2;
    bf16x8 xf[4], xr[2];
    s16x4 wl[4][2], wh[4][2], rl[2], rh[2];
    sfor<0, 4>([&](auto SC) {
      constexpr int s = decltype(SC)::value;
      xf[s] = rd128<OB>(xa_ ^ (s << 5));
      wl[s][0] = rdtr<OB + s * 2048>(w0); wh[s][0] = rdtr<OB + s * 2048 + 512>(w0);
      wl[s][1] = rdtr<OB + s * 2048>(w1); wh[s][1] = rdtr<OB + s * 2048 + 512>(w1);
      if constexpr (s >= 1) {
        asm volatile("s_waitcnt lgkmcnt(5)" : "+v"(xf[s - 1]), "+v"(wl[s - 1][0]), "+v"(wh[s - 1][0]), "+v"(wl[s - 1][1]), "+v"(wh[s - 1][1]) :: "memory");
        acc[0] = MFMA32(cat8(wl[s - 1][0], wh[s - 1][0]), xf[s - 1], acc[0]);
        acc[1] = MFMA32(cat8(wl[s - 1][1], wh[s - 1][1]), xf[s - 1], acc[1]);
      }
    });
    xr[0] = rd128<OB>(xa2_); rl[0] = rdtr<OB>(wa2_); rh[0] = rdtr<OB + 512>(wa2_);
    xr[1] = rd128<OB>(xa2_ ^ 64); rl[1] = rdtr<OB + 4096>(wa2_); rh[1] = rdtr<OB + 4096 + 512>(wa2_);
    asm volatile("s_waitcnt lgkmcnt(6)" : "+v"(xf[3]), "+v"(wl[3][0]), "+v"(wh[3][0]), "+v"(wl[3][1]), "+v"(wh[3][1]) :: "memory");
    acc[0] = MFMA32(cat8(wl[3][0], wh[3][0]), xf[3], acc[0]);
    acc[1] = MFMA32(cat8(wl[3][1], wh[3][1]), xf[3], acc[1]);
    asm volatile("s_waitcnt lgkmcnt(0)" : "+v"(xr[0]), "+v"(rl[0]), "+v"(rh[0]), "+v"(xr[1]), "+v"(rl[1]), "+v"(rh[1]) :: "memory");
    acc2 = MFMA16(cat8(rl[0], rh[0]), xr[0], acc2);
    acc2 = MFMA16(cat8(rl[1], rh[1]), xr[1], acc2);
  };
  for (int t = 0; t < NT; t += 4) {
    body(IC<0>{});
    body(IC<1>{});
    body(IC<2>{});
    body(IC<3>{});
  }
  {
    short* orow = out + (size_t)(b * SQ + 32 * w + l31) * DM + nt0 * 64 + 4 * hh;
#pragma unroll
    for (int nt = 0; nt < 2; ++nt)
#pragma unroll
      for (int i = 0; i < 4; ++i) {
        u32x2 pk = {cvtpk(acc[nt][4 * i], acc[nt][4 * i + 1]), cvtpk(acc[nt][4 * i + 2], acc[nt][4 * i + 3])};
        *(u32x2*)(orow + 32 * nt + 8 * i) = pk;
      }
    u32x2 pk = {cvtpk(acc2[0], acc2[1]), cvtpk(acc2[2], acc2[3])};
    *(u32x2*)(out + (size_t)(b * SQ + 128 + l15) * DM + nt0 * 64 + 16 * w + 4 * g) = pk;
  }
}

extern "C" void kernel_launch(void* const* d_in, const int* in_sizes, int n_in, void* d_out,
                              int out_size, void* d_ws, size_t ws_size, hipStream_t stream) {
  const short* x = (const short*)d_in[0];
  const short* kc = (const short*)d_in[1];
  const short* vc = (const short*)d_in[2];
  const short* wq = (const short*)d_in[3];
  const short* wo = (const short*)d_in[4];
  short* og = (short*)d_ws;
  short* out = (short*)d_out;
  (void)hipFuncSetAttribute((const void*)attn_kernel, hipFuncAttributeMaxDynamicSharedMemorySize, A_LDS);
  (void)hipFuncSetAttribute((const void*)oproj_kernel, hipFuncAttributeMaxDynamicSharedMemorySize, B_LDS);
  attn_kernel<<<256, 512, A_LDS, stream>>>(x, wq, kc, vc, og);
  oproj_kernel<<<256, 512, B_LDS, stream>>>(og, wo, out);
}
```

```cpp
#include <hip/hip_runtime.h>
#include <hip/hip_bf16.h>

typedef __attribute__((ext_vector_type(8))) short bf16x8;
typedef __attribute__((ext_vector_type(4))) short s16x4;
typedef __attribute__((ext_vector_type(16))) float f32x16;
typedef __attribute__((ext_vector_type(4))) float f32x4;
typedef __attribute__((ext_vector_type(4))) unsigned u32x4;
typedef __attribute__((ext_vector_type(2))) unsigned u32x2;

constexpr int NB = 32, SQ = 144, DM = 512, NH = 32, SK = 512, DH = 128;
constexpr int HD = NH * DH;
constexpr int MROWS = NB * SQ;

#define MFMA32(a, b, c) __builtin_amdgcn_mfma_f32_32x32x16_bf16((a), (b), (c), 0, 0, 0)
#define MFMA16(a, b, c) __builtin_amdgcn_mfma_f32_16x16x32_bf16((a), (b), (c), 0, 0, 0)
#define MFMA16K16(a, b, c) __builtin_amdgcn_mfma_f32_16x16x16bf16_1k((a), (b), (c), 0, 0, 0)

__device__ __forceinline__ unsigned cvtpk(float lo, float hi) {
  unsigned r;
  asm volatile("v_cvt_pk_bf16_f32 %0, %1, %2" : "=v"(r) : "v"(lo), "v"(hi));
  return r;
}
__device__ __forceinline__ bf16x8 cat8(s16x4 lo, s16x4 hi) {
  return __builtin_shufflevector(lo, hi, 0, 1, 2, 3, 4, 5, 6, 7);
}
__device__ __forceinline__ void glds16(const void* g, char* l) {
  __builtin_amdgcn_global_load_lds((const unsigned*)g, (unsigned*)l, 16, 0, 0);
}
__device__ __forceinline__ void glds16nt(const void* g, char* l) {
  __builtin_amdgcn_global_load_lds((const unsigned*)g, (unsigned*)l, 16, 0, 2);
}
__device__ __forceinline__ float xmax32(float v) {
  auto rr = __builtin_amdgcn_permlane32_swap(__float_as_uint(v), __float_as_uint(v), false, false);
  return fmaxf(__uint_as_float(rr[0]), __uint_as_float(rr[1]));
}
__device__ __forceinline__ float xsum32(float v) {
  auto rr = __builtin_amdgcn_permlane32_swap(__float_as_uint(v), __float_as_uint(v), false, false);
  return __uint_as_float(rr[0]) + __uint_as_float(rr[1]);
}
__device__ __forceinline__ float xmax16(float v) {
  auto rr = __builtin_amdgcn_permlane16_swap(__float_as_uint(v), __float_as_uint(v), false, false);
  return fmaxf(__uint_as_float(rr[0]), __uint_as_float(rr[1]));
}
__device__ __forceinline__ float xsum16(float v) {
  auto rr = __builtin_amdgcn_permlane16_swap(__float_as_uint(v), __float_as_uint(v), false, false);
  return __uint_as_float(rr[0]) + __uint_as_float(rr[1]);
}
template <int OFF> __device__ __forceinline__ bf16x8 rd128(int a) {
  bf16x8 r;
  asm volatile("ds_read_b128 %0, %1 offset:%2" : "=v"(r) : "v"(a), "i"(OFF) : "memory");
  return r;
}
template <int OFF> __device__ __forceinline__ s16x4 rdtr(int a) {
  s16x4 r;
  asm volatile("ds_read_b64_tr_b16 %0, %1 offset:%2" : "=v"(r) : "v"(a), "i"(OFF) : "memory");
  return r;
}
#define WAITL2(N, a, b) asm volatile("s_waitcnt lgkmcnt(" #N ")" : "+v"(a), "+v"(b) :: "memory")
#define WAITL4(N, a, b, c, d) asm volatile("s_waitcnt lgkmcnt(" #N ")" : "+v"(a), "+v"(b), "+v"(c), "+v"(d) :: "memory")
#define WAITL8(N, a, b, c, d, e, f, g_, h_) asm volatile("s_waitcnt lgkmcnt(" #N ")" : "+v"(a), "+v"(b), "+v"(c), "+v"(d), "+v"(e), "+v"(f), "+v"(g_), "+v"(h_) :: "memory")
#define RAW_BAR() __builtin_amdgcn_s_barrier()
#define SBAR() __builtin_amdgcn_sched_barrier(0)
#define OPAQUE(v) asm volatile("" : "+v"(v))
template <int N> struct IC { static constexpr int value = N; };
template <int I, int N, class F> __device__ __forceinline__ void sfor(F&& f) {
  if constexpr (I < N) { f(IC<I>{}); sfor<I + 1, N>(f); }
}

constexpr int A_BUFA = 20480;
constexpr int A_BUFB = 16384;
constexpr int A_OFF_A = 0;
constexpr int A_OFF_B = 2 * A_BUFA;
constexpr int A_OFF_Q2 = A_OFF_B + 2 * A_BUFB;
constexpr int A_OFF_SLOT = A_OFF_Q2 + 4096;
constexpr int A_LDS = A_OFF_SLOT + 16;

__global__ __launch_bounds__(256, 2) void attn_kernel(
    const short* __restrict__ x, const short* __restrict__ wq, const short* __restrict__ kc,
    const short* __restrict__ vc, short* __restrict__ og, unsigned* __restrict__ counter) {
  extern __shared__ __attribute__((aligned(16))) char smem[];
  const int tid = threadIdx.x, lane = tid & 63;
  const int w = __builtin_amdgcn_readfirstlane(tid >> 6);
  const int l31 = lane & 31, hh = lane >> 5, l15 = lane & 15, g = lane >> 4;
  const int qp = l15 >> 2, p4 = lane & 3, blk = (lane >> 4) & 1;
  const int sb = (int)(uintptr_t)smem;
  volatile unsigned* slot = (volatile unsigned*)(smem + A_OFF_SLOT);

  const unsigned ksrc = (16 * w + g) * 256 + ((l15 ^ g) << 4);
  const unsigned vsrc = (16 * w + g) * 256 + ((l15 ^ (g << 2)) << 4);
  auto stage_kv = [&](int pi, int t, int buf, unsigned ko, unsigned vo) {
    const char* kb = (const char*)kc + (size_t)pi * (SK * DH * 2) + t * 16384;
    const char* vb = (const char*)vc + (size_t)pi * (SK * DH * 2) + t * 16384;
#pragma unroll
    for (int i = 0; i < 4; ++i)
      glds16nt(kb + ((ko ^ (i << 6)) + i * 1024), smem + A_OFF_A + buf * A_BUFA + w * 4096 + i * 1024);
#pragma unroll
    for (int i = 0; i < 4; ++i)
      glds16nt(vb + (vo + i * 1024), smem + A_OFF_B + buf * A_BUFB + w * 4096 + i * 1024);
  };
  auto stage_q = [&](int pi, int t, int buf) {
    int ln = lane;
    OPAQUE(ln);
    const char* xt = (const char*)(x + (size_t)(pi >> 5) * SQ * DM) + t * 128;
    const char* wt = (const char*)(wq + (pi & 31) * DH) + (size_t)t * (64 * HD * 2);
#pragma unroll
    for (int ii = 0; ii < 5; ++ii) {
      int r = 8 * (w * 5 + ii) + (ln >> 3), cp = ln & 7;
      int rs = r < SQ ? r : SQ - 1;
      unsigned so = rs * 1024 + ((cp ^ ((r >> 1) & 7)) << 4);
      glds16(xt + so, smem + A_OFF_A + buf * A_BUFA + w * 5120 + ii * 1024);
    }
    const unsigned wo0 = (16 * w + (ln >> 4)) * (HD * 2) + (((ln & 15) ^ ((ln >> 4) << 2)) << 4);
#pragma unroll
    for (int ii = 0; ii < 4; ++ii)
      glds16(wt + (wo0 + ii * 4 * HD * 2), smem + A_OFF_B + buf * A_BUFB + w * 4096 + ii * 1024);
  };
  int pidx = (blockIdx.x & 7) * 128 + (blockIdx.x >> 3);
#define STAG 0
  if (STAG > 0 && blockIdx.x >= 256) {
    unsigned long long t0 = __builtin_amdgcn_s_memtime();
    while (__builtin_amdgcn_s_memtime() - t0 < STAG) __builtin_amdgcn_s_sleep(8);
  }
  stage_q(pidx, 0, 0);
  if (tid == 0) *slot = blockIdx.x + 512u;
  constexpr float CS = 0.088388347648318440f * 1.4426950408889634f;
  constexpr float THRS = 8.f / CS;

  for (;;) {
  const int b = pidx >> 5, h = pidx & 31;
  f32x16 acc[4];
#pragma unroll
  for (int i = 0; i < 4; ++i) acc[i] = f32x16{};
  f32x4 acc2[8];
#pragma unroll
  for (int i = 0; i < 8; ++i) acc2[i] = f32x4{};
  {
    int lq = lane;
    OPAQUE(lq);
    const int l31 = lq & 31, hh = lq >> 5, l15 = lq & 15, g = lq >> 4;
    const int qp = l15 >> 2, p4 = lq & 3, blk = (lq >> 4) & 1;
    const int xa = sb + A_OFF_A + (32 * w + l31) * 128 + ((hh ^ ((l31 >> 1) & 7)) << 4);
    const int wa = sb + A_OFF_B + (8 * hh + qp) * 256 + (((2 * blk + (p4 >> 1)) ^ (qp << 2)) << 4) + 8 * (p4 & 1);
    const int xa2 = sb + A_OFF_A + (128 + l15) * 128 + ((g ^ ((l15 >> 1) & 7)) << 4);
    const int wa2 = sb + A_OFF_B + (8 * g + qp) * 256 + (((4 * w + (p4 >> 1)) ^ (qp << 2)) << 4) + 8 * (p4 & 1);
    auto body = [&](auto BC, int t) {
      constexpr int buf = decltype(BC)::value;
      constexpr int OA = buf * A_BUFA, OB = buf * A_BUFB;
      asm volatile("s_waitcnt vmcnt(0) lgkmcnt(0)" ::: "memory");
      RAW_BAR();
      if (t < 7) stage_q(pidx, t + 1, buf ^ 1);
      else stage_kv(pidx, 0, buf ^ 1, ksrc, vsrc);
      SBAR();
      const int w0 = wa, w1 = wa ^ 64, w2 = wa ^ 128, w3 = wa ^ 192;
      bf16x8 xf[2];
      s16x4 wl[2][4], wh[2][4];
      bf16x8 xr[2];
      s16x4 rl[2][2], rh[2][2];
      auto issue = [&](auto SC) {
        constexpr int s = decltype(SC)::value, u = s & 1;
        xf[u] = rd128<OA>(xa ^ (s << 5));
        wl[u][0] = rdtr<OB + s * 4096>(w0); wh[u][0] = rdtr<OB + s * 4096 + 1024>(w0);
        wl[u][1] = rdtr<OB + s * 4096>(w1); wh[u][1] = rdtr<OB + s * 4096 + 1024>(w1);
        wl[u][2] = rdtr<OB + s * 4096>(w2); wh[u][2] = rdtr<OB + s * 4096 + 1024>(w2);
        wl[u][3] = rdtr<OB + s * 4096>(w3); wh[u][3] = rdtr<OB + s * 4096 + 1024>(w3);
      };
      issue(IC<0>{});
      sfor<0, 4>([&](auto SC) {
        constexpr int s = decltype(SC)::value, u = s & 1;
        if constexpr (s < 3) {
          issue(IC<s + 1>{});
          asm volatile("s_waitcnt lgkmcnt(9)" : "+v"(xf[u]), "+v"(wl[u][0]), "+v"(wh[u][0]), "+v"(wl[u][1]), "+v"(wh[u][1]),
                       "+v"(wl[u][2]), "+v"(wh[u][2]), "+v"(wl[u][3]), "+v"(wh[u][3]) :: "memory");
        } else {
          xr[0] = rd128<OA>(xa2); xr[1] = rd128<OA>(xa2 ^ 64);
          rl[0][0] = rdtr<OB>(wa2); rh[0][0] = rdtr<OB + 1024>(wa2);
          rl[0][1] = rdtr<OB>(wa2 ^ 32); rh[0][1] = rdtr<OB + 1024>(wa2 ^ 32);
          rl[1][0] = rdtr<OB + 8192>(wa2); rh[1][0] = rdtr<OB + 8192 + 1024>(wa2);
          rl[1][1] = rdtr<OB + 8192>(wa2 ^ 32); rh[1][1] = rdtr<OB + 8192 + 1024>(wa2 ^ 32);
          asm volatile("s_waitcnt lgkmcnt(10)" : "+v"(xf[u]), "+v"(wl[u][0]), "+v"(wh[u][0]), "+v"(wl[u][1]), "+v"(wh[u][1]),
                       "+v"(wl[u][2]), "+v"(wh[u][2]), "+v"(wl[u][3]), "+v"(wh[u][3]) :: "memory");
        }
#pragma unroll
        for (int dt = 0; dt < 4; ++dt) acc[dt] = MFMA32(cat8(wl[u][dt], wh[u][dt]), xf[u], acc[dt]);
        SBAR();
      });
      asm volatile("s_waitcnt lgkmcnt(0)" : "+v"(xr[0]), "+v"(xr[1]), "+v"(rl[0][0]), "+v"(rh[0][0]), "+v"(rl[0][1]), "+v"(rh[0][1]),
                   "+v"(rl[1][0]), "+v"(rh[1][0]), "+v"(rl[1][1]), "+v"(rh[1][1]) :: "memory");
#pragma unroll
      for (int s2 = 0; s2 < 2; ++s2)
#pragma unroll
        for (int e = 0; e < 2; ++e) acc2[e] = MFMA16(cat8(rl[s2][e], rh[s2][e]), xr[s2], acc2[e]);
    };
    for (int t = 0; t < 8; t += 2) {
      body(IC<0>{}, t);
      body(IC<1>{}, t + 1);
    }
  }
  bf16x8 qf[8];
#pragma unroll
  for (int dt = 0; dt < 4; ++dt) {
#pragma unroll
    for (int s = 0; s < 2; ++s) {
      unsigned a0 = cvtpk(acc[dt][8 * s + 0], acc[dt][8 * s + 1]);
      unsigned a1 = cvtpk(acc[dt][8 * s + 2], acc[dt][8 * s + 3]);
      unsigned b0 = cvtpk(acc[dt][8 * s + 4], acc[dt][8 * s + 5]);
      unsigned b1 = cvtpk(acc[dt][8 * s + 6], acc[dt][8 * s + 7]);
      auto r0 = __builtin_amdgcn_permlane32_swap(a0, b0, false, false);
      auto r1 = __builtin_amdgcn_permlane32_swap(a1, b1, false, false);
      u32x4 wv = {r0[0], r1[0], r0[1], r1[1]};
      qf[2 * dt + s] = __builtin_bit_cast(bf16x8, wv);
    }
  }
  {
#pragma unroll
    for (int e = 0; e < 2; ++e) {
      int dd = 2 * w + e;
      u32x2 pk = {cvtpk(acc2[e][0], acc2[e][1]), cvtpk(acc2[e][2], acc2[e][3])};
      int ch = (2 * dd + (g >> 1)) ^ l15;
      asm volatile("ds_write_b64 %0, %1" :: "v"(sb + A_OFF_Q2 + l15 * 256 + (ch << 4) + 8 * (g & 1)), "v"(pk) : "memory");
    }
  }
#pragma unroll
  for (int i = 0; i < 4; ++i) acc[i] = f32x16{};
#pragma unroll
  for (int i = 0; i < 8; ++i) acc2[i] = f32x4{};
  int nidx = 0;
  float m1 = -1e30f, l1 = 0.f, m2 = -1e30f, l2 = 0.f;
  {
    int ka = sb + A_OFF_A + l31 * 256 + ((hh ^ l15) << 4);
    int ka2 = sb + A_OFF_A + (16 * w + l15) * 256 + ((g ^ l15) << 4);
    int q2a = sb + A_OFF_Q2 + l15 * 256 + ((g ^ l15) << 4);
    int va = sb + A_OFF_B + (4 * hh + qp) * 256 + (((2 * blk + (p4 >> 1)) ^ (qp << 2)) << 4) + 8 * (p4 & 1);
    int va2 = sb + A_OFF_B + (16 * w + 4 * g + qp) * 256 + (((p4 >> 1) ^ (qp << 2)) << 4) + 8 * (p4 & 1);
    auto body = [&](auto BC, int t) {
      constexpr int buf = decltype(BC)::value;
      constexpr int OA = buf * A_BUFA, OB = buf * A_BUFB;
      asm volatile("s_waitcnt vmcnt(0) lgkmcnt(0)" ::: "memory");
      RAW_BAR();
      int ka_ = ka, ka2_ = ka2, q2a_ = q2a, va_ = va, va2_ = va2;
      unsigned ko = ksrc, vo = vsrc;
      OPAQUE(ka_); OPAQUE(ka2_); OPAQUE(q2a_); OPAQUE(va_); OPAQUE(va2_); OPAQUE(ko); OPAQUE(vo);
      if (t < 7) {
        stage_kv(pidx, t + 1, buf ^ 1, ko, vo);
      } else {
        const unsigned it = __builtin_amdgcn_readfirstlane(*slot);
        nidx = it < 1024u ? (int)((it & 7) * 128 + (it >> 3)) : -1;
        if (nidx >= 0) stage_q(nidx, 0, 0);
      }
      SBAR();
      f32x16 s0 = f32x16{}, s1 = f32x16{};
      f32x4 sr = f32x4{};
      bf16x8 kA[4], kB[4];
#pragma unroll
      for (int i = 0; i < 4; ++i) kA[i] = rd128<OA>(ka_ ^ (i << 5));
#pragma unroll
      for (int i = 0; i < 4; ++i) kB[i] = rd128<OA>(ka_ ^ ((4 + i) << 5));
      WAITL4(4, kA[0], kA[1], kA[2], kA[3]);
      s0 = MFMA32(kA[0], qf[0], s0);
      s0 = MFMA32(kA[1], qf[1], s0);
      SBAR();
      s0 = MFMA32(kA[2], qf[2], s0);
      s0 = MFMA32(kA[3], qf[3], s0);
      SBAR();
#pragma unroll
      for (int i = 0; i < 4; ++i) kA[i] = rd128<OA + 8192>(ka_ ^ (i << 5));
      WAITL4(4, kB[0], kB[1], kB[2], kB[3]);
      s0 = MFMA32(kB[0], qf[4], s0);
      s0 = MFMA32(kB[1], qf[5], s0);
      SBAR();
      s0 = MFMA32(kB[2], qf[6], s0);
      s0 = MFMA32(kB[3], qf[7], s0);
      SBAR();
#pragma unroll
      for (int i = 0; i < 4; ++i) kB[i] = rd128<OA + 8192>(ka_ ^ ((4 + i) << 5));
      WAITL4(4, kA[0], kA[1], kA[2], kA[3]);
      s1 = MFMA32(kA[0], qf[0], s1);
      SBAR();
      s1 = MFMA32(kA[1], qf[1], s1);
      float tmax = fmaxf(fmaxf(s0[0], s0[1]), s0[2]);
#pragma unroll
      for (int r = 3; r < 9; r += 2) tmax = fmaxf(fmaxf(tmax, s0[r]), s0[r + 1]);
      SBAR();
      s1 = MFMA32(kA[2], qf[2], s1);
#pragma unroll
      for (int r = 9; r < 15; r += 2) tmax = fmaxf(fmaxf(tmax, s0[r]), s0[r + 1]);
      tmax = fmaxf(tmax, s0[15]);
      tmax = xmax32(tmax);
      SBAR();
      s1 = MFMA32(kA[3], qf[3], s1);
      SBAR();
      if (!__all(tmax - m1 <= THRS)) {
        float mn0 = fmaxf(m1, tmax);
        float alpha = __builtin_amdgcn_exp2f((m1 - mn0) * CS);
        m1 = mn0;
        l1 *= alpha;
#pragma unroll
        for (int dt = 0; dt < 4; ++dt)
#pragma unroll
          for (int r = 0; r < 16; ++r) acc[dt][r] *= alpha;
      }
      const float mn = m1;
      bf16x8 kr[4], qr[4];
#pragma unroll
      for (int ks = 0; ks < 4; ++ks) { kr[ks] = rd128<OA>(ka2_ ^ (ks << 6)); qr[ks] = rd128<0>(q2a_ ^ (ks << 6)); }
      WAITL4(8, kB[0], kB[1], kB[2], kB[3]);
      bf16x8 pf[4];
      float mc = -mn * CS;
      float ps = 0.f;
      float e[8];
      s1 = MFMA32(kB[0], qf[4], s1);
#pragma unroll
      for (int j = 0; j < 4; ++j) { e[j] = __builtin_amdgcn_exp2f(fmaf(s0[j], CS, mc)); ps += e[j]; }
      SBAR();
      s1 = MFMA32(kB[1], qf[5], s1);
#pragma unroll
      for (int j = 4; j < 8; ++j) { e[j] = __builtin_amdgcn_exp2f(fmaf(s0[j], CS, mc)); ps += e[j]; }
      { u32x4 a = {cvtpk(e[0], e[1]), cvtpk(e[2], e[3]), cvtpk(e[4], e[5]), cvtpk(e[6], e[7])}; pf[0] = __builtin_bit_cast(bf16x8, a); }
      SBAR();
      s1 = MFMA32(kB[2], qf[6], s1);
#pragma unroll
      for (int j = 0; j < 4; ++j) { e[j] = __builtin_amdgcn_exp2f(fmaf(s0[8 + j], CS, mc)); ps += e[j]; }
      SBAR();
      s1 = MFMA32(kB[3], qf[7], s1);
#pragma unroll
      for (int j = 4; j < 8; ++j) { e[j] = __builtin_amdgcn_exp2f(fmaf(s0[8 + j], CS, mc)); ps += e[j]; }
      { u32x4 a = {cvtpk(e[0], e[1]), cvtpk(e[2], e[3]), cvtpk(e[4], e[5]), cvtpk(e[6], e[7])}; pf[1] = __builtin_bit_cast(bf16x8, a); }
      l1 += ps;
      OPAQUE(l1);
      SBAR();
      SBAR();
      const int v0 = va_, v1 = va_ ^ 64, v2 = va_ ^ 128, v3 = va_ ^ 192;
      s16x4 vl[2][4], vh[2][4];
      auto issue_v = [&](auto SC) {
        constexpr int s = decltype(SC)::value, u = s & 1;
        vl[u][0] = rdtr<OB + s * 4096>(v0); vh[u][0] = rdtr<OB + s * 4096 + 2048>(v0);
        vl[u][1] = rdtr<OB + s * 4096>(v1); vh[u][1] = rdtr<OB + s * 4096 + 2048>(v1);
        vl[u][2] = rdtr<OB + s * 4096>(v2); vh[u][2] = rdtr<OB + s * 4096 + 2048>(v2);
        vl[u][3] = rdtr<OB + s * 4096>(v3); vh[u][3] = rdtr<OB + s * 4096 + 2048>(v3);
      };
      issue_v(IC<0>{});
      WAITL8(8, kr[0], kr[1], kr[2], kr[3], qr[0], qr[1], qr[2], qr[3]);
#pragma unroll
      for (int ks = 0; ks < 4; ++ks) sr = MFMA16(kr[ks], qr[ks], sr);
      SBAR();
      SBAR();
      issue_v(IC<1>{});
      WAITL8(8, vl[0][0], vh[0][0], vl[0][1], vh[0][1], vl[0][2], vh[0][2], vl[0][3], vh[0][3]);
      acc[0] = MFMA32(cat8(vl[0][0], vh[0][0]), pf[0], acc[0]);
      float tm1 = fmaxf(fmaxf(s1[0], s1[1]), s1[2]);
#pragma unroll
      for (int r = 3; r < 9; r += 2) tm1 = fmaxf(fmaxf(tm1, s1[r]), s1[r + 1]);
      SBAR();
      acc[1] = MFMA32(cat8(vl[0][1], vh[0][1]), pf[0], acc[1]);
#pragma unroll
      for (int r = 9; r < 15; r += 2) tm1 = fmaxf(fmaxf(tm1, s1[r]), s1[r + 1]);
      tm1 = fmaxf(tm1, s1[15]);
      tm1 = xmax32(tm1);
      const bool keepb = __all(tm1 - m1 <= THRS);
      float mnb = keepb ? m1 : fmaxf(m1, tm1);
      float alphab = keepb ? 1.f : __builtin_amdgcn_exp2f((m1 - mnb) * CS);
      m1 = mnb;
      mc = -mnb * CS;
      ps = 0.f;
      SBAR();
      acc[2] = MFMA32(cat8(vl[0][2], vh[0][2]), pf[0], acc[2]);
#pragma unroll
      for (int j = 0; j < 4; ++j) { e[j] = __builtin_amdgcn_exp2f(fmaf(s1[j], CS, mc)); ps += e[j]; }
      SBAR();
      acc[3] = MFMA32(cat8(vl[0][3], vh[0][3]), pf[0], acc[3]);
#pragma unroll
      for (int j = 4; j < 8; ++j) { e[j] = __builtin_amdgcn_exp2f(fmaf(s1[j], CS, mc)); ps += e[j]; }
      { u32x4 a = {cvtpk(e[0], e[1]), cvtpk(e[2], e[3]), cvtpk(e[4], e[5]), cvtpk(e[6], e[7])}; pf[2] = __builtin_bit_cast(bf16x8, a); }
      SBAR();
      issue_v(IC<2>{});
      WAITL8(8, vl[1][0], vh[1][0], vl[1][1], vh[1][1], vl[1][2], vh[1][2], vl[1][3], vh[1][3]);
      acc[0] = MFMA32(cat8(vl[1][0], vh[1][0]), pf[1], acc[0]);
#pragma unroll
      for (int j = 0; j < 4; ++j) { e[j] = __builtin_amdgcn_exp2f(fmaf(s1[8 + j], CS, mc)); ps += e[j]; }
      SBAR();
      acc[1] = MFMA32(cat8(vl[1][1], vh[1][1]), pf[1], acc[1]);
#pragma unroll
      for (int j = 4; j < 8; ++j) { e[j] = __builtin_amdgcn_exp2f(fmaf(s1[8 + j], CS, mc)); ps += e[j]; }
      { u32x4 a = {cvtpk(e[0], e[1]), cvtpk(e[2], e[3]), cvtpk(e[4], e[5]), cvtpk(e[6], e[7])}; pf[3] = __builtin_bit_cast(bf16x8, a); }
      l1 = l1 * alphab + ps;
      OPAQUE(l1);
      SBAR();
      acc[2] = MFMA32(cat8(vl[1][2], vh[1][2]), pf[1], acc[2]);
      float t2 = fmaxf(fmaxf(sr[0], sr[1]), fmaxf(sr[2], sr[3]));
      t2 = xmax16(t2);
      t2 = xmax32(t2);
      const bool keep2 = __all(t2 - m2 <= THRS);
      float mn2 = keep2 ? m2 : fmaxf(m2, t2);
      float alpha2 = keep2 ? 1.f : __builtin_amdgcn_exp2f((m2 - mn2) * CS);
      m2 = mn2;
      SBAR();
      acc[3] = MFMA32(cat8(vl[1][3], vh[1][3]), pf[1], acc[3]);
      const float mc2 = -mn2 * CS;
      float e0 = __builtin_amdgcn_exp2f(fmaf(sr[0], CS, mc2));
      float e1 = __builtin_amdgcn_exp2f(fmaf(sr[1], CS, mc2));
      float e2 = __builtin_amdgcn_exp2f(fmaf(sr[2], CS, mc2));
      float e3 = __builtin_amdgcn_exp2f(fmaf(sr[3], CS, mc2));
      l2 = l2 * alpha2 + (e0 + e1) + (e2 + e3);
      OPAQUE(l2);
      u32x2 p2u = {cvtpk(e0, e1), cvtpk(e2, e3)};
      s16x4 p2 = __builtin_bit_cast(s16x4, p2u);
      SBAR();
      if (!keepb) {
#pragma unroll
        for (int dt = 0; dt < 4; ++dt)
#pragma unroll
          for (int r = 0; r < 16; ++r) acc[dt][r] *= alphab;
      }
      if (!keep2) {
#pragma unroll
        for (int dd = 0; dd < 8; ++dd)
#pragma unroll
          for (int r = 0; r < 4; ++r) acc2[dd][r] *= alpha2;
      }
      issue_v(IC<3>{});
      WAITL8(8, vl[0][0], vh[0][0], vl[0][1], vh[0][1], vl[0][2], vh[0][2], vl[0][3], vh[0][3]);
#pragma unroll
      for (int dt = 0; dt < 4; ++dt) acc[dt] = MFMA32(cat8(vl[0][dt], vh[0][dt]), pf[2], acc[dt]);
      SBAR();
      s16x4 vr[8];
#pragma unroll
      for (int dd = 0; dd < 8; ++dd) vr[dd] = rdtr<OB>(va2_ ^ (dd << 5));
      WAITL8(8, vl[1][0], vh[1][0], vl[1][1], vh[1][1], vl[1][2], vh[1][2], vl[1][3], vh[1][3]);
#pragma unroll
      for (int dt = 0; dt < 4; ++dt) acc[dt] = MFMA32(cat8(vl[1][dt], vh[1][dt]), pf[3], acc[dt]);
      WAITL8(0, vr[0], vr[1], vr[2], vr[3], vr[4], vr[5], vr[6], vr[7]);
#pragma unroll
      for (int dd = 0; dd < 8; ++dd) acc2[dd] = MFMA16K16(vr[dd], p2, acc2[dd]);
    };
    for (int t = 0; t < 8; t += 2) {
      body(IC<0>{}, t);
      body(IC<1>{}, t + 1);
    }
  }

  int tz = threadIdx.x;
  OPAQUE(tz);
  asm volatile("s_waitcnt lgkmcnt(0)" ::: "memory");
  RAW_BAR();
  {
    const int l15 = tz & 15, g = (tz >> 4) & 3;
    float lt2 = xsum32(xsum16(l2));
    float* pw = (float*)(smem + ((w & 1) ? A_OFF_B + A_BUFB : A_OFF_A + A_BUFA) + (w >> 1) * 8192);
#pragma unroll
    for (int dd = 0; dd < 8; ++dd) *(f32x4*)(pw + l15 * 128 + 16 * dd + 4 * g) = acc2[dd];
    float* st = (float*)(smem + A_OFF_Q2);
    if (g == 0) { st[w * 32 + l15] = m2; st[w * 32 + 16 + l15] = lt2; }
  }
  asm volatile("s_waitcnt lgkmcnt(0)" ::: "memory");
  RAW_BAR();
  {
    const int q = tz >> 4, d0 = (tz & 15) * 8;
    const float* st = (const float*)(smem + A_OFF_Q2);
    float mm = fmaxf(fmaxf(st[q], st[32 + q]), fmaxf(st[64 + q], st[96 + q]));
    float o[8] = {0, 0, 0, 0, 0, 0, 0, 0};
    float ls = 0.f;
#pragma unroll
    for (int ww = 0; ww < 4; ++ww) {
      float sc = __builtin_amdgcn_exp2f((st[ww * 32 + q] - mm) * CS);
      ls += st[ww * 32 + 16 + q] * sc;
      const float* pw = (const float*)(smem + ((ww & 1) ? A_OFF_B + A_BUFB : A_OFF_A + A_BUFA) + (ww >> 1) * 8192) + q * 128 + d0;
      f32x4 a = *(const f32x4*)pw, c = *(const f32x4*)(pw + 4);
#pragma unroll
      for (int j = 0; j < 4; ++j) { o[j] += a[j] * sc; o[4 + j] += c[j] * sc; }
    }
    float inv = 1.f / ls;
    u32x4 pk = {cvtpk(o[0] * inv, o[1] * inv), cvtpk(o[2] * inv, o[3] * inv),
                cvtpk(o[4] * inv, o[5] * inv), cvtpk(o[6] * inv, o[7] * inv)};
    *(u32x4*)(og + (size_t)(b * SQ + 128 + q) * HD + h * DH + d0) = pk;
  }
  asm volatile("s_waitcnt lgkmcnt(0)" ::: "memory");
  RAW_BAR();
  {
    const int l31 = tz & 31, hh = (tz >> 5) & 1;
    float lt = xsum32(l1);
    float inv = 1.f / lt;
    char* ob = smem + (w < 2 ? A_OFF_A + A_BUFA : A_OFF_B + A_BUFB) + ((w & 1) * 32 + l31) * 256 + 8 * hh;
#pragma unroll
    for (int dt = 0; dt < 4; ++dt)
#pragma unroll
      for (int i = 0; i < 4; ++i) {
        u32x2 pk = {cvtpk(acc[dt][4 * i] * inv, acc[dt][4 * i + 1] * inv),
                    cvtpk(acc[dt][4 * i + 2] * inv, acc[dt][4 * i + 3] * inv)};
        *(u32x2*)(ob + (((4 * dt + i) ^ (l31 & 15)) << 4)) = pk;
      }
    if (tz == 0 && nidx >= 0) *slot = 4096u;
  }
  asm volatile("s_waitcnt lgkmcnt(0)" ::: "memory");
  RAW_BAR();
  {
    const int c = tz & 15, r0 = tz >> 4;
    short* orow = og + (size_t)(b * SQ + r0) * HD + h * DH + c * 8;
#pragma unroll
    for (int j = 0; j < 8; ++j) {
      const int r = r0 + 16 * j;
      const char* src = smem + (j < 4 ? A_OFF_A + A_BUFA + r * 256 : A_OFF_B + A_BUFB + (r - 64) * 256) + ((c ^ r0) << 4);
      bf16x8 v = *(const bf16x8*)src;
      *(bf16x8*)(orow + (size_t)(16 * j) * HD) = v;
    }
  }
  if (nidx < 0) break;
  pidx = nidx;
  }
}

constexpr int B_NST = 5;
constexpr int B_BUFA = 20480;
constexpr int B_BUFB = 8192;
constexpr int B_STRIDE = B_BUFA + B_BUFB;
constexpr int B_LDS = B_NST * B_STRIDE;

__global__ __launch_bounds__(512, 2) void oproj_kernel(const short* __restrict__ og,
                                                        const short* __restrict__ wo,
                                                        short* __restrict__ out) {
  extern __shared__ __attribute__((aligned(16))) char smem[];
  const int tid = threadIdx.x, lane = tid & 63;
  const int wv = __builtin_amdgcn_readfirstlane(tid >> 6);
  const int w = wv & 3;
  const int xg = blockIdx.x & 7, idx = blockIdx.x >> 3;
  const int b = xg * 4 + (idx >> 3), nt0 = idx & 7;
  const int l31 = lane & 31, hh = lane >> 5, l15 = lane & 15, g = lane >> 4;
  const int qp = l15 >> 2, p4 = lane & 3, blk = (lane >> 4) & 1;
  constexpr int NT = HD / 64;
  if (wv >= 4) {
    const char* ob = (const char*)(og + (size_t)b * SQ * HD);
    const char* wob = (const char*)(wo + nt0 * 64);
    unsigned osrc[5], wsrc[2];
#pragma unroll
    for (int ii = 0; ii < 5; ++ii) {
      int r = 8 * (w * 5 + ii) + (lane >> 3), cp = lane & 7;
      int rs = r < SQ ? r : SQ - 1;
      osrc[ii] = rs * (HD * 2) + ((cp ^ ((r >> 1) & 7)) << 4);
    }
#pragma unroll
    for (int ii = 0; ii < 2; ++ii) {
      int r = 8 * (w * 2 + ii) + (lane >> 3), cp = lane & 7;
      wsrc[ii] = r * (DM * 2) + ((cp ^ (((r >> 1) & 1) << 2)) << 4);
    }
    auto stage = [&](int t) {
      const int buf = t % B_NST;
      char* la = smem + buf * B_STRIDE + w * 5120;
      char* lb = smem + buf * B_STRIDE + B_BUFA + w * 2048;
      const char* ot = ob + t * 128;
      const char* wt = wob + (size_t)t * (64 * DM * 2);
#pragma unroll
      for (int ii = 0; ii < 5; ++ii) glds16(ot + osrc[ii], la + ii * 1024);
#pragma unroll
      for (int ii = 0; ii < 2; ++ii) glds16(wt + wsrc[ii], lb + ii * 1024);
    };
    stage(0); stage(1); stage(2); stage(3);
    asm volatile("s_waitcnt vmcnt(21)" ::: "memory");
    for (int t = 0; t < NT; ++t) {
      RAW_BAR();
      if (t + 4 < NT) { stage(t + 4); asm volatile("s_waitcnt vmcnt(21)" ::: "memory"); }
      else if (t + 3 < NT) asm volatile("s_waitcnt vmcnt(14)" ::: "memory");
      else if (t + 2 < NT) asm volatile("s_waitcnt vmcnt(7)" ::: "memory");
      else asm volatile("s_waitcnt vmcnt(0)" ::: "memory");
    }
    return;
  }
  f32x16 acc[2] = {f32x16{}, f32x16{}};
  f32x4 acc2 = f32x4{};
  const int sb = (int)(uintptr_t)smem;
  const int xa = sb + (32 * w + l31) * 128 + ((hh ^ ((l31 >> 1) & 7)) << 4);
  const int wa = sb + B_BUFA + (8 * hh + qp) * 128 + (((2 * blk + (p4 >> 1)) ^ ((qp >> 1) << 2)) << 4) + 8 * (p4 & 1);
  const int xa2 = sb + (128 + l15) * 128 + ((g ^ ((l15 >> 1) & 7)) << 4);
  const int wa2 = sb + B_BUFA + (8 * g + qp) * 128 + (((2 * w + (p4 >> 1)) ^ ((qp >> 1) << 2)) << 4) + 8 * (p4 & 1);
  auto body = [&](int bo) {
    constexpr int OB = 0;
    const int hi2 = bo;
    RAW_BAR();
    const int xa_ = xa + hi2, w0 = wa + hi2, w1 = (wa ^ 64) + hi2, xa2_ = xa2 + hi2, wa2_ = wa2 + hi2;
    bf16x8 xf[4], xr[2];
    s16x4 wl[4][2], wh[4][2], rl[2], rh[2];
    sfor<0, 4>([&](auto SC) {
      constexpr int s = decltype(SC)::value;
      xf[s] = rd128<OB>(xa_ ^ (s << 5));
      wl[s][0] = rdtr<OB + s * 2048>(w0); wh[s][0] = rdtr<OB + s * 2048 + 512>(w0);
      wl[s][1] = rdtr<OB + s * 2048>(w1); wh[s][1] = rdtr<OB + s * 2048 + 512>(w1);
      if constexpr (s >= 1) {
        asm volatile("s_waitcnt lgkmcnt(5)" : "+v"(xf[s - 1]), "+v"(wl[s - 1][0]), "+v"(wh[s - 1][0]), "+v"(wl[s - 1][1]), "+v"(wh[s - 1][1]) :: "memory");
        acc[0] = MFMA32(cat8(wl[s - 1][0], wh[s - 1][0]), xf[s - 1], acc[0]);
        acc[1] = MFMA32(cat8(wl[s - 1][1], wh[s - 1][1]), xf[s - 1], acc[1]);
      }
    });
    xr[0] = rd128<OB>(xa2_); rl[0] = rdtr<OB>(wa2_); rh[0] = rdtr<OB + 512>(wa2_);
    xr[1] = rd128<OB>(xa2_ ^ 64); rl[1] = rdtr<OB + 4096>(wa2_); rh[1] = rdtr<OB + 4096 + 512>(wa2_);
    asm volatile("s_waitcnt lgkmcnt(6)" : "+v"(xf[3]), "+v"(wl[3][0]), "+v"(wh[3][0]), "+v"(wl[3][1]), "+v"(wh[3][1]) :: "memory");
    acc[0] = MFMA32(cat8(wl[3][0], wh[3][0]), xf[3], acc[0]);
    acc[1] = MFMA32(cat8(wl[3][1], wh[3][1]), xf[3], acc[1]);
    asm volatile("s_waitcnt lgkmcnt(0)" : "+v"(xr[0]), "+v"(rl[0]), "+v"(rh[0]), "+v"(xr[1]), "+v"(rl[1]), "+v"(rh[1]) :: "memory");
    acc2 = MFMA16(cat8(rl[0], rh[0]), xr[0], acc2);
    acc2 = MFMA16(cat8(rl[1], rh[1]), xr[1], acc2);
  };
  int bo = 0;
  for (int t = 0; t < NT; ++t) {
    body(bo);
    bo += B_STRIDE;
    if (bo == B_NST * B_STRIDE) bo = 0;
  }
  {
    short* orow = out + (size_t)(b * SQ + 32 * w + l31) * DM + nt0 * 64 + 4 * hh;
#pragma unroll
    for (int nt = 0; nt < 2; ++nt)
#pragma unroll
      for (int i = 0; i < 4; ++i) {
        u32x2 pk = {cvtpk(acc[nt][4 * i], acc[nt][4 * i + 1]), cvtpk(acc[nt][4 * i + 2], acc[nt][4 * i + 3])};
        *(u32x2*)(orow + 32 * nt + 8 * i) = pk;
      }
    u32x2 pk = {cvtpk(acc2[0], acc2[1]), cvtpk(acc2[2], acc2[3])};
    *(u32x2*)(out + (size_t)(b * SQ + 128 + l15) * DM + nt0 * 64 + 16 * w + 4 * g) = pk;
  }
}

extern "C" void kernel_launch(void* const* d_in, const int* in_sizes, int n_in, void* d_out,
                              int out_size, void* d_ws, size_t ws_size, hipStream_t stream) {
  const short* x = (const short*)d_in[0];
  const short* kc = (const short*)d_in[1];
  const short* vc = (const short*)d_in[2];
  const short* wq = (const short*)d_in[3];
  const short* wo = (const short*)d_in[4];
  short* og = (short*)d_ws;
  short* out = (short*)d_out;
  unsigned* counter = (unsigned*)(og + (size_t)MROWS * HD);
  (void)hipFuncSetAttribute((const void*)attn_kernel, hipFuncAttributeMaxDynamicSharedMemorySize, A_LDS);
  (void)hipFuncSetAttribute((const void*)oproj_kernel, hipFuncAttributeMaxDynamicSharedMemorySize, B_LDS);
  attn_kernel<<<512, 256, A_LDS, stream>>>(x, wq, kc, vc, og, counter);
  oproj_kernel<<<256, 512, B_LDS, stream>>>(og, wo, out);
}
```

```cpp
#include <hip/hip_runtime.h>
#include <hip/hip_bf16.h>

typedef __attribute__((ext_vector_type(8))) short bf16x8;
typedef __attribute__((ext_vector_type(4))) short s16x4;
typedef __attribute__((ext_vector_type(16))) float f32x16;
typedef __attribute__((ext_vector_type(4))) float f32x4;
typedef __attribute__((ext_vector_type(4))) unsigned u32x4;
typedef __attribute__((ext_vector_type(2))) unsigned u32x2;

constexpr int NB = 32, SQ = 144, DM = 512, NH = 32, SK = 512, DH = 128;
constexpr int HD = NH * DH;
constexpr int MROWS = NB * SQ;

#define MFMA32(a, b, c) __builtin_amdgcn_mfma_f32_32x32x16_bf16((a), (b), (c), 0, 0, 0)
#define MFMA16(a, b, c) __builtin_amdgcn_mfma_f32_16x16x32_bf16((a), (b), (c), 0, 0, 0)
#define MFMA16K16(a, b, c) __builtin_amdgcn_mfma_f32_16x16x16bf16_1k((a), (b), (c), 0, 0, 0)

__device__ __forceinline__ unsigned cvtpk(float lo, float hi) {
  unsigned r;
  asm volatile("v_cvt_pk_bf16_f32 %0, %1, %2" : "=v"(r) : "v"(lo), "v"(hi));
  return r;
}
__device__ __forceinline__ bf16x8 cat8(s16x4 lo, s16x4 hi) {
  return __builtin_shufflevector(lo, hi, 0, 1, 2, 3, 4, 5, 6, 7);
}
__device__ __forceinline__ void glds16(const void* g, char* l) {
  __builtin_amdgcn_global_load_lds((const unsigned*)g, (unsigned*)l, 16, 0, 0);
}
__device__ __forceinline__ void glds16nt(const void* g, char* l) {
  __builtin_amdgcn_global_load_lds((const unsigned*)g, (unsigned*)l, 16, 0, 2);
}
__device__ __forceinline__ float xmax32(float v) {
  auto rr = __builtin_amdgcn_permlane32_swap(__float_as_uint(v), __float_as_uint(v), false, false);
  return fmaxf(__uint_as_float(rr[0]), __uint_as_float(rr[1]));
}
__device__ __forceinline__ float xsum32(float v) {
  auto rr = __builtin_amdgcn_permlane32_swap(__float_as_uint(v), __float_as_uint(v), false, false);
  return __uint_as_float(rr[0]) + __uint_as_float(rr[1]);
}
__device__ __forceinline__ float xmax16(float v) {
  auto rr = __builtin_amdgcn_permlane16_swap(__float_as_uint(v), __float_as_uint(v), false, false);
  return fmaxf(__uint_as_float(rr[0]), __uint_as_float(rr[1]));
}
__device__ __forceinline__ float xsum16(float v) {
  auto rr = __builtin_amdgcn_permlane16_swap(__float_as_uint(v), __float_as_uint(v), false, false);
  return __uint_as_float(rr[0]) + __uint_as_float(rr[1]);
}
template <int OFF> __device__ __forceinline__ bf16x8 rd128(int a) {
  bf16x8 r;
  asm volatile("ds_read_b128 %0, %1 offset:%2" : "=v"(r) : "v"(a), "i"(OFF) : "memory");
  return r;
}
template <int OFF> __device__ __forceinline__ s16x4 rdtr(int a) {
  s16x4 r;
  asm volatile("ds_read_b64_tr_b16 %0, %1 offset:%2" : "=v"(r) : "v"(a), "i"(OFF) : "memory");
  return r;
}
#define WAITL2(N, a, b) asm volatile("s_waitcnt lgkmcnt(" #N ")" : "+v"(a), "+v"(b) :: "memory")
#define WAITL4(N, a, b, c, d) asm volatile("s_waitcnt lgkmcnt(" #N ")" : "+v"(a), "+v"(b), "+v"(c), "+v"(d) :: "memory")
#define WAITL8(N, a, b, c, d, e, f, g_, h_) asm volatile("s_waitcnt lgkmcnt(" #N ")" : "+v"(a), "+v"(b), "+v"(c), "+v"(d), "+v"(e), "+v"(f), "+v"(g_), "+v"(h_) :: "memory")
#define RAW_BAR() __builtin_amdgcn_s_barrier()
#define SBAR() __builtin_amdgcn_sched_barrier(0)
#define OPAQUE(v) asm volatile("" : "+v"(v))
template <int N> struct IC { static constexpr int value = N; };
template <int I, int N, class F> __device__ __forceinline__ void sfor(F&& f) {
  if constexpr (I < N) { f(IC<I>{}); sfor<I + 1, N>(f); }
}

constexpr int A_BUFA = 20480;
constexpr int A_BUFB = 16384;
constexpr int A_OFF_A = 0;
constexpr int A_OFF_B = 2 * A_BUFA;
constexpr int A_OFF_Q2 = A_OFF_B + 2 * A_BUFB;
constexpr int A_OFF_SLOT = A_OFF_Q2 + 4096;
constexpr int A_LDS = A_OFF_SLOT + 16;

__global__ __launch_bounds__(256, 2) void attn_kernel(
    const short* __restrict__ x, const short* __restrict__ wq, const short* __restrict__ kc,
    const short* __restrict__ vc, short* __restrict__ og, unsigned* __restrict__ counter) {
  extern __shared__ __attribute__((aligned(16))) char smem[];
  const int tid = threadIdx.x, lane = tid & 63;
  const int w = __builtin_amdgcn_readfirstlane(tid >> 6);
  const int l31 = lane & 31, hh = lane >> 5, l15 = lane & 15, g = lane >> 4;
  const int qp = l15 >> 2, p4 = lane & 3, blk = (lane >> 4) & 1;
  const int sb = (int)(uintptr_t)smem;
  volatile unsigned* slot = (volatile unsigned*)(smem + A_OFF_SLOT);

  const unsigned ksrc = (16 * w + g) * 256 + ((l15 ^ g) << 4);
  const unsigned vsrc = (16 * w + g) * 256 + ((l15 ^ (g << 2)) << 4);
  auto stage_kv = [&](int pi, int t, int buf, unsigned ko, unsigned vo) {
    const char* kb = (const char*)kc + (size_t)pi * (SK * DH * 2) + t * 16384;
    const char* vb = (const char*)vc + (size_t)pi * (SK * DH * 2) + t * 16384;
#pragma unroll
    for (int i = 0; i < 4; ++i)
      glds16nt(kb + ((ko ^ (i << 6)) + i * 1024), smem + A_OFF_A + buf * A_BUFA + w * 4096 + i * 1024);
#pragma unroll
    for (int i = 0; i < 4; ++i)
      glds16nt(vb + (vo + i * 1024), smem + A_OFF_B + buf * A_BUFB + w * 4096 + i * 1024);
  };
  auto stage_q = [&](int pi, int t, int buf) {
    int ln = lane;
    OPAQUE(ln);
    const char* xt = (const char*)(x + (size_t)(pi >> 5) * SQ * DM) + t * 128;
    const char* wt = (const char*)(wq + (pi & 31) * DH) + (size_t)t * (64 * HD * 2);
#pragma unroll
    for (int ii = 0; ii < 5; ++ii) {
      int r = 8 * (w * 5 + ii) + (ln >> 3), cp = ln & 7;
      int rs = r < SQ ? r : SQ - 1;
      unsigned so = rs * 1024 + ((cp ^ ((r >> 1) & 7)) << 4);
      glds16(xt + so, smem + A_OFF_A + buf * A_BUFA + w * 5120 + ii * 1024);
    }
    const unsigned wo0 = (16 * w + (ln >> 4)) * (HD * 2) + (((ln & 15) ^ ((ln >> 4) << 2)) << 4);
#pragma unroll
    for (int ii = 0; ii < 4; ++ii)
      glds16(wt + (wo0 + ii * 4 * HD * 2), smem + A_OFF_B + buf * A_BUFB + w * 4096 + ii * 1024);
  };
  int pidx = (blockIdx.x & 7) * 128 + (blockIdx.x >> 3);
#define STAG 0
  if (STAG > 0 && blockIdx.x >= 256) {
    unsigned long long t0 = __builtin_amdgcn_s_memtime();
    while (__builtin_amdgcn_s_memtime() - t0 < STAG) __builtin_amdgcn_s_sleep(8);
  }
  stage_q(pidx, 0, 0);
  if (tid == 0) *slot = blockIdx.x + 512u;
  constexpr float CS = 0.088388347648318440f * 1.4426950408889634f;
  constexpr float THRS = 8.f / CS;

  for (;;) {
  const int b = pidx >> 5, h = pidx & 31;
  f32x16 acc[4];
#pragma unroll
  for (int i = 0; i < 4; ++i) acc[i] = f32x16{};
  f32x4 acc2[8];
#pragma unroll
  for (int i = 0; i < 8; ++i) acc2[i] = f32x4{};
  {
    int lq = lane;
    OPAQUE(lq);
    const int l31 = lq & 31, hh = lq >> 5, l15 = lq & 15, g = lq >> 4;
    const int qp = l15 >> 2, p4 = lq & 3, blk = (lq >> 4) & 1;
    const int xa = sb + A_OFF_A + (32 * w + l31) * 128 + ((hh ^ ((l31 >> 1) & 7)) << 4);
    const int wa = sb + A_OFF_B + (8 * hh + qp) * 256 + (((2 * blk + (p4 >> 1)) ^ (qp << 2)) << 4) + 8 * (p4 & 1);
    const int xa2 = sb + A_OFF_A + (128 + l15) * 128 + ((g ^ ((l15 >> 1) & 7)) << 4);
    const int wa2 = sb + A_OFF_B + (8 * g + qp) * 256 + (((4 * w + (p4 >> 1)) ^ (qp << 2)) << 4) + 8 * (p4 & 1);
    auto body = [&](auto BC, int t) {
      constexpr int buf = decltype(BC)::value;
      constexpr int OA = buf * A_BUFA, OB = buf * A_BUFB;
      asm volatile("s_waitcnt vmcnt(0) lgkmcnt(0)" ::: "memory");
      RAW_BAR();
      if (t < 7) stage_q(pidx, t + 1, buf ^ 1);
      else stage_kv(pidx, 0, buf ^ 1, ksrc, vsrc);
      SBAR();
      const int w0 = wa, w1 = wa ^ 64, w2 = wa ^ 128, w3 = wa ^ 192;
      bf16x8 xf[2];
      s16x4 wl[2][4], wh[2][4];
      bf16x8 xr[2];
      s16x4 rl[2][2], rh[2][2];
      auto issue = [&](auto SC) {
        constexpr int s = decltype(SC)::value, u = s & 1;
        xf[u] = rd128<OA>(xa ^ (s << 5));
        wl[u][0] = rdtr<OB + s * 4096>(w0); wh[u][0] = rdtr<OB + s * 4096 + 1024>(w0);
        wl[u][1] = rdtr<OB + s * 4096>(w1); wh[u][1] = rdtr<OB + s * 4096 + 1024>(w1);
        wl[u][2] = rdtr<OB + s * 4096>(w2); wh[u][2] = rdtr<OB + s * 4096 + 1024>(w2);
        wl[u][3] = rdtr<OB + s * 4096>(w3); wh[u][3] = rdtr<OB + s * 4096 + 1024>(w3);
      };
      issue(IC<0>{});
      sfor<0, 4>([&](auto SC) {
        constexpr int s = decltype(SC)::value, u = s & 1;
        if constexpr (s < 3) {
          issue(IC<s + 1>{});
          asm volatile("s_waitcnt lgkmcnt(9)" : "+v"(xf[u]), "+v"(wl[u][0]), "+v"(wh[u][0]), "+v"(wl[u][1]), "+v"(wh[u][1]),
                       "+v"(wl[u][2]), "+v"(wh[u][2]), "+v"(wl[u][3]), "+v"(wh[u][3]) :: "memory");
        } else {
          xr[0] = rd128<OA>(xa2); xr[1] = rd128<OA>(xa2 ^ 64);
          rl[0][0] = rdtr<OB>(wa2); rh[0][0] = rdtr<OB + 1024>(wa2);
          rl[0][1] = rdtr<OB>(wa2 ^ 32); rh[0][1] = rdtr<OB + 1024>(wa2 ^ 32);
          rl[1][0] = rdtr<OB + 8192>(wa2); rh[1][0] = rdtr<OB + 8192 + 1024>(wa2);
          rl[1][1] = rdtr<OB + 8192>(wa2 ^ 32); rh[1][1] = rdtr<OB + 8192 + 1024>(wa2 ^ 32);
          asm volatile("s_waitcnt lgkmcnt(10)" : "+v"(xf[u]), "+v"(wl[u][0]), "+v"(wh[u][0]), "+v"(wl[u][1]), "+v"(wh[u][1]),
                       "+v"(wl[u][2]), "+v"(wh[u][2]), "+v"(wl[u][3]), "+v"(wh[u][3]) :: "memory");
        }
#pragma unroll
        for (int dt = 0; dt < 4; ++dt) acc[dt] = MFMA32(cat8(wl[u][dt], wh[u][dt]), xf[u], acc[dt]);
        SBAR();
      });
      asm volatile("s_waitcnt lgkmcnt(0)" : "+v"(xr[0]), "+v"(xr[1]), "+v"(rl[0][0]), "+v"(rh[0][0]), "+v"(rl[0][1]), "+v"(rh[0][1]),
                   "+v"(rl[1][0]), "+v"(rh[1][0]), "+v"(rl[1][1]), "+v"(rh[1][1]) :: "memory");
#pragma unroll
      for (int s2 = 0; s2 < 2; ++s2)
#pragma unroll
        for (int e = 0; e < 2; ++e) acc2[e] = MFMA16(cat8(rl[s2][e], rh[s2][e]), xr[s2], acc2[e]);
    };
    for (int t = 0; t < 8; t += 2) {
      body(IC<0>{}, t);
      body(IC<1>{}, t + 1);
    }
  }
  bf16x8 qf[8];
#pragma unroll
  for (int dt = 0; dt < 4; ++dt) {
#pragma unroll
    for (int s = 0; s < 2; ++s) {
      unsigned a0 = cvtpk(acc[dt][8 * s + 0], acc[dt][8 * s + 1]);
      unsigned a1 = cvtpk(acc[dt][8 * s + 2], acc[dt][8 * s + 3]);
      unsigned b0 = cvtpk(acc[dt][8 * s + 4], acc[dt][8 * s + 5]);
      unsigned b1 = cvtpk(acc[dt][8 * s + 6], acc[dt][8 * s + 7]);
      auto r0 = __builtin_amdgcn_permlane32_swap(a0, b0, false, false);
      auto r1 = __builtin_amdgcn_permlane32_swap(a1, b1, false, false);
      u32x4 wv = {r0[0], r1[0], r0[1], r1[1]};
      qf[2 * dt + s] = __builtin_bit_cast(bf16x8, wv);
    }
  }
  {
#pragma unroll
    for (int e = 0; e < 2; ++e) {
      int dd = 2 * w + e;
      u32x2 pk = {cvtpk(acc2[e][0], acc2[e][1]), cvtpk(acc2[e][2], acc2[e][3])};
      int ch = (2 * dd + (g >> 1)) ^ l15;
      asm volatile("ds_write_b64 %0, %1" :: "v"(sb + A_OFF_Q2 + l15 * 256 + (ch << 4) + 8 * (g & 1)), "v"(pk) : "memory");
    }
  }
#pragma unroll
  for (int i = 0; i < 4; ++i) acc[i] = f32x16{};
#pragma unroll
  for (int i = 0; i < 8; ++i) acc2[i] = f32x4{};
  int nidx = 0;
  float m1 = -1e30f, l1 = 0.f, m2 = -1e30f, l2 = 0.f;
  {
    int ka = sb + A_OFF_A + l31 * 256 + ((hh ^ l15) << 4);
    int ka2 = sb + A_OFF_A + (16 * w + l15) * 256 + ((g ^ l15) << 4);
    int q2a = sb + A_OFF_Q2 + l15 * 256 + ((g ^ l15) << 4);
    int va = sb + A_OFF_B + (4 * hh + qp) * 256 + (((2 * blk + (p4 >> 1)) ^ (qp << 2)) << 4) + 8 * (p4 & 1);
    int va2 = sb + A_OFF_B + (16 * w + 4 * g + qp) * 256 + (((p4 >> 1) ^ (qp << 2)) << 4) + 8 * (p4 & 1);
    auto body = [&](auto BC, int t) {
      constexpr int buf = decltype(BC)::value;
      constexpr int OA = buf * A_BUFA, OB = buf * A_BUFB;
      asm volatile("s_waitcnt vmcnt(0) lgkmcnt(0)" ::: "memory");
      RAW_BAR();
      int ka_ = ka, ka2_ = ka2, q2a_ = q2a, va_ = va, va2_ = va2;
      unsigned ko = ksrc, vo = vsrc;
      OPAQUE(ka_); OPAQUE(ka2_); OPAQUE(q2a_); OPAQUE(va_); OPAQUE(va2_); OPAQUE(ko); OPAQUE(vo);
      if (t < 7) {
        stage_kv(pidx, t + 1, buf ^ 1, ko, vo);
      } else {
        const unsigned it = __builtin_amdgcn_readfirstlane(*slot);
        nidx = it < 1024u ? (int)((it & 7) * 128 + (it >> 3)) : -1;
        if (nidx >= 0) stage_q(nidx, 0, 0);
      }
      SBAR();
      f32x16 s0 = f32x16{}, s1 = f32x16{};
      f32x4 sr = f32x4{};
      bf16x8 kA[4], kB[4];
#pragma unroll
      for (int i = 0; i < 4; ++i) kA[i] = rd128<OA>(ka_ ^ (i << 5));
#pragma unroll
      for (int i = 0; i < 4; ++i) kB[i] = rd128<OA>(ka_ ^ ((4 + i) << 5));
      WAITL4(4, kA[0], kA[1], kA[2], kA[3]);
      s0 = MFMA32(kA[0], qf[0], s0);
      s0 = MFMA32(kA[1], qf[1], s0);
      SBAR();
      s0 = MFMA32(kA[2], qf[2], s0);
      s0 = MFMA32(kA[3], qf[3], s0);
      SBAR();
#pragma unroll
      for (int i = 0; i < 4; ++i) kA[i] = rd128<OA + 8192>(ka_ ^ (i << 5));
      WAITL4(4, kB[0], kB[1], kB[2], kB[3]);
      s0 = MFMA32(kB[0], qf[4], s0);
      s0 = MFMA32(kB[1], qf[5], s0);
      SBAR();
      s0 = MFMA32(kB[2], qf[6], s0);
      s0 = MFMA32(kB[3], qf[7], s0);
      SBAR();
#pragma unroll
      for (int i = 0; i < 4; ++i) kB[i] = rd128<OA + 8192>(ka_ ^ ((4 + i) << 5));
      WAITL4(4, kA[0], kA[1], kA[2], kA[3]);
      s1 = MFMA32(kA[0], qf[0], s1);
      SBAR();
      s1 = MFMA32(kA[1], qf[1], s1);
      float tmax = fmaxf(fmaxf(s0[0], s0[1]), s0[2]);
#pragma unroll
      for (int r = 3; r < 9; r += 2) tmax = fmaxf(fmaxf(tmax, s0[r]), s0[r + 1]);
      SBAR();
      s1 = MFMA32(kA[2], qf[2], s1);
#pragma unroll
      for (int r = 9; r < 15; r += 2) tmax = fmaxf(fmaxf(tmax, s0[r]), s0[r + 1]);
      tmax = fmaxf(tmax, s0[15]);
      tmax = xmax32(tmax);
      SBAR();
      s1 = MFMA32(kA[3], qf[3], s1);
      SBAR();
      if (!__all(tmax - m1 <= THRS)) {
        float mn0 = fmaxf(m1, tmax);
        float alpha = __builtin_amdgcn_exp2f((m1 - mn0) * CS);
        m1 = mn0;
        l1 *= alpha;
#pragma unroll
        for (int dt = 0; dt < 4; ++dt)
#pragma unroll
          for (int r = 0; r < 16; ++r) acc[dt][r] *= alpha;
      }
      const float mn = m1;
      bf16x8 kr[4], qr[4];
#pragma unroll
      for (int ks = 0; ks < 4; ++ks) { kr[ks] = rd128<OA>(ka2_ ^ (ks << 6)); qr[ks] = rd128<0>(q2a_ ^ (ks << 6)); }
      WAITL4(8, kB[0], kB[1], kB[2], kB[3]);
      bf16x8 pf[4];
      float mc = -mn * CS;
      float ps = 0.f;
      float e[8];
      s1 = MFMA32(kB[0], qf[4], s1);
#pragma unroll
      for (int j = 0; j < 4; ++j) { e[j] = __builtin_amdgcn_exp2f(fmaf(s0[j], CS, mc)); ps += e[j]; }
      SBAR();
      s1 = MFMA32(kB[1], qf[5], s1);
#pragma unroll
      for (int j = 4; j < 8; ++j) { e[j] = __builtin_amdgcn_exp2f(fmaf(s0[j], CS, mc)); ps += e[j]; }
      { u32x4 a = {cvtpk(e[0], e[1]), cvtpk(e[2], e[3]), cvtpk(e[4], e[5]), cvtpk(e[6], e[7])}; pf[0] = __builtin_bit_cast(bf16x8, a); }
      SBAR();
      s1 = MFMA32(kB[2], qf[6], s1);
#pragma unroll
      for (int j = 0; j < 4; ++j) { e[j] = __builtin_amdgcn_exp2f(fmaf(s0[8 + j], CS, mc)); ps += e[j]; }
      SBAR();
      s1 = MFMA32(kB[3], qf[7], s1);
#pragma unroll
      for (int j = 4; j < 8; ++j) { e[j] = __builtin_amdgcn_exp2f(fmaf(s0[8 + j], CS, mc)); ps += e[j]; }
      { u32x4 a = {cvtpk(e[0], e[1]), cvtpk(e[2], e[3]), cvtpk(e[4], e[5]), cvtpk(e[6], e[7])}; pf[1] = __builtin_bit_cast(bf16x8, a); }
      l1 += ps;
      OPAQUE(l1);
      SBAR();
      SBAR();
      const int v0 = va_, v1 = va_ ^ 64, v2 = va_ ^ 128, v3 = va_ ^ 192;
      s16x4 vl[2][4], vh[2][4];
      auto issue_v = [&](auto SC) {
        constexpr int s = decltype(SC)::value, u = s & 1;
        vl[u][0] = rdtr<OB + s * 4096>(v0); vh[u][0] = rdtr<OB + s * 4096 + 2048>(v0);
        vl[u][1] = rdtr<OB + s * 4096>(v1); vh[u][1] = rdtr<OB + s * 4096 + 2048>(v1);
        vl[u][2] = rdtr<OB + s * 4096>(v2); vh[u][2] = rdtr<OB + s * 4096 + 2048>(v2);
        vl[u][3] = rdtr<OB + s * 4096>(v3); vh[u][3] = rdtr<OB + s * 4096 + 2048>(v3);
      };
      issue_v(IC<0>{});
      WAITL8(8, kr[0], kr[1], kr[2], kr[3], qr[0], qr[1], qr[2], qr[3]);
#pragma unroll
      for (int ks = 0; ks < 4; ++ks) sr = MFMA16(kr[ks], qr[ks], sr);
      SBAR();
      SBAR();
      issue_v(IC<1>{});
      WAITL8(8, vl[0][0], vh[0][0], vl[0][1], vh[0][1], vl[0][2], vh[0][2], vl[0][3], vh[0][3]);
      acc[0] = MFMA32(cat8(vl[0][0], vh[0][0]), pf[0], acc[0]);
      float tm1 = fmaxf(fmaxf(s1[0], s1[1]), s1[2]);
#pragma unroll
      for (int r = 3; r < 9; r += 2) tm1 = fmaxf(fmaxf(tm1, s1[r]), s1[r + 1]);
      SBAR();
      acc[1] = MFMA32(cat8(vl[0][1], vh[0][1]), pf[0], acc[1]);
#pragma unroll
      for (int r = 9; r < 15; r += 2) tm1 = fmaxf(fmaxf(tm1, s1[r]), s1[r + 1]);
      tm1 = fmaxf(tm1, s1[15]);
      tm1 = xmax32(tm1);
      const bool keepb = __all(tm1 - m1 <= THRS);
      float mnb = keepb ? m1 : fmaxf(m1, tm1);
      float alphab = keepb ? 1.f : __builtin_amdgcn_exp2f((m1 - mnb) * CS);
      m1 = mnb;
      mc = -mnb * CS;
      ps = 0.f;
      SBAR();
      acc[2] = MFMA32(cat8(vl[0][2], vh[0][2]), pf[0], acc[2]);
#pragma unroll
      for (int j = 0; j < 4; ++j) { e[j] = __builtin_amdgcn_exp2f(fmaf(s1[j], CS, mc)); ps += e[j]; }
      SBAR();
      acc[3] = MFMA32(cat8(vl[0][3], vh[0][3]), pf[0], acc[3]);
#pragma unroll
      for (int j = 4; j < 8; ++j) { e[j] = __builtin_amdgcn_exp2f(fmaf(s1[j], CS, mc)); ps += e[j]; }
      { u32x4 a = {cvtpk(e[0], e[1]), cvtpk(e[2], e[3]), cvtpk(e[4], e[5]), cvtpk(e[6], e[7])}; pf[2] = __builtin_bit_cast(bf16x8, a); }
      SBAR();
      issue_v(IC<2>{});
      WAITL8(8, vl[1][0], vh[1][0], vl[1][1], vh[1][1], vl[1][2], vh[1][2], vl[1][3], vh[1][3]);
      acc[0] = MFMA32(cat8(vl[1][0], vh[1][0]), pf[1], acc[0]);
#pragma unroll
      for (int j = 0; j < 4; ++j) { e[j] = __builtin_amdgcn_exp2f(fmaf(s1[8 + j], CS, mc)); ps += e[j]; }
      SBAR();
      acc[1] = MFMA32(cat8(vl[1][1], vh[1][1]), pf[1], acc[1]);
#pragma unroll
      for (int j = 4; j < 8; ++j) { e[j] = __builtin_amdgcn_exp2f(fmaf(s1[8 + j], CS, mc)); ps += e[j]; }
      { u32x4 a = {cvtpk(e[0], e[1]), cvtpk(e[2], e[3]), cvtpk(e[4], e[5]), cvtpk(e[6], e[7])}; pf[3] = __builtin_bit_cast(bf16x8, a); }
      l1 = l1 * alphab + ps;
      OPAQUE(l1);
      SBAR();
      acc[2] = MFMA32(cat8(vl[1][2], vh[1][2]), pf[1], acc[2]);
      float t2 = fmaxf(fmaxf(sr[0], sr[1]), fmaxf(sr[2], sr[3]));
      t2 = xmax16(t2);
      t2 = xmax32(t2);
      const bool keep2 = __all(t2 - m2 <= THRS);
      float mn2 = keep2 ? m2 : fmaxf(m2, t2);
      float alpha2 = keep2 ? 1.f : __builtin_amdgcn_exp2f((m2 - mn2) * CS);
      m2 = mn2;
      SBAR();
      acc[3] = MFMA32(cat8(vl[1][3], vh[1][3]), pf[1], acc[3]);
      const float mc2 = -mn2 * CS;
      float e0 = __builtin_amdgcn_exp2f(fmaf(sr[0], CS, mc2));
      float e1 = __builtin_amdgcn_exp2f(fmaf(sr[1], CS, mc2));
      float e2 = __builtin_amdgcn_exp2f(fmaf(sr[2], CS, mc2));
      float e3 = __builtin_amdgcn_exp2f(fmaf(sr[3], CS, mc2));
      l2 = l2 * alpha2 + (e0 + e1) + (e2 + e3);
      OPAQUE(l2);
      u32x2 p2u = {cvtpk(e0, e1), cvtpk(e2, e3)};
      s16x4 p2 = __builtin_bit_cast(s16x4, p2u);
      SBAR();
      if (!keepb) {
#pragma unroll
        for (int dt = 0; dt < 4; ++dt)
#pragma unroll
          for (int r = 0; r < 16; ++r) acc[dt][r] *= alphab;
      }
      if (!keep2) {
#pragma unroll
        for (int dd = 0; dd < 8; ++dd)
#pragma unroll
          for (int r = 0; r < 4; ++r) acc2[dd][r] *= alpha2;
      }
      issue_v(IC<3>{});
      WAITL8(8, vl[0][0], vh[0][0], vl[0][1], vh[0][1], vl[0][2], vh[0][2], vl[0][3], vh[0][3]);
#pragma unroll
      for (int dt = 0; dt < 4; ++dt) acc[dt] = MFMA32(cat8(vl[0][dt], vh[0][dt]), pf[2], acc[dt]);
      SBAR();
      s16x4 vr[8];
#pragma unroll
      for (int dd = 0; dd < 8; ++dd) vr[dd] = rdtr<OB>(va2_ ^ (dd << 5));
      WAITL8(8, vl[1][0], vh[1][0], vl[1][1], vh[1][1], vl[1][2], vh[1][2], vl[1][3], vh[1][3]);
#pragma unroll
      for (int dt = 0; dt < 4; ++dt) acc[dt] = MFMA32(cat8(vl[1][dt], vh[1][dt]), pf[3], acc[dt]);
      WAITL8(0, vr[0], vr[1], vr[2], vr[3], vr[4], vr[5], vr[6], vr[7]);
#pragma unroll
      for (int dd = 0; dd < 8; ++dd) acc2[dd] = MFMA16K16(vr[dd], p2, acc2[dd]);
    };
    for (int t = 0; t < 8; t += 2) {
      body(IC<0>{}, t);
      body(IC<1>{}, t + 1);
    }
  }

  int tz = threadIdx.x;
  OPAQUE(tz);
  asm volatile("s_waitcnt lgkmcnt(0)" ::: "memory");
  RAW_BAR();
  {
    const int l15 = tz & 15, g = (tz >> 4) & 3;
    float lt2 = xsum32(xsum16(l2));
    float* pw = (float*)(smem + ((w & 1) ? A_OFF_B + A_BUFB : A_OFF_A + A_BUFA) + (w >> 1) * 8192);
#pragma unroll
    for (int dd = 0; dd < 8; ++dd) *(f32x4*)(pw + l15 * 128 + 16 * dd + 4 * g) = acc2[dd];
    float* st = (float*)(smem + A_OFF_Q2);
    if (g == 0) { st[w * 32 + l15] = m2; st[w * 32 + 16 + l15] = lt2; }
  }
  asm volatile("s_waitcnt lgkmcnt(0)" ::: "memory");
  RAW_BAR();
  {
    const int q = tz >> 4, d0 = (tz & 15) * 8;
    const float* st = (const float*)(smem + A_OFF_Q2);
    float mm = fmaxf(fmaxf(st[q], st[32 + q]), fmaxf(st[64 + q], st[96 + q]));
    float o[8] = {0, 0, 0, 0, 0, 0, 0, 0};
    float ls = 0.f;
#pragma unroll
    for (int ww = 0; ww < 4; ++ww) {
      float sc = __builtin_amdgcn_exp2f((st[ww * 32 + q] - mm) * CS);
      ls += st[ww * 32 + 16 + q] * sc;
      const float* pw = (const float*)(smem + ((ww & 1) ? A_OFF_B + A_BUFB : A_OFF_A + A_BUFA) + (ww >> 1) * 8192) + q * 128 + d0;
      f32x4 a = *(const f32x4*)pw, c = *(const f32x4*)(pw + 4);
#pragma unroll
      for (int j = 0; j < 4; ++j) { o[j] += a[j] * sc; o[4 + j] += c[j] * sc; }
    }
    float inv = 1.f / ls;
    u32x4 pk = {cvtpk(o[0] * inv, o[1] * inv), cvtpk(o[2] * inv, o[3] * inv),
                cvtpk(o[4] * inv, o[5] * inv), cvtpk(o[6] * inv, o[7] * inv)};
    *(u32x4*)(og + (size_t)(b * SQ + 128 + q) * HD + h * DH + d0) = pk;
  }
  asm volatile("s_waitcnt lgkmcnt(0)" ::: "memory");
  RAW_BAR();
  {
    const int l31 = tz & 31, hh = (tz >> 5) & 1;
    float lt = xsum32(l1);
    float inv = 1.f / lt;
    char* ob = smem + (w < 2 ? A_OFF_A + A_BUFA : A_OFF_B + A_BUFB) + ((w & 1) * 32 + l31) * 256 + 8 * hh;
#pragma unroll
    for (int dt = 0; dt < 4; ++dt)
#pragma unroll
      for (int i = 0; i < 4; ++i) {
        u32x2 pk = {cvtpk(acc[dt][4 * i] * inv, acc[dt][4 * i + 1] * inv),
                    cvtpk(acc[dt][4 * i + 2] * inv, acc[dt][4 * i + 3] * inv)};
        *(u32x2*)(ob + (((4 * dt + i) ^ (l31 & 15)) << 4)) = pk;
      }
    if (tz == 0 && nidx >= 0) *slot = 4096u;
  }
  asm volatile("s_waitcnt lgkmcnt(0)" ::: "memory");
  RAW_BAR();
  {
    const int c = tz & 15, r0 = tz >> 4;
    short* orow = og + (size_t)(b * SQ + r0) * HD + h * DH + c * 8;
#pragma unroll
    for (int j = 0; j < 8; ++j) {
      const int r = r0 + 16 * j;
      const char* src = smem + (j < 4 ? A_OFF_A + A_BUFA + r * 256 : A_OFF_B + A_BUFB + (r - 64) * 256) + ((c ^ r0) << 4);
      bf16x8 v = *(const bf16x8*)src;
      *(bf16x8*)(orow + (size_t)(16 * j) * HD) = v;
    }
  }
  if (nidx < 0) break;
  pidx = nidx;
  }
}

constexpr int B_NST = 5;
constexpr int B_BUFA = 20480;
constexpr int B_BUFB = 8192;
constexpr int B_STRIDE = B_BUFA + B_BUFB;
constexpr int B_LDS = B_NST * B_STRIDE;

__global__ __launch_bounds__(768, 3) void oproj_kernel(const short* __restrict__ og,
                                                        const short* __restrict__ wo,
                                                        short* __restrict__ out) {
  extern __shared__ __attribute__((aligned(16))) char smem[];
  const int tid = threadIdx.x, lane = tid & 63;
  const int wv = __builtin_amdgcn_readfirstlane(tid >> 6);
  const int w = wv & 3, grp = wv >> 2;
  const int xg = blockIdx.x & 7, idx = blockIdx.x >> 3;
  const int b = xg * 4 + (idx >> 3), nt0 = idx & 7;
  const int l31 = lane & 31, hh = lane >> 5, l15 = lane & 15, g = lane >> 4;
  const int qp = l15 >> 2, p4 = lane & 3, blk = (lane >> 4) & 1;
  constexpr int NT = HD / 64;
  if (grp == 2) {
    const char* ob = (const char*)(og + (size_t)b * SQ * HD);
    const char* wob = (const char*)(wo + nt0 * 64);
    unsigned osrc[5], wsrc[2];
#pragma unroll
    for (int ii = 0; ii < 5; ++ii) {
      int r = 8 * (w * 5 + ii) + (lane >> 3), cp = lane & 7;
      int rs = r < SQ ? r : SQ - 1;
      osrc[ii] = rs * (HD * 2) + ((cp ^ ((r >> 1) & 7)) << 4);
    }
#pragma unroll
    for (int ii = 0; ii < 2; ++ii) {
      int r = 8 * (w * 2 + ii) + (lane >> 3), cp = lane & 7;
      wsrc[ii] = r * (DM * 2) + ((cp ^ (((r >> 1) & 1) << 2)) << 4);
    }
    auto stage = [&](int t) {
      const int buf = t % B_NST;
      char* la = smem + buf * B_STRIDE + w * 5120;
      char* lb = smem + buf * B_STRIDE + B_BUFA + w * 2048;
      const char* ot = ob + t * 128;
      const char* wt = wob + (size_t)t * (64 * DM * 2);
#pragma unroll
      for (int ii = 0; ii < 5; ++ii) glds16(ot + osrc[ii], la + ii * 1024);
#pragma unroll
      for (int ii = 0; ii < 2; ++ii) glds16(wt + wsrc[ii], lb + ii * 1024);
    };
    stage(0); stage(1); stage(2);
    asm volatile("s_waitcnt vmcnt(14)" ::: "memory");
    for (int t = 0; t <= NT; ++t) {
      RAW_BAR();
      if (t + 3 < NT) { stage(t + 3); asm volatile("s_waitcnt vmcnt(14)" ::: "memory"); }
      else if (t + 2 < NT) asm volatile("s_waitcnt vmcnt(7)" ::: "memory");
      else asm volatile("s_waitcnt vmcnt(0)" ::: "memory");
    }
    RAW_BAR();
    RAW_BAR();
    return;
  }
  f32x16 acc[2] = {f32x16{}, f32x16{}};
  f32x4 acc2 = f32x4{};
  const int sb = (int)(uintptr_t)smem;
  const int xa = sb + (32 * w + l31) * 128 + ((hh ^ ((l31 >> 1) & 7)) << 4);
  const int wa = sb + B_BUFA + (8 * hh + qp) * 128 + (((2 * blk + (p4 >> 1)) ^ ((qp >> 1) << 2)) << 4) + 8 * (p4 & 1);
  const int xa2 = sb + (128 + l15) * 128 + ((g ^ ((l15 >> 1) & 7)) << 4);
  const int wa2 = sb + B_BUFA + (8 * g + qp) * 128 + (((2 * w + (p4 >> 1)) ^ ((qp >> 1) << 2)) << 4) + 8 * (p4 & 1);
  auto half = [&](auto HC, int bo) {
    constexpr int hf = decltype(HC)::value;
    const int xa_ = xa + bo, w0 = wa + bo, w1 = (wa ^ 64) + bo, xa2_ = xa2 + bo, wa2_ = wa2 + bo;
    bf16x8 x0 = rd128<0>(xa_ ^ ((2 * hf) << 5));
    s16x4 a0 = rdtr<(2 * hf) * 2048>(w0), a1 = rdtr<(2 * hf) * 2048 + 512>(w0);
    s16x4 b0 = rdtr<(2 * hf) * 2048>(w1), b1 = rdtr<(2 * hf) * 2048 + 512>(w1);
    bf16x8 x1 = rd128<0>(xa_ ^ ((2 * hf + 1) << 5));
    s16x4 c0 = rdtr<(2 * hf + 1) * 2048>(w0), c1 = rdtr<(2 * hf + 1) * 2048 + 512>(w0);
    s16x4 d0 = rdtr<(2 * hf + 1) * 2048>(w1), d1 = rdtr<(2 * hf + 1) * 2048 + 512>(w1);
    bf16x8 xr = rd128<0>(xa2_ ^ (hf << 6));
    s16x4 r0 = rdtr<hf * 4096>(wa2_), r1 = rdtr<hf * 4096 + 512>(wa2_);
    asm volatile("s_waitcnt lgkmcnt(8)" : "+v"(x0), "+v"(a0), "+v"(a1), "+v"(b0), "+v"(b1) :: "memory");
    acc[0] = MFMA32(cat8(a0, a1), x0, acc[0]);
    acc[1] = MFMA32(cat8(b0, b1), x0, acc[1]);
    asm volatile("s_waitcnt lgkmcnt(3)" : "+v"(x1), "+v"(c0), "+v"(c1), "+v"(d0), "+v"(d1) :: "memory");
    acc[0] = MFMA32(cat8(c0, c1), x1, acc[0]);
    acc[1] = MFMA32(cat8(d0, d1), x1, acc[1]);
    asm volatile("s_waitcnt lgkmcnt(0)" : "+v"(xr), "+v"(r0), "+v"(r1) :: "memory");
    acc2 = MFMA16(cat8(r0, r1), xr, acc2);
  };
  if (grp == 1) RAW_BAR();
  int bo = grp * B_STRIDE;
  for (int t = grp; t < NT; t += 2) {
    RAW_BAR();
    half(IC<0>{}, bo);
    RAW_BAR();
    half(IC<1>{}, bo);
    bo += 2 * B_STRIDE;
    if (bo >= B_NST * B_STRIDE) bo -= B_NST * B_STRIDE;
  }
  if (grp == 0) RAW_BAR();
  RAW_BAR();
  float* red = (float*)smem + (w * 64 + lane) * 36;
  if (grp == 1) {
#pragma unroll
    for (int i = 0; i < 4; ++i) {
      *(f32x4*)(red + 4 * i) = f32x4{acc[0][4 * i], acc[0][4 * i + 1], acc[0][4 * i + 2], acc[0][4 * i + 3]};
      *(f32x4*)(red + 16 + 4 * i) = f32x4{acc[1][4 * i], acc[1][4 * i + 1], acc[1][4 * i + 2], acc[1][4 * i + 3]};
    }
    *(f32x4*)(red + 32) = acc2;
    asm volatile("s_waitcnt lgkmcnt(0)" ::: "memory");
  }
  RAW_BAR();
  if (grp == 0) {
#pragma unroll
    for (int i = 0; i < 4; ++i) {
      f32x4 u = *(const f32x4*)(red + 4 * i), v = *(const f32x4*)(red + 16 + 4 * i);
#pragma unroll
      for (int j = 0; j < 4; ++j) { acc[0][4 * i + j] += u[j]; acc[1][4 * i + j] += v[j]; }
    }
    f32x4 u = *(const f32x4*)(red + 32);
#pragma unroll
    for (int j = 0; j < 4; ++j) acc2[j] += u[j];
    short* orow = out + (size_t)(b * SQ + 32 * w + l31) * DM + nt0 * 64 + 4 * hh;
#pragma unroll
    for (int nt = 0; nt < 2; ++nt)
#pragma unroll
      for (int i = 0; i < 4; ++i) {
        u32x2 pk = {cvtpk(acc[nt][4 * i], acc[nt][4 * i + 1]), cvtpk(acc[nt][4 * i + 2], acc[nt][4 * i + 3])};
        *(u32x2*)(orow + 32 * nt + 8 * i) = pk;
      }
    u32x2 pk = {cvtpk(acc2[0], acc2[1]), cvtpk(acc2[2], acc2[3])};
    *(u32x2*)(out + (size_t)(b * SQ + 128 + l15) * DM + nt0 * 64 + 16 * w + 4 * g) = pk;
  }
}

extern "C" void kernel_launch(void* const* d_in, const int* in_sizes, int n_in, void* d_out,
                              int out_size, void* d_ws, size_t ws_size, hipStream_t stream) {
  const short* x = (const short*)d_in[0];
  const short* kc = (const short*)d_in[1];
  const short* vc = (const short*)d_in[2];
  const short* wq = (const short*)d_in[3];
  const short* wo = (const short*)d_in[4];
  short* og = (short*)d_ws;
  short* out = (short*)d_out;
  unsigned* counter = (unsigned*)(og + (size_t)MROWS * HD);
  (void)hipFuncSetAttribute((const void*)attn_kernel, hipFuncAttributeMaxDynamicSharedMemorySize, A_LDS);
  (void)hipFuncSetAttribute((const void*)oproj_kernel, hipFuncAttributeMaxDynamicSharedMemorySize, B_LDS);
  attn_kernel<<<512, 256, A_LDS, stream>>>(x, wq, kc, vc, og, counter);
  oproj_kernel<<<256, 768, B_LDS, stream>>>(og, wo, out);
}
```

```cpp
#include <hip/hip_runtime.h>
#include <hip/hip_bf16.h>

typedef __attribute__((ext_vector_type(8))) short bf16x8;
typedef __attribute__((ext_vector_type(4))) short s16x4;
typedef __attribute__((ext_vector_type(16))) float f32x16;
typedef __attribute__((ext_vector_type(4))) float f32x4;
typedef __attribute__((ext_vector_type(4))) unsigned u32x4;
typedef __attribute__((ext_vector_type(2))) unsigned u32x2;

constexpr int NB = 32, SQ = 144, DM = 512, NH = 32, SK = 512, DH = 128;
constexpr int HD = NH * DH;
constexpr int MROWS = NB * SQ;

#define MFMA32(a, b, c) __builtin_amdgcn_mfma_f32_32x32x16_bf16((a), (b), (c), 0, 0, 0)
#define MFMA16(a, b, c) __builtin_amdgcn_mfma_f32_16x16x32_bf16((a), (b), (c), 0, 0, 0)
#define MFMA16K16(a, b, c) __builtin_amdgcn_mfma_f32_16x16x16bf16_1k((a), (b), (c), 0, 0, 0)

__device__ __forceinline__ unsigned cvtpk(float lo, float hi) {
  unsigned r;
  asm volatile("v_cvt_pk_bf16_f32 %0, %1, %2" : "=v"(r) : "v"(lo), "v"(hi));
  return r;
}
__device__ __forceinline__ bf16x8 cat8(s16x4 lo, s16x4 hi) {
  return __builtin_shufflevector(lo, hi, 0, 1, 2, 3, 4, 5, 6, 7);
}
__device__ __forceinline__ void glds16(const void* g, char* l) {
  __builtin_amdgcn_global_load_lds((const unsigned*)g, (unsigned*)l, 16, 0, 0);
}
__device__ __forceinline__ void glds16nt(const void* g, char* l) {
  __builtin_amdgcn_global_load_lds((const unsigned*)g, (unsigned*)l, 16, 0, 2);
}
__device__ __forceinline__ float xmax32(float v) {
  auto rr = __builtin_amdgcn_permlane32_swap(__float_as_uint(v), __float_as_uint(v), false, false);
  return fmaxf(__uint_as_float(rr[0]), __uint_as_float(rr[1]));
}
__device__ __forceinline__ float xsum32(float v) {
  auto rr = __builtin_amdgcn_permlane32_swap(__float_as_uint(v), __float_as_uint(v), false, false);
  return __uint_as_float(rr[0]) + __uint_as_float(rr[1]);
}
__device__ __forceinline__ float xmax16(float v) {
  auto rr = __builtin_amdgcn_permlane16_swap(__float_as_uint(v), __float_as_uint(v), false, false);
  return fmaxf(__uint_as_float(rr[0]), __uint_as_float(rr[1]));
}
__device__ __forceinline__ float xsum16(float v) {
  auto rr = __builtin_amdgcn_permlane16_swap(__float_as_uint(v), __float_as_uint(v), false, false);
  return __uint_as_float(rr[0]) + __uint_as_float(rr[1]);
}
template <int OFF> __device__ __forceinline__ bf16x8 rd128(int a) {
  bf16x8 r;
  asm volatile("ds_read_b128 %0, %1 offset:%2" : "=v"(r) : "v"(a), "i"(OFF) : "memory");
  return r;
}
template <int OFF> __device__ __forceinline__ s16x4 rdtr(int a) {
  s16x4 r;
  asm volatile("ds_read_b64_tr_b16 %0, %1 offset:%2" : "=v"(r) : "v"(a), "i"(OFF) : "memory");
  return r;
}
#define WAITL2(N, a, b) asm volatile("s_waitcnt lgkmcnt(" #N ")" : "+v"(a), "+v"(b) :: "memory")
#define WAITL4(N, a, b, c, d) asm volatile("s_waitcnt lgkmcnt(" #N ")" : "+v"(a), "+v"(b), "+v"(c), "+v"(d) :: "memory")
#define WAITL8(N, a, b, c, d, e, f, g_, h_) asm volatile("s_waitcnt lgkmcnt(" #N ")" : "+v"(a), "+v"(b), "+v"(c), "+v"(d), "+v"(e), "+v"(f), "+v"(g_), "+v"(h_) :: "memory")
#define RAW_BAR() __builtin_amdgcn_s_barrier()
#define SBAR() __builtin_amdgcn_sched_barrier(0)
#define OPAQUE(v) asm volatile("" : "+v"(v))
template <int N> struct IC { static constexpr int value = N; };
template <int I, int N, class F> __device__ __forceinline__ void sfor(F&& f) {
  if constexpr (I < N) { f(IC<I>{}); sfor<I + 1, N>(f); }
}

constexpr int A_BUFA = 20480;
constexpr int A_BUFB = 16384;
constexpr int A_OFF_A = 0;
constexpr int A_OFF_B = 2 * A_BUFA;
constexpr int A_OFF_Q2 = A_OFF_B + 2 * A_BUFB;
constexpr int A_OFF_SLOT = A_OFF_Q2 + 4096;
constexpr int A_LDS = A_OFF_SLOT + 16;

__global__ __launch_bounds__(256, 2) void attn_kernel(
    const short* __restrict__ x, const short* __restrict__ wq, const short* __restrict__ kc,
    const short* __restrict__ vc, short* __restrict__ og, unsigned* __restrict__ counter) {
  extern __shared__ __attribute__((aligned(16))) char smem[];
  const int tid = threadIdx.x, lane = tid & 63;
  const int w = __builtin_amdgcn_readfirstlane(tid >> 6);
  const int l31 = lane & 31, hh = lane >> 5, l15 = lane & 15, g = lane >> 4;
  const int qp = l15 >> 2, p4 = lane & 3, blk = (lane >> 4) & 1;
  const int sb = (int)(uintptr_t)smem;
  volatile unsigned* slot = (volatile unsigned*)(smem + A_OFF_SLOT);

  const unsigned ksrc = (16 * w + g) * 256 + ((l15 ^ g) << 4);
  const unsigned vsrc = (16 * w + g) * 256 + ((l15 ^ (g << 2)) << 4);
  auto stage_kv = [&](int pi, int t, int buf, unsigned ko, unsigned vo) {
    const char* kb = (const char*)kc + (size_t)pi * (SK * DH * 2) + t * 16384;
    const char* vb = (const char*)vc + (size_t)pi * (SK * DH * 2) + t * 16384;
#pragma unroll
    for (int i = 0; i < 4; ++i)
      glds16nt(kb + ((ko ^ (i << 6)) + i * 1024), smem + A_OFF_A + buf * A_BUFA + w * 4096 + i * 1024);
#pragma unroll
    for (int i = 0; i < 4; ++i)
      glds16nt(vb + (vo + i * 1024), smem + A_OFF_B + buf * A_BUFB + w * 4096 + i * 1024);
  };
  auto stage_q = [&](int pi, int t, int buf) {
    int ln = lane;
    OPAQUE(ln);
    const char* xt = (const char*)(x + (size_t)(pi >> 5) * SQ * DM) + t * 128;
    const char* wt = (const char*)(wq + (pi & 31) * DH) + (size_t)t * (64 * HD * 2);
#pragma unroll
    for (int ii = 0; ii < 5; ++ii) {
      int r = 8 * (w * 5 + ii) + (ln >> 3), cp = ln & 7;
      int rs = r < SQ ? r : SQ - 1;
      unsigned so = rs * 1024 + ((cp ^ ((r >> 1) & 7)) << 4);
      glds16(xt + so, smem + A_OFF_A + buf * A_BUFA + w * 5120 + ii * 1024);
    }
    const unsigned wo0 = (16 * w + (ln >> 4)) * (HD * 2) + (((ln & 15) ^ ((ln >> 4) << 2)) << 4);
#pragma unroll
    for (int ii = 0; ii < 4; ++ii)
      glds16(wt + (wo0 + ii * 4 * HD * 2), smem + A_OFF_B + buf * A_BUFB + w * 4096 + ii * 1024);
  };
  int pidx = (blockIdx.x & 7) * 128 + (blockIdx.x >> 3);
#define STAG 0
  if (STAG > 0 && blockIdx.x >= 256) {
    unsigned long long t0 = __builtin_amdgcn_s_memtime();
    while (__builtin_amdgcn_s_memtime() - t0 < STAG) __builtin_amdgcn_s_sleep(8);
  }
  stage_q(pidx, 0, 0);
  if (tid == 0) *slot = blockIdx.x + 512u;
  constexpr float CS = 0.088388347648318440f * 1.4426950408889634f;
  constexpr float THRS = 8.f / CS;

  for (;;) {
  const int b = pidx >> 5, h = pidx & 31;
  f32x16 acc[4];
#pragma unroll
  for (int i = 0; i < 4; ++i) acc[i] = f32x16{};
  f32x4 acc2[8];
#pragma unroll
  for (int i = 0; i < 8; ++i) acc2[i] = f32x4{};
  {
    int lq = lane;
    OPAQUE(lq);
    const int l31 = lq & 31, hh = lq >> 5, l15 = lq & 15, g = lq >> 4;
    const int qp = l15 >> 2, p4 = lq & 3, blk = (lq >> 4) & 1;
    const int xa = sb + A_OFF_A + (32 * w + l31) * 128 + ((hh ^ ((l31 >> 1) & 7)) << 4);
    const int wa = sb + A_OFF_B + (8 * hh + qp) * 256 + (((2 * blk + (p4 >> 1)) ^ (qp << 2)) << 4) + 8 * (p4 & 1);
    const int xa2 = sb + A_OFF_A + (128 + l15) * 128 + ((g ^ ((l15 >> 1) & 7)) << 4);
    const int wa2 = sb + A_OFF_B + (8 * g + qp) * 256 + (((4 * w + (p4 >> 1)) ^ (qp << 2)) << 4) + 8 * (p4 & 1);
    auto body = [&](auto BC, int t) {
      constexpr int buf = decltype(BC)::value;
      constexpr int OA = buf * A_BUFA, OB = buf * A_BUFB;
      asm volatile("s_waitcnt vmcnt(0) lgkmcnt(0)" ::: "memory");
      RAW_BAR();
      if (t < 7) stage_q(pidx, t + 1, buf ^ 1);
      else stage_kv(pidx, 0, buf ^ 1, ksrc, vsrc);
      SBAR();
      const int w0 = wa, w1 = wa ^ 64, w2 = wa ^ 128, w3 = wa ^ 192;
      bf16x8 xf[2];
      s16x4 wl[2][4], wh[2][4];
      bf16x8 xr[2];
      s16x4 rl[2][2], rh[2][2];
      auto issue = [&](auto SC) {
        constexpr int s = decltype(SC)::value, u = s & 1;
        xf[u] = rd128<OA>(xa ^ (s << 5));
        wl[u][0] = rdtr<OB + s * 4096>(w0); wh[u][0] = rdtr<OB + s * 4096 + 1024>(w0);
        wl[u][1] = rdtr<OB + s * 4096>(w1); wh[u][1] = rdtr<OB + s * 4096 + 1024>(w1);
        wl[u][2] = rdtr<OB + s * 4096>(w2); wh[u][2] = rdtr<OB + s * 4096 + 1024>(w2);
        wl[u][3] = rdtr<OB + s * 4096>(w3); wh[u][3] = rdtr<OB + s * 4096 + 1024>(w3);
      };
      issue(IC<0>{});
      sfor<0, 4>([&](auto SC) {
        constexpr int s = decltype(SC)::value, u = s & 1;
        if constexpr (s < 3) {
          issue(IC<s + 1>{});
          asm volatile("s_waitcnt lgkmcnt(9)" : "+v"(xf[u]), "+v"(wl[u][0]), "+v"(wh[u][0]), "+v"(wl[u][1]), "+v"(wh[u][1]),
                       "+v"(wl[u][2]), "+v"(wh[u][2]), "+v"(wl[u][3]), "+v"(wh[u][3]) :: "memory");
        } else {
          xr[0] = rd128<OA>(xa2); xr[1] = rd128<OA>(xa2 ^ 64);
          rl[0][0] = rdtr<OB>(wa2); rh[0][0] = rdtr<OB + 1024>(wa2);
          rl[0][1] = rdtr<OB>(wa2 ^ 32); rh[0][1] = rdtr<OB + 1024>(wa2 ^ 32);
          rl[1][0] = rdtr<OB + 8192>(wa2); rh[1][0] = rdtr<OB + 8192 + 1024>(wa2);
          rl[1][1] = rdtr<OB + 8192>(wa2 ^ 32); rh[1][1] = rdtr<OB + 8192 + 1024>(wa2 ^ 32);
          asm volatile("s_waitcnt lgkmcnt(10)" : "+v"(xf[u]), "+v"(wl[u][0]), "+v"(wh[u][0]), "+v"(wl[u][1]), "+v"(wh[u][1]),
                       "+v"(wl[u][2]), "+v"(wh[u][2]), "+v"(wl[u][3]), "+v"(wh[u][3]) :: "memory");
        }
#pragma unroll
        for (int dt = 0; dt < 4; ++dt) acc[dt] = MFMA32(cat8(wl[u][dt], wh[u][dt]), xf[u], acc[dt]);
        SBAR();
      });
      asm volatile("s_waitcnt lgkmcnt(0)" : "+v"(xr[0]), "+v"(xr[1]), "+v"(rl[0][0]), "+v"(rh[0][0]), "+v"(rl[0][1]), "+v"(rh[0][1]),
                   "+v"(rl[1][0]), "+v"(rh[1][0]), "+v"(rl[1][1]), "+v"(rh[1][1]) :: "memory");
#pragma unroll
      for (int s2 = 0; s2 < 2; ++s2)
#pragma unroll
        for (int e = 0; e < 2; ++e) acc2[e] = MFMA16(cat8(rl[s2][e], rh[s2][e]), xr[s2], acc2[e]);
    };
    for (int t = 0; t < 8; t += 2) {
      body(IC<0>{}, t);
      body(IC<1>{}, t + 1);
    }
  }
  bf16x8 qf[8];
#pragma unroll
  for (int dt = 0; dt < 4; ++dt) {
#pragma unroll
    for (int s = 0; s < 2; ++s) {
      unsigned a0 = cvtpk(acc[dt][8 * s + 0], acc[dt][8 * s + 1]);
      unsigned a1 = cvtpk(acc[dt][8 * s + 2], acc[dt][8 * s + 3]);
      unsigned b0 = cvtpk(acc[dt][8 * s + 4], acc[dt][8 * s + 5]);
      unsigned b1 = cvtpk(acc[dt][8 * s + 6], acc[dt][8 * s + 7]);
      auto r0 = __builtin_amdgcn_permlane32_swap(a0, b0, false, false);
      auto r1 = __builtin_amdgcn_permlane32_swap(a1, b1, false, false);
      u32x4 wv = {r0[0], r1[0], r0[1], r1[1]};
      qf[2 * dt + s] = __builtin_bit_cast(bf16x8, wv);
    }
  }
  {
#pragma unroll
    for (int e = 0; e < 2; ++e) {
      int dd = 2 * w + e;
      u32x2 pk = {cvtpk(acc2[e][0], acc2[e][1]), cvtpk(acc2[e][2], acc2[e][3])};
      int ch = (2 * dd + (g >> 1)) ^ l15;
      asm volatile("ds_write_b64 %0, %1" :: "v"(sb + A_OFF_Q2 + l15 * 256 + (ch << 4) + 8 * (g & 1)), "v"(pk) : "memory");
    }
  }
#pragma unroll
  for (int i = 0; i < 4; ++i) acc[i] = f32x16{};
#pragma unroll
  for (int i = 0; i < 8; ++i) acc2[i] = f32x4{};
  int nidx = 0;
  float m1 = -1e30f, l1 = 0.f, m2 = -1e30f, l2 = 0.f;
  {
    int ka = sb + A_OFF_A + l31 * 256 + ((hh ^ l15) << 4);
    int ka2 = sb + A_OFF_A + (16 * w + l15) * 256 + ((g ^ l15) << 4);
    int q2a = sb + A_OFF_Q2 + l15 * 256 + ((g ^ l15) << 4);
    int va = sb + A_OFF_B + (4 * hh + qp) * 256 + (((2 * blk + (p4 >> 1)) ^ (qp << 2)) << 4) + 8 * (p4 & 1);
    int va2 = sb + A_OFF_B + (16 * w + 4 * g + qp) * 256 + (((p4 >> 1) ^ (qp << 2)) << 4) + 8 * (p4 & 1);
    auto body = [&](auto BC, int t) {
      constexpr int buf = decltype(BC)::value;
      constexpr int OA = buf * A_BUFA, OB = buf * A_BUFB;
      asm volatile("s_waitcnt vmcnt(0) lgkmcnt(0)" ::: "memory");
      RAW_BAR();
      int ka_ = ka, ka2_ = ka2, q2a_ = q2a, va_ = va, va2_ = va2;
      unsigned ko = ksrc, vo = vsrc;
      OPAQUE(ka_); OPAQUE(ka2_); OPAQUE(q2a_); OPAQUE(va_); OPAQUE(va2_); OPAQUE(ko); OPAQUE(vo);
      if (t < 7) {
        stage_kv(pidx, t + 1, buf ^ 1, ko, vo);
      } else {
        const unsigned it = __builtin_amdgcn_readfirstlane(*slot);
        nidx = it < 1024u ? (int)((it & 7) * 128 + (it >> 3)) : -1;
        if (nidx >= 0) stage_q(nidx, 0, 0);
      }
      SBAR();
      f32x16 s0 = f32x16{}, s1 = f32x16{};
      f32x4 sr = f32x4{};
      bf16x8 kA[4], kB[4];
#pragma unroll
      for (int i = 0; i < 4; ++i) kA[i] = rd128<OA>(ka_ ^ (i << 5));
#pragma unroll
      for (int i = 0; i < 4; ++i) kB[i] = rd128<OA>(ka_ ^ ((4 + i) << 5));
      WAITL4(4, kA[0], kA[1], kA[2], kA[3]);
      s0 = MFMA32(kA[0], qf[0], s0);
      s0 = MFMA32(kA[1], qf[1], s0);
      SBAR();
      s0 = MFMA32(kA[2], qf[2], s0);
      s0 = MFMA32(kA[3], qf[3], s0);
      SBAR();
#pragma unroll
      for (int i = 0; i < 4; ++i) kA[i] = rd128<OA + 8192>(ka_ ^ (i << 5));
      WAITL4(4, kB[0], kB[1], kB[2], kB[3]);
      s0 = MFMA32(kB[0], qf[4], s0);
      s0 = MFMA32(kB[1], qf[5], s0);
      SBAR();
      s0 = MFMA32(kB[2], qf[6], s0);
      s0 = MFMA32(kB[3], qf[7], s0);
      SBAR();
#pragma unroll
      for (int i = 0; i < 4; ++i) kB[i] = rd128<OA + 8192>(ka_ ^ ((4 + i) << 5));
      WAITL4(4, kA[0], kA[1], kA[2], kA[3]);
      s1 = MFMA32(kA[0], qf[0], s1);
      SBAR();
      s1 = MFMA32(kA[1], qf[1], s1);
      float tmax = fmaxf(fmaxf(s0[0], s0[1]), s0[2]);
#pragma unroll
      for (int r = 3; r < 9; r += 2) tmax = fmaxf(fmaxf(tmax, s0[r]), s0[r + 1]);
      SBAR();
      s1 = MFMA32(kA[2], qf[2], s1);
#pragma unroll
      for (int r = 9; r < 15; r += 2) tmax = fmaxf(fmaxf(tmax, s0[r]), s0[r + 1]);
      tmax = fmaxf(tmax, s0[15]);
      tmax = xmax32(tmax);
      SBAR();
      s1 = MFMA32(kA[3], qf[3], s1);
      SBAR();
      if (!__all(tmax - m1 <= THRS)) {
        float mn0 = fmaxf(m1, tmax);
        float alpha = __builtin_amdgcn_exp2f((m1 - mn0) * CS);
        m1 = mn0;
        l1 *= alpha;
#pragma unroll
        for (int dt = 0; dt < 4; ++dt)
#pragma unroll
          for (int r = 0; r < 16; ++r) acc[dt][r] *= alpha;
      }
      const float mn = m1;
      bf16x8 kr[4], qr[4];
#pragma unroll
      for (int ks = 0; ks < 4; ++ks) { kr[ks] = rd128<OA>(ka2_ ^ (ks << 6)); qr[ks] = rd128<0>(q2a_ ^ (ks << 6)); }
      WAITL4(8, kB[0], kB[1], kB[2], kB[3]);
      bf16x8 pf[4];
      float mc = -mn * CS;
      float ps = 0.f;
      float e[8];
      s1 = MFMA32(kB[0], qf[4], s1);
#pragma unroll
      for (int j = 0; j < 4; ++j) { e[j] = __builtin_amdgcn_exp2f(fmaf(s0[j], CS, mc)); ps += e[j]; }
      SBAR();
      s1 = MFMA32(kB[1], qf[5], s1);
#pragma unroll
      for (int j = 4; j < 8; ++j) { e[j] = __builtin_amdgcn_exp2f(fmaf(s0[j], CS, mc)); ps += e[j]; }
      { u32x4 a = {cvtpk(e[0], e[1]), cvtpk(e[2], e[3]), cvtpk(e[4], e[5]), cvtpk(e[6], e[7])}; pf[0] = __builtin_bit_cast(bf16x8, a); }
      SBAR();
      s1 = MFMA32(kB[2], qf[6], s1);
#pragma unroll
      for (int j = 0; j < 4; ++j) { e[j] = __builtin_amdgcn_exp2f(fmaf(s0[8 + j], CS, mc)); ps += e[j]; }
      SBAR();
      s1 = MFMA32(kB[3], qf[7], s1);
#pragma unroll
      for (int j = 4; j < 8; ++j) { e[j] = __builtin_amdgcn_exp2f(fmaf(s0[8 + j], CS, mc)); ps += e[j]; }
      { u32x4 a = {cvtpk(e[0], e[1]), cvtpk(e[2], e[3]), cvtpk(e[4], e[5]), cvtpk(e[6], e[7])}; pf[1] = __builtin_bit_cast(bf16x8, a); }
      l1 += ps;
      OPAQUE(l1);
      SBAR();
      SBAR();
      const int v0 = va_, v1 = va_ ^ 64, v2 = va_ ^ 128, v3 = va_ ^ 192;
      s16x4 vl[2][4], vh[2][4];
      auto issue_v = [&](auto SC) {
        constexpr int s = decltype(SC)::value, u = s & 1;
        vl[u][0] = rdtr<OB + s * 4096>(v0); vh[u][0] = rdtr<OB + s * 4096 + 2048>(v0);
        vl[u][1] = rdtr<OB + s * 4096>(v1); vh[u][1] = rdtr<OB + s * 4096 + 2048>(v1);
        vl[u][2] = rdtr<OB + s * 4096>(v2); vh[u][2] = rdtr<OB + s * 4096 + 2048>(v2);
        vl[u][3] = rdtr<OB + s * 4096>(v3); vh[u][3] = rdtr<OB + s * 4096 + 2048>(v3);
      };
      issue_v(IC<0>{});
      WAITL8(8, kr[0], kr[1], kr[2], kr[3], qr[0], qr[1], qr[2], qr[3]);
#pragma unroll
      for (int ks = 0; ks < 4; ++ks) sr = MFMA16(kr[ks], qr[ks], sr);
      SBAR();
      SBAR();
      issue_v(IC<1>{});
      WAITL8(8, vl[0][0], vh[0][0], vl[0][1], vh[0][1], vl[0][2], vh[0][2], vl[0][3], vh[0][3]);
      acc[0] = MFMA32(cat8(vl[0][0], vh[0][0]), pf[0], acc[0]);
      float tm1 = fmaxf(fmaxf(s1[0], s1[1]), s1[2]);
#pragma unroll
      for (int r = 3; r < 9; r += 2) tm1 = fmaxf(fmaxf(tm1, s1[r]), s1[r + 1]);
      SBAR();
      acc[1] = MFMA32(cat8(vl[0][1], vh[0][1]), pf[0], acc[1]);
#pragma unroll
      for (int r = 9; r < 15; r += 2) tm1 = fmaxf(fmaxf(tm1, s1[r]), s1[r + 1]);
      tm1 = fmaxf(tm1, s1[15]);
      tm1 = xmax32(tm1);
      const bool keepb = __all(tm1 - m1 <= THRS);
      float mnb = keepb ? m1 : fmaxf(m1, tm1);
      float alphab = keepb ? 1.f : __builtin_amdgcn_exp2f((m1 - mnb) * CS);
      m1 = mnb;
      mc = -mnb * CS;
      ps = 0.f;
      SBAR();
      acc[2] = MFMA32(cat8(vl[0][2], vh[0][2]), pf[0], acc[2]);
#pragma unroll
      for (int j = 0; j < 4; ++j) { e[j] = __builtin_amdgcn_exp2f(fmaf(s1[j], CS, mc)); ps += e[j]; }
      SBAR();
      acc[3] = MFMA32(cat8(vl[0][3], vh[0][3]), pf[0], acc[3]);
#pragma unroll
      for (int j = 4; j < 8; ++j) { e[j] = __builtin_amdgcn_exp2f(fmaf(s1[j], CS, mc)); ps += e[j]; }
      { u32x4 a = {cvtpk(e[0], e[1]), cvtpk(e[2], e[3]), cvtpk(e[4], e[5]), cvtpk(e[6], e[7])}; pf[2] = __builtin_bit_cast(bf16x8, a); }
      SBAR();
      issue_v(IC<2>{});
      WAITL8(8, vl[1][0], vh[1][0], vl[1][1], vh[1][1], vl[1][2], vh[1][2], vl[1][3], vh[1][3]);
      acc[0] = MFMA32(cat8(vl[1][0], vh[1][0]), pf[1], acc[0]);
#pragma unroll
      for (int j = 0; j < 4; ++j) { e[j] = __builtin_amdgcn_exp2f(fmaf(s1[8 + j], CS, mc)); ps += e[j]; }
      SBAR();
      acc[1] = MFMA32(cat8(vl[1][1], vh[1][1]), pf[1], acc[1]);
#pragma unroll
      for (int j = 4; j < 8; ++j) { e[j] = __builtin_amdgcn_exp2f(fmaf(s1[8 + j], CS, mc)); ps += e[j]; }
      { u32x4 a = {cvtpk(e[0], e[1]), cvtpk(e[2], e[3]), cvtpk(e[4], e[5]), cvtpk(e[6], e[7])}; pf[3] = __builtin_bit_cast(bf16x8, a); }
      l1 = l1 * alphab + ps;
      OPAQUE(l1);
      SBAR();
      acc[2] = MFMA32(cat8(vl[1][2], vh[1][2]), pf[1], acc[2]);
      float t2 = fmaxf(fmaxf(sr[0], sr[1]), fmaxf(sr[2], sr[3]));
      t2 = xmax16(t2);
      t2 = xmax32(t2);
      const bool keep2 = __all(t2 - m2 <= THRS);
      float mn2 = keep2 ? m2 : fmaxf(m2, t2);
      float alpha2 = keep2 ? 1.f : __builtin_amdgcn_exp2f((m2 - mn2) * CS);
      m2 = mn2;
      SBAR();
      acc[3] = MFMA32(cat8(vl[1][3], vh[1][3]), pf[1], acc[3]);
      const float mc2 = -mn2 * CS;
      float e0 = __builtin_amdgcn_exp2f(fmaf(sr[0], CS, mc2));
      float e1 = __builtin_amdgcn_exp2f(fmaf(sr[1], CS, mc2));
      float e2 = __builtin_amdgcn_exp2f(fmaf(sr[2], CS, mc2));
      float e3 = __builtin_amdgcn_exp2f(fmaf(sr[3], CS, mc2));
      l2 = l2 * alpha2 + (e0 + e1) + (e2 + e3);
      OPAQUE(l2);
      u32x2 p2u = {cvtpk(e0, e1), cvtpk(e2, e3)};
      s16x4 p2 = __builtin_bit_cast(s16x4, p2u);
      SBAR();
      if (!keepb) {
#pragma unroll
        for (int dt = 0; dt < 4; ++dt)
#pragma unroll
          for (int r = 0; r < 16; ++r) acc[dt][r] *= alphab;
      }
      if (!keep2) {
#pragma unroll
        for (int dd = 0; dd < 8; ++dd)
#pragma unroll
          for (int r = 0; r < 4; ++r) acc2[dd][r] *= alpha2;
      }
      issue_v(IC<3>{});
      WAITL8(8, vl[0][0], vh[0][0], vl[0][1], vh[0][1], vl[0][2], vh[0][2], vl[0][3], vh[0][3]);
#pragma unroll
      for (int dt = 0; dt < 4; ++dt) acc[dt] = MFMA32(cat8(vl[0][dt], vh[0][dt]), pf[2], acc[dt]);
      SBAR();
      s16x4 vr[8];
#pragma unroll
      for (int dd = 0; dd < 8; ++dd) vr[dd] = rdtr<OB>(va2_ ^ (dd << 5));
      WAITL8(8, vl[1][0], vh[1][0], vl[1][1], vh[1][1], vl[1][2], vh[1][2], vl[1][3], vh[1][3]);
#pragma unroll
      for (int dt = 0; dt < 4; ++dt) acc[dt] = MFMA32(cat8(vl[1][dt], vh[1][dt]), pf[3], acc[dt]);
      WAITL8(0, vr[0], vr[1], vr[2], vr[3], vr[4], vr[5], vr[6], vr[7]);
#pragma unroll
      for (int dd = 0; dd < 8; ++dd) acc2[dd] = MFMA16K16(vr[dd], p2, acc2[dd]);
    };
    for (int t = 0; t < 8; t += 2) {
      body(IC<0>{}, t);
      body(IC<1>{}, t + 1);
    }
  }

  int tz = threadIdx.x;
  OPAQUE(tz);
  asm volatile("s_waitcnt lgkmcnt(0)" ::: "memory");
  RAW_BAR();
  {
    const int l15 = tz & 15, g = (tz >> 4) & 3;
    float lt2 = xsum32(xsum16(l2));
    float* pw = (float*)(smem + ((w & 1) ? A_OFF_B + A_BUFB : A_OFF_A + A_BUFA) + (w >> 1) * 8192);
#pragma unroll
    for (int dd = 0; dd < 8; ++dd) *(f32x4*)(pw + l15 * 128 + 16 * dd + 4 * g) = acc2[dd];
    float* st = (float*)(smem + A_OFF_Q2);
    if (g == 0) { st[w * 32 + l15] = m2; st[w * 32 + 16 + l15] = lt2; }
  }
  asm volatile("s_waitcnt lgkmcnt(0)" ::: "memory");
  RAW_BAR();
  {
    const int q = tz >> 4, d0 = (tz & 15) * 8;
    const float* st = (const float*)(smem + A_OFF_Q2);
    float mm = fmaxf(fmaxf(st[q], st[32 + q]), fmaxf(st[64 + q], st[96 + q]));
    float o[8] = {0, 0, 0, 0, 0, 0, 0, 0};
    float ls = 0.f;
#pragma unroll
    for (int ww = 0; ww < 4; ++ww) {
      float sc = __builtin_amdgcn_exp2f((st[ww * 32 + q] - mm) * CS);
      ls += st[ww * 32 + 16 + q] * sc;
      const float* pw = (const float*)(smem + ((ww & 1) ? A_OFF_B + A_BUFB : A_OFF_A + A_BUFA) + (ww >> 1) * 8192) + q * 128 + d0;
      f32x4 a = *(const f32x4*)pw, c = *(const f32x4*)(pw + 4);
#pragma unroll
      for (int j = 0; j < 4; ++j) { o[j] += a[j] * sc; o[4 + j] += c[j] * sc; }
    }
    float inv = 1.f / ls;
    u32x4 pk = {cvtpk(o[0] * inv, o[1] * inv), cvtpk(o[2] * inv, o[3] * inv),
                cvtpk(o[4] * inv, o[5] * inv), cvtpk(o[6] * inv, o[7] * inv)};
    *(u32x4*)(og + ((size_t)pidx * SQ + 128 + q) * DH + d0) = pk;
  }
  asm volatile("s_waitcnt lgkmcnt(0)" ::: "memory");
  RAW_BAR();
  {
    const int l31 = tz & 31, hh = (tz >> 5) & 1;
    float lt = xsum32(l1);
    float inv = 1.f / lt;
    char* ob = smem + (w < 2 ? A_OFF_A + A_BUFA : A_OFF_B + A_BUFB) + ((w & 1) * 32 + l31) * 256 + 8 * hh;
#pragma unroll
    for (int dt = 0; dt < 4; ++dt)
#pragma unroll
      for (int i = 0; i < 4; ++i) {
        u32x2 pk = {cvtpk(acc[dt][4 * i] * inv, acc[dt][4 * i + 1] * inv),
                    cvtpk(acc[dt][4 * i + 2] * inv, acc[dt][4 * i + 3] * inv)};
        *(u32x2*)(ob + (((4 * dt + i) ^ (l31 & 15)) << 4)) = pk;
      }
    if (tz == 0 && nidx >= 0) *slot = 4096u;
  }
  asm volatile("s_waitcnt lgkmcnt(0)" ::: "memory");
  RAW_BAR();
  {
    const int c = tz & 15, r0 = tz >> 4;
    short* orow = og + ((size_t)pidx * SQ + r0) * DH + c * 8;
#pragma unroll
    for (int j = 0; j < 8; ++j) {
      const int r = r0 + 16 * j;
      const char* src = smem + (j < 4 ? A_OFF_A + A_BUFA + r * 256 : A_OFF_B + A_BUFB + (r - 64) * 256) + ((c ^ r0) << 4);
      bf16x8 v = *(const bf16x8*)src;
      *(bf16x8*)(orow + (size_t)(16 * j) * DH) = v;
    }
  }
  if (nidx < 0) break;
  pidx = nidx;
  }
}

constexpr int B_NST = 4;
constexpr int B_BUFA = 20480;
constexpr int B_BUFB = 8192;
constexpr int B_STRIDE = B_BUFA + B_BUFB;
constexpr int B_LDS = B_NST * B_STRIDE;

__global__ __launch_bounds__(512, 2) void oproj_kernel(const short* __restrict__ og,
                                                        const short* __restrict__ wo,
                                                        short* __restrict__ out) {
  extern __shared__ __attribute__((aligned(16))) char smem[];
  const int tid = threadIdx.x, lane = tid & 63;
  const int wv = __builtin_amdgcn_readfirstlane(tid >> 6);
  const int w = wv & 3;
  const int xg = blockIdx.x & 7, idx = blockIdx.x >> 3;
  const int b = xg * 4 + (idx >> 3), nt0 = idx & 7;
  const int l31 = lane & 31, hh = lane >> 5, l15 = lane & 15, g = lane >> 4;
  const int qp = l15 >> 2, p4 = lane & 3, blk = (lane >> 4) & 1;
  constexpr int NT = HD / 64;
  if (wv >= 4) {
    const char* ob = (const char*)(og + (size_t)b * SQ * HD);
    const char* wob = (const char*)(wo + nt0 * 64);
    unsigned osrc[5], wsrc[2];
#pragma unroll
    for (int ii = 0; ii < 5; ++ii) {
      int r = 8 * (w * 5 + ii) + (lane >> 3), cp = lane & 7;
      int rs = r < SQ ? r : SQ - 1;
      osrc[ii] = rs * (DH * 2) + ((cp ^ ((r >> 1) & 7)) << 4);
    }
#pragma unroll
    for (int ii = 0; ii < 2; ++ii) {
      int r = 8 * (w * 2 + ii) + (lane >> 3), cp = lane & 7;
      wsrc[ii] = r * (DM * 2) + ((cp ^ (((r >> 1) & 1) << 2)) << 4);
    }
    auto stage = [&](int t) {
      const int buf = t & 3;
      char* la = smem + buf * B_STRIDE + w * 5120;
      char* lb = smem + buf * B_STRIDE + B_BUFA + w * 2048;
      const char* ot = ob + (t >> 1) * (SQ * DH * 2) + (t & 1) * 128;
      const char* wt = wob + (size_t)t * (64 * DM * 2);
#pragma unroll
      for (int ii = 0; ii < 5; ++ii) glds16(ot + osrc[ii], la + ii * 1024);
#pragma unroll
      for (int ii = 0; ii < 2; ++ii) glds16(wt + wsrc[ii], lb + ii * 1024);
    };
    stage(0); stage(1); stage(2);
    asm volatile("s_waitcnt vmcnt(14)" ::: "memory");
    for (int t = 0; t < NT; ++t) {
      RAW_BAR();
      if (t + 3 < NT) { stage(t + 3); asm volatile("s_waitcnt vmcnt(14)" ::: "memory"); }
      else if (t + 2 < NT) asm volatile("s_waitcnt vmcnt(7)" ::: "memory");
      else asm volatile("s_waitcnt vmcnt(0)" ::: "memory");
    }
    return;
  }
  f32x16 acc[2] = {f32x16{}, f32x16{}};
  f32x4 acc2 = f32x4{};
  const int sb = (int)(uintptr_t)smem;
  const int xa = sb + (32 * w + l31) * 128 + ((hh ^ ((l31 >> 1) & 7)) << 4);
  const int wa = sb + B_BUFA + (8 * hh + qp) * 128 + (((2 * blk + (p4 >> 1)) ^ ((qp >> 1) << 2)) << 4) + 8 * (p4 & 1);
  const int xa2 = sb + (128 + l15) * 128 + ((g ^ ((l15 >> 1) & 7)) << 4);
  const int wa2 = sb + B_BUFA + (8 * g + qp) * 128 + (((2 * w + (p4 >> 1)) ^ ((qp >> 1) << 2)) << 4) + 8 * (p4 & 1);
  auto body = [&](auto BC) {
    constexpr int buf = decltype(BC)::value;
    constexpr int OB = (buf & 1) * B_STRIDE;
    const int hi2 = (buf >> 1) * 2 * B_STRIDE;
    RAW_BAR();
    const int xa_ = xa + hi2, w0 = wa + hi2, w1 = (wa ^ 64) + hi2, xa2_ = xa2 + hi2, wa2_ = wa2 + hi2;
    bf16x8 xf[4], xr[2];
    s16x4 wl[4][2], wh[4][2], rl[2], rh[2];
    sfor<0, 4>([&](auto SC) {
      constexpr int s = decltype(SC)::value;
      xf[s] = rd128<OB>(xa_ ^ (s << 5));
      wl[s][0] = rdtr<OB + s * 2048>(w0); wh[s][0] = rdtr<OB + s * 2048 + 512>(w0);
      wl[s][1] = rdtr<OB + s * 2048>(w1); wh[s][1] = rdtr<OB + s * 2048 + 512>(w1);
      if constexpr (s >= 1) {
        asm volatile("s_waitcnt lgkmcnt(5)" : "+v"(xf[s - 1]), "+v"(wl[s - 1][0]), "+v"(wh[s - 1][0]), "+v"(wl[s - 1][1]), "+v"(wh[s - 1][1]) :: "memory");
        acc[0] = MFMA32(cat8(wl[s - 1][0], wh[s - 1][0]), xf[s - 1], acc[0]);
        acc[1] = MFMA32(cat8(wl[s - 1][1], wh[s - 1][1]), xf[s - 1], acc[1]);
      }
    });
    xr[0] = rd128<OB>(xa2_); rl[0] = rdtr<OB>(wa2_); rh[0] = rdtr<OB + 512>(wa2_);
    xr[1] = rd128<OB>(xa2_ ^ 64); rl[1] = rdtr<OB + 4096>(wa2_); rh[1] = rdtr<OB + 4096 + 512>(wa2_);
    asm volatile("s_waitcnt lgkmcnt(6)" : "+v"(xf[3]), "+v"(wl[3][0]), "+v"(wh[3][0]), "+v"(wl[3][1]), "+v"(wh[3][1]) :: "memory");
    acc[0] = MFMA32(cat8(wl[3][0], wh[3][0]), xf[3], acc[0]);
    acc[1] = MFMA32(cat8(wl[3][1], wh[3][1]), xf[3], acc[1]);
    asm volatile("s_waitcnt lgkmcnt(0)" : "+v"(xr[0]), "+v"(rl[0]), "+v"(rh[0]), "+v"(xr[1]), "+v"(rl[1]), "+v"(rh[1]) :: "memory");
    acc2 = MFMA16(cat8(rl[0], rh[0]), xr[0], acc2);
    acc2 = MFMA16(cat8(rl[1], rh[1]), xr[1], acc2);
  };
  for (int t = 0; t < NT; t += 4) {
    body(IC<0>{});
    body(IC<1>{});
    body(IC<2>{});
    body(IC<3>{});
  }
  {
    short* orow = out + (size_t)(b * SQ + 32 * w + l31) * DM + nt0 * 64 + 4 * hh;
#pragma unroll
    for (int nt = 0; nt < 2; ++nt)
#pragma unroll
      for (int i = 0; i < 4; ++i) {
        u32x2 pk = {cvtpk(acc[nt][4 * i], acc[nt][4 * i + 1]), cvtpk(acc[nt][4 * i + 2], acc[nt][4 * i + 3])};
        *(u32x2*)(orow + 32 * nt + 8 * i) = pk;
      }
    u32x2 pk = {cvtpk(acc2[0], acc2[1]), cvtpk(acc2[2], acc2[3])};
    *(u32x2*)(out + (size_t)(b * SQ + 128 + l15) * DM + nt0 * 64 + 16 * w + 4 * g) = pk;
  }
}

extern "C" void kernel_launch(void* const* d_in, const int* in_sizes, int n_in, void* d_out,
                              int out_size, void* d_ws, size_t ws_size, hipStream_t stream) {
  const short* x = (const short*)d_in[0];
  const short* kc = (const short*)d_in[1];
  const short* vc = (const short*)d_in[2];
  const short* wq = (const short*)d_in[3];
  const short* wo = (const short*)d_in[4];
  short* og = (short*)d_ws;
  short* out = (short*)d_out;
  unsigned* counter = (unsigned*)(og + (size_t)MROWS * HD);
  (void)hipFuncSetAttribute((const void*)attn_kernel, hipFuncAttributeMaxDynamicSharedMemorySize, A_LDS);
  (void)hipFuncSetAttribute((const void*)oproj_kernel, hipFuncAttributeMaxDynamicSharedMemorySize, B_LDS);
  attn_kernel<<<512, 256, A_LDS, stream>>>(x, wq, kc, vc, og, counter);
  oproj_kernel<<<256, 512, B_LDS, stream>>>(og, wo, out);
}
```

```cpp
#include <hip/hip_runtime.h>
#include <hip/hip_bf16.h>

typedef __attribute__((ext_vector_type(8))) short bf16x8;
typedef __attribute__((ext_vector_type(4))) short s16x4;
typedef __attribute__((ext_vector_type(16))) float f32x16;
typedef __attribute__((ext_vector_type(4))) float f32x4;
typedef __attribute__((ext_vector_type(4))) unsigned u32x4;
typedef __attribute__((ext_vector_type(2))) unsigned u32x2;

constexpr int NB = 32, SQ = 144, DM = 512, NH = 32, SK = 512, DH = 128;
constexpr int HD = NH * DH;
constexpr int MROWS = NB * SQ;

#define MFMA32(a, b, c) __builtin_amdgcn_mfma_f32_32x32x16_bf16((a), (b), (c), 0, 0, 0)
#define MFMA16(a, b, c) __builtin_amdgcn_mfma_f32_16x16x32_bf16((a), (b), (c), 0, 0, 0)
#define MFMA16K16(a, b, c) __builtin_amdgcn_mfma_f32_16x16x16bf16_1k((a), (b), (c), 0, 0, 0)

__device__ __forceinline__ unsigned cvtpk(float lo, float hi) {
  unsigned r;
  asm volatile("v_cvt_pk_bf16_f32 %0, %1, %2" : "=v"(r) : "v"(lo), "v"(hi));
  return r;
}
__device__ __forceinline__ bf16x8 cat8(s16x4 lo, s16x4 hi) {
  return __builtin_shufflevector(lo, hi, 0, 1, 2, 3, 4, 5, 6, 7);
}
__device__ __forceinline__ void glds16(const void* g, char* l) {
  __builtin_amdgcn_global_load_lds((const unsigned*)g, (unsigned*)l, 16, 0, 0);
}
__device__ __forceinline__ void glds16nt(const void* g, char* l) {
  __builtin_amdgcn_global_load_lds((const unsigned*)g, (unsigned*)l, 16, 0, 2);
}
__device__ __forceinline__ float xmax32(float v) {
  auto rr = __builtin_amdgcn_permlane32_swap(__float_as_uint(v), __float_as_uint(v), false, false);
  return fmaxf(__uint_as_float(rr[0]), __uint_as_float(rr[1]));
}
__device__ __forceinline__ float xsum32(float v) {
  auto rr = __builtin_amdgcn_permlane32_swap(__float_as_uint(v), __float_as_uint(v), false, false);
  return __uint_as_float(rr[0]) + __uint_as_float(rr[1]);
}
__device__ __forceinline__ float xmax16(float v) {
  auto rr = __builtin_amdgcn_permlane16_swap(__float_as_uint(v), __float_as_uint(v), false, false);
  return fmaxf(__uint_as_float(rr[0]), __uint_as_float(rr[1]));
}
__device__ __forceinline__ float xsum16(float v) {
  auto rr = __builtin_amdgcn_permlane16_swap(__float_as_uint(v), __float_as_uint(v), false, false);
  return __uint_as_float(rr[0]) + __uint_as_float(rr[1]);
}
template <int OFF> __device__ __forceinline__ bf16x8 rd128(int a) {
  bf16x8 r;
  asm volatile("ds_read_b128 %0, %1 offset:%2" : "=v"(r) : "v"(a), "i"(OFF) : "memory");
  return r;
}
template <int OFF> __device__ __forceinline__ s16x4 rdtr(int a) {
  s16x4 r;
  asm volatile("ds_read_b64_tr_b16 %0, %1 offset:%2" : "=v"(r) : "v"(a), "i"(OFF) : "memory");
  return r;
}
#define WAITL2(N, a, b) asm volatile("s_waitcnt lgkmcnt(" #N ")" : "+v"(a), "+v"(b) :: "memory")
#define WAITL4(N, a, b, c, d) asm volatile("s_waitcnt lgkmcnt(" #N ")" : "+v"(a), "+v"(b), "+v"(c), "+v"(d) :: "memory")
#define WAITL8(N, a, b, c, d, e, f, g_, h_) asm volatile("s_waitcnt lgkmcnt(" #N ")" : "+v"(a), "+v"(b), "+v"(c), "+v"(d), "+v"(e), "+v"(f), "+v"(g_), "+v"(h_) :: "memory")
#define RAW_BAR() __builtin_amdgcn_s_barrier()
#define SBAR() __builtin_amdgcn_sched_barrier(0)
#define OPAQUE(v) asm volatile("" : "+v"(v))
template <int N> struct IC { static constexpr int value = N; };
template <int I, int N, class F> __device__ __forceinline__ void sfor(F&& f) {
  if constexpr (I < N) { f(IC<I>{}); sfor<I + 1, N>(f); }
}

constexpr int A_BUFA = 20480;
constexpr int A_BUFB = 16384;
constexpr int A_OFF_A = 0;
constexpr int A_OFF_B = 2 * A_BUFA;
constexpr int A_OFF_Q2 = A_OFF_B + 2 * A_BUFB;
constexpr int A_OFF_SLOT = A_OFF_Q2 + 4096;
constexpr int A_LDS = A_OFF_SLOT + 16;

__global__ __launch_bounds__(256, 2) void attn_kernel(
    const short* __restrict__ x, const short* __restrict__ wq, const short* __restrict__ kc,
    const short* __restrict__ vc, short* __restrict__ og, unsigned* __restrict__ counter) {
  extern __shared__ __attribute__((aligned(16))) char smem[];
  const int tid = threadIdx.x, lane = tid & 63;
  const int w = __builtin_amdgcn_readfirstlane(tid >> 6);
  const int l31 = lane & 31, hh = lane >> 5, l15 = lane & 15, g = lane >> 4;
  const int qp = l15 >> 2, p4 = lane & 3, blk = (lane >> 4) & 1;
  const int sb = (int)(uintptr_t)smem;
  volatile unsigned* slot = (volatile unsigned*)(smem + A_OFF_SLOT);

  const unsigned ksrc = (16 * w + g) * 256 + ((l15 ^ g) << 4);
  const unsigned vsrc = (16 * w + g) * 256 + ((l15 ^ (g << 2)) << 4);
  auto stage_kv = [&](int pi, int t, int buf, unsigned ko, unsigned vo) {
    const char* kb = (const char*)kc + (size_t)pi * (SK * DH * 2) + t * 16384;
    const char* vb = (const char*)vc + (size_t)pi * (SK * DH * 2) + t * 16384;
#pragma unroll
    for (int i = 0; i < 4; ++i)
      glds16nt(kb + ((ko ^ (i << 6)) + i * 1024), smem + A_OFF_A + buf * A_BUFA + w * 4096 + i * 1024);
#pragma unroll
    for (int i = 0; i < 4; ++i)
      glds16nt(vb + (vo + i * 1024), smem + A_OFF_B + buf * A_BUFB + w * 4096 + i * 1024);
  };
  auto stage_q = [&](int pi, int t, int buf) {
    int ln = lane;
    OPAQUE(ln);
    const char* xt = (const char*)(x + (size_t)(pi >> 5) * SQ * DM) + t * 128;
    const char* wt = (const char*)(wq + (pi & 31) * DH) + (size_t)t * (64 * HD * 2);
#pragma unroll
    for (int ii = 0; ii < 5; ++ii) {
      int r = 8 * (w * 5 + ii) + (ln >> 3), cp = ln & 7;
      int rs = r < SQ ? r : SQ - 1;
      unsigned so = rs * 1024 + ((cp ^ ((r >> 1) & 7)) << 4);
      glds16(xt + so, smem + A_OFF_A + buf * A_BUFA + w * 5120 + ii * 1024);
    }
    const unsigned wo0 = (16 * w + (ln >> 4)) * (HD * 2) + (((ln & 15) ^ ((ln >> 4) << 2)) << 4);
#pragma unroll
    for (int ii = 0; ii < 4; ++ii)
      glds16(wt + (wo0 + ii * 4 * HD * 2), smem + A_OFF_B + buf * A_BUFB + w * 4096 + ii * 1024);
  };
  int pidx = (blockIdx.x & 7) * 128 + (blockIdx.x >> 3);
#define STAG 0
  if (STAG > 0 && blockIdx.x >= 256) {
    unsigned long long t0 = __builtin_amdgcn_s_memtime();
    while (__builtin_amdgcn_s_memtime() - t0 < STAG) __builtin_amdgcn_s_sleep(8);
  }
  stage_q(pidx, 0, 0);
  if (tid == 0) *slot = blockIdx.x + 512u;
  constexpr float CS = 0.088388347648318440f * 1.4426950408889634f;
  constexpr float THRS = 8.f / CS;

  for (;;) {
  const int b = pidx >> 5, h = pidx & 31;
  f32x16 acc[4];
#pragma unroll
  for (int i = 0; i < 4; ++i) acc[i] = f32x16{};
  f32x4 acc2[8];
#pragma unroll
  for (int i = 0; i < 8; ++i) acc2[i] = f32x4{};
  {
    int lq = lane;
    OPAQUE(lq);
    const int l31 = lq & 31, hh = lq >> 5, l15 = lq & 15, g = lq >> 4;
    const int qp = l15 >> 2, p4 = lq & 3, blk = (lq >> 4) & 1;
    const int xa = sb + A_OFF_A + (32 * w + l31) * 128 + ((hh ^ ((l31 >> 1) & 7)) << 4);
    const int wa = sb + A_OFF_B + (8 * hh + qp) * 256 + (((2 * blk + (p4 >> 1)) ^ (qp << 2)) << 4) + 8 * (p4 & 1);
    const int xa2 = sb + A_OFF_A + (128 + l15) * 128 + ((g ^ ((l15 >> 1) & 7)) << 4);
    const int wa2 = sb + A_OFF_B + (8 * g + qp) * 256 + (((4 * w + (p4 >> 1)) ^ (qp << 2)) << 4) + 8 * (p4 & 1);
    auto body = [&](auto BC, int t) {
      constexpr int buf = decltype(BC)::value;
      constexpr int OA = buf * A_BUFA, OB = buf * A_BUFB;
      asm volatile("s_waitcnt vmcnt(0) lgkmcnt(0)" ::: "memory");
      RAW_BAR();
      if (t < 7) stage_q(pidx, t + 1, buf ^ 1);
      else stage_kv(pidx, 0, buf ^ 1, ksrc, vsrc);
      SBAR();
      const int w0 = wa, w1 = wa ^ 64, w2 = wa ^ 128, w3 = wa ^ 192;
      bf16x8 xf[2];
      s16x4 wl[2][4], wh[2][4];
      bf16x8 xr[2];
      s16x4 rl[2][2], rh[2][2];
      auto issue = [&](auto SC) {
        constexpr int s = decltype(SC)::value, u = s & 1;
        xf[u] = rd128<OA>(xa ^ (s << 5));
        wl[u][0] = rdtr<OB + s * 4096>(w0); wh[u][0] = rdtr<OB + s * 4096 + 1024>(w0);
        wl[u][1] = rdtr<OB + s * 4096>(w1); wh[u][1] = rdtr<OB + s * 4096 + 1024>(w1);
        wl[u][2] = rdtr<OB + s * 4096>(w2); wh[u][2] = rdtr<OB + s * 4096 + 1024>(w2);
        wl[u][3] = rdtr<OB + s * 4096>(w3); wh[u][3] = rdtr<OB + s * 4096 + 1024>(w3);
      };
      issue(IC<0>{});
      sfor<0, 4>([&](auto SC) {
        constexpr int s = decltype(SC)::value, u = s & 1;
        if constexpr (s < 3) {
          issue(IC<s + 1>{});
          asm volatile("s_waitcnt lgkmcnt(9)" : "+v"(xf[u]), "+v"(wl[u][0]), "+v"(wh[u][0]), "+v"(wl[u][1]), "+v"(wh[u][1]),
                       "+v"(wl[u][2]), "+v"(wh[u][2]), "+v"(wl[u][3]), "+v"(wh[u][3]) :: "memory");
        } else {
          xr[0] = rd128<OA>(xa2); xr[1] = rd128<OA>(xa2 ^ 64);
          rl[0][0] = rdtr<OB>(wa2); rh[0][0] = rdtr<OB + 1024>(wa2);
          rl[0][1] = rdtr<OB>(wa2 ^ 32); rh[0][1] = rdtr<OB + 1024>(wa2 ^ 32);
          rl[1][0] = rdtr<OB + 8192>(wa2); rh[1][0] = rdtr<OB + 8192 + 1024>(wa2);
          rl[1][1] = rdtr<OB + 8192>(wa2 ^ 32); rh[1][1] = rdtr<OB + 8192 + 1024>(wa2 ^ 32);
          asm volatile("s_waitcnt lgkmcnt(10)" : "+v"(xf[u]), "+v"(wl[u][0]), "+v"(wh[u][0]), "+v"(wl[u][1]), "+v"(wh[u][1]),
                       "+v"(wl[u][2]), "+v"(wh[u][2]), "+v"(wl[u][3]), "+v"(wh[u][3]) :: "memory");
        }
#pragma unroll
        for (int dt = 0; dt < 4; ++dt) acc[dt] = MFMA32(cat8(wl[u][dt], wh[u][dt]), xf[u], acc[dt]);
        SBAR();
      });
      asm volatile("s_waitcnt lgkmcnt(0)" : "+v"(xr[0]), "+v"(xr[1]), "+v"(rl[0][0]), "+v"(rh[0][0]), "+v"(rl[0][1]), "+v"(rh[0][1]),
                   "+v"(rl[1][0]), "+v"(rh[1][0]), "+v"(rl[1][1]), "+v"(rh[1][1]) :: "memory");
#pragma unroll
      for (int s2 = 0; s2 < 2; ++s2)
#pragma unroll
        for (int e = 0; e < 2; ++e) acc2[e] = MFMA16(cat8(rl[s2][e], rh[s2][e]), xr[s2], acc2[e]);
    };
    for (int t = 0; t < 8; t += 2) {
      body(IC<0>{}, t);
      body(IC<1>{}, t + 1);
    }
  }
  bf16x8 qf[8];
#pragma unroll
  for (int dt = 0; dt < 4; ++dt) {
#pragma unroll
    for (int s = 0; s < 2; ++s) {
      unsigned a0 = cvtpk(acc[dt][8 * s + 0], acc[dt][8 * s + 1]);
      unsigned a1 = cvtpk(acc[dt][8 * s + 2], acc[dt][8 * s + 3]);
      unsigned b0 = cvtpk(acc[dt][8 * s + 4], acc[dt][8 * s + 5]);
      unsigned b1 = cvtpk(acc[dt][8 * s + 6], acc[dt][8 * s + 7]);
      auto r0 = __builtin_amdgcn_permlane32_swap(a0, b0, false, false);
      auto r1 = __builtin_amdgcn_permlane32_swap(a1, b1, false, false);
      u32x4 wv = {r0[0], r1[0], r0[1], r1[1]};
      qf[2 * dt + s] = __builtin_bit_cast(bf16x8, wv);
    }
  }
  {
#pragma unroll
    for (int e = 0; e < 2; ++e) {
      int dd = 2 * w + e;
      u32x2 pk = {cvtpk(acc2[e][0], acc2[e][1]), cvtpk(acc2[e][2], acc2[e][3])};
      int ch = (2 * dd + (g >> 1)) ^ l15;
      asm volatile("ds_write_b64 %0, %1" :: "v"(sb + A_OFF_Q2 + l15 * 256 + (ch << 4) + 8 * (g & 1)), "v"(pk) : "memory");
    }
  }
#pragma unroll
  for (int i = 0; i < 4; ++i) acc[i] = f32x16{};
#pragma unroll
  for (int i = 0; i < 8; ++i) acc2[i] = f32x4{};
  int nidx = 0;
  float m1 = -1e30f, l1 = 0.f, m2 = -1e30f, l2 = 0.f;
  {
    int ka = sb + A_OFF_A + l31 * 256 + ((hh ^ l15) << 4);
    int ka2 = sb + A_OFF_A + (16 * w + l15) * 256 + ((g ^ l15) << 4);
    int q2a = sb + A_OFF_Q2 + l15 * 256 + ((g ^ l15) << 4);
    int va = sb + A_OFF_B + (4 * hh + qp) * 256 + (((2 * blk + (p4 >> 1)) ^ (qp << 2)) << 4) + 8 * (p4 & 1);
    int va2 = sb + A_OFF_B + (16 * w + 4 * g + qp) * 256 + (((p4 >> 1) ^ (qp << 2)) << 4) + 8 * (p4 & 1);
    auto body = [&](auto BC, int t) {
      constexpr int buf = decltype(BC)::value;
      constexpr int OA = buf * A_BUFA, OB = buf * A_BUFB;
      asm volatile("s_waitcnt vmcnt(0) lgkmcnt(0)" ::: "memory");
      RAW_BAR();
      int ka_ = ka, ka2_ = ka2, q2a_ = q2a, va_ = va, va2_ = va2;
      unsigned ko = ksrc, vo = vsrc;
      OPAQUE(ka_); OPAQUE(ka2_); OPAQUE(q2a_); OPAQUE(va_); OPAQUE(va2_); OPAQUE(ko); OPAQUE(vo);
      if (t < 7) {
        stage_kv(pidx, t + 1, buf ^ 1, ko, vo);
      } else {
        const unsigned it = __builtin_amdgcn_readfirstlane(*slot);
        nidx = it < 1024u ? (int)((it & 7) * 128 + (it >> 3)) : -1;
        if (nidx >= 0) stage_q(nidx, 0, 0);
      }
      SBAR();
      f32x16 s0 = f32x16{}, s1 = f32x16{};
      f32x4 sr = f32x4{};
      bf16x8 kA[4], kB[4];
#pragma unroll
      for (int i = 0; i < 4; ++i) kA[i] = rd128<OA>(ka_ ^ (i << 5));
#pragma unroll
      for (int i = 0; i < 4; ++i) kB[i] = rd128<OA>(ka_ ^ ((4 + i) << 5));
      WAITL4(4, kA[0], kA[1], kA[2], kA[3]);
      s0 = MFMA32(kA[0], qf[0], s0);
      s0 = MFMA32(kA[1], qf[1], s0);
      SBAR();
      s0 = MFMA32(kA[2], qf[2], s0);
      s0 = MFMA32(kA[3], qf[3], s0);
      SBAR();
#pragma unroll
      for (int i = 0; i < 4; ++i) kA[i] = rd128<OA + 8192>(ka_ ^ (i << 5));
      WAITL4(4, kB[0], kB[1], kB[2], kB[3]);
      s0 = MFMA32(kB[0], qf[4], s0);
      s0 = MFMA32(kB[1], qf[5], s0);
      SBAR();
      s0 = MFMA32(kB[2], qf[6], s0);
      s0 = MFMA32(kB[3], qf[7], s0);
      SBAR();
#pragma unroll
      for (int i = 0; i < 4; ++i) kB[i] = rd128<OA + 8192>(ka_ ^ ((4 + i) << 5));
      WAITL4(4, kA[0], kA[1], kA[2], kA[3]);
      s1 = MFMA32(kA[0], qf[0], s1);
      SBAR();
      s1 = MFMA32(kA[1], qf[1], s1);
      float tmax = fmaxf(fmaxf(s0[0], s0[1]), s0[2]);
#pragma unroll
      for (int r = 3; r < 9; r += 2) tmax = fmaxf(fmaxf(tmax, s0[r]), s0[r + 1]);
      SBAR();
      s1 = MFMA32(kA[2], qf[2], s1);
#pragma unroll
      for (int r = 9; r < 15; r += 2) tmax = fmaxf(fmaxf(tmax, s0[r]), s0[r + 1]);
      tmax = fmaxf(tmax, s0[15]);
      tmax = xmax32(tmax);
      SBAR();
      s1 = MFMA32(kA[3], qf[3], s1);
      SBAR();
      if (!__all(tmax - m1 <= THRS)) {
        float mn0 = fmaxf(m1, tmax);
        float alpha = __builtin_amdgcn_exp2f((m1 - mn0) * CS);
        m1 = mn0;
        l1 *= alpha;
#pragma unroll
        for (int dt = 0; dt < 4; ++dt)
#pragma unroll
          for (int r = 0; r < 16; ++r) acc[dt][r] *= alpha;
      }
      const float mn = m1;
      bf16x8 kr[4], qr[4];
#pragma unroll
      for (int ks = 0; ks < 4; ++ks) { kr[ks] = rd128<OA>(ka2_ ^ (ks << 6)); qr[ks] = rd128<0>(q2a_ ^ (ks << 6)); }
      WAITL4(8, kB[0], kB[1], kB[2], kB[3]);
      bf16x8 pf[4];
      float mc = -mn * CS;
      float ps = 0.f;
      float e[8];
      s1 = MFMA32(kB[0], qf[4], s1);
#pragma unroll
      for (int j = 0; j < 4; ++j) { e[j] = __builtin_amdgcn_exp2f(fmaf(s0[j], CS, mc)); ps += e[j]; }
      SBAR();
      s1 = MFMA32(kB[1], qf[5], s1);
#pragma unroll
      for (int j = 4; j < 8; ++j) { e[j] = __builtin_amdgcn_exp2f(fmaf(s0[j], CS, mc)); ps += e[j]; }
      { u32x4 a = {cvtpk(e[0], e[1]), cvtpk(e[2], e[3]), cvtpk(e[4], e[5]), cvtpk(e[6], e[7])}; pf[0] = __builtin_bit_cast(bf16x8, a); }
      SBAR();
      s1 = MFMA32(kB[2], qf[6], s1);
#pragma unroll
      for (int j = 0; j < 4; ++j) { e[j] = __builtin_amdgcn_exp2f(fmaf(s0[8 + j], CS, mc)); ps += e[j]; }
      SBAR();
      s1 = MFMA32(kB[3], qf[7], s1);
#pragma unroll
      for (int j = 4; j < 8; ++j) { e[j] = __builtin_amdgcn_exp2f(fmaf(s0[8 + j], CS, mc)); ps += e[j]; }
      { u32x4 a = {cvtpk(e[0], e[1]), cvtpk(e[2], e[3]), cvtpk(e[4], e[5]), cvtpk(e[6], e[7])}; pf[1] = __builtin_bit_cast(bf16x8, a); }
      l1 += ps;
      OPAQUE(l1);
      SBAR();
      SBAR();
      const int v0 = va_, v1 = va_ ^ 64, v2 = va_ ^ 128, v3 = va_ ^ 192;
      s16x4 vl[2][4], vh[2][4];
      auto issue_v = [&](auto SC) {
        constexpr int s = decltype(SC)::value, u = s & 1;
        vl[u][0] = rdtr<OB + s * 4096>(v0); vh[u][0] = rdtr<OB + s * 4096 + 2048>(v0);
        vl[u][1] = rdtr<OB + s * 4096>(v1); vh[u][1] = rdtr<OB + s * 4096 + 2048>(v1);
        vl[u][2] = rdtr<OB + s * 4096>(v2); vh[u][2] = rdtr<OB + s * 4096 + 2048>(v2);
        vl[u][3] = rdtr<OB + s * 4096>(v3); vh[u][3] = rdtr<OB + s * 4096 + 2048>(v3);
      };
      issue_v(IC<0>{});
      WAITL8(8, kr[0], kr[1], kr[2], kr[3], qr[0], qr[1], qr[2], qr[3]);
#pragma unroll
      for (int ks = 0; ks < 4; ++ks) sr = MFMA16(kr[ks], qr[ks], sr);
      SBAR();
      SBAR();
      issue_v(IC<1>{});
      WAITL8(8, vl[0][0], vh[0][0], vl[0][1], vh[0][1], vl[0][2], vh[0][2], vl[0][3], vh[0][3]);
      acc[0] = MFMA32(cat8(vl[0][0], vh[0][0]), pf[0], acc[0]);
      float tm1 = fmaxf(fmaxf(s1[0], s1[1]), s1[2]);
#pragma unroll
      for (int r = 3; r < 9; r += 2) tm1 = fmaxf(fmaxf(tm1, s1[r]), s1[r + 1]);
      SBAR();
      acc[1] = MFMA32(cat8(vl[0][1], vh[0][1]), pf[0], acc[1]);
#pragma unroll
      for (int r = 9; r < 15; r += 2) tm1 = fmaxf(fmaxf(tm1, s1[r]), s1[r + 1]);
      tm1 = fmaxf(tm1, s1[15]);
      tm1 = xmax32(tm1);
      const bool keepb = __all(tm1 - m1 <= THRS);
      float mnb = keepb ? m1 : fmaxf(m1, tm1);
      float alphab = keepb ? 1.f : __builtin_amdgcn_exp2f((m1 - mnb) * CS);
      m1 = mnb;
      mc = -mnb * CS;
      ps = 0.f;
      SBAR();
      acc[2] = MFMA32(cat8(vl[0][2], vh[0][2]), pf[0], acc[2]);
#pragma unroll
      for (int j = 0; j < 4; ++j) { e[j] = __builtin_amdgcn_exp2f(fmaf(s1[j], CS, mc)); ps += e[j]; }
      SBAR();
      acc[3] = MFMA32(cat8(vl[0][3], vh[0][3]), pf[0], acc[3]);
#pragma unroll
      for (int j = 4; j < 8; ++j) { e[j] = __builtin_amdgcn_exp2f(fmaf(s1[j], CS, mc)); ps += e[j]; }
      { u32x4 a = {cvtpk(e[0], e[1]), cvtpk(e[2], e[3]), cvtpk(e[4], e[5]), cvtpk(e[6], e[7])}; pf[2] = __builtin_bit_cast(bf16x8, a); }
      SBAR();
      issue_v(IC<2>{});
      WAITL8(8, vl[1][0], vh[1][0], vl[1][1], vh[1][1], vl[1][2], vh[1][2], vl[1][3], vh[1][3]);
      acc[0] = MFMA32(cat8(vl[1][0], vh[1][0]), pf[1], acc[0]);
#pragma unroll
      for (int j = 0; j < 4; ++j) { e[j] = __builtin_amdgcn_exp2f(fmaf(s1[8 + j], CS, mc)); ps += e[j]; }
      SBAR();
      acc[1] = MFMA32(cat8(vl[1][1], vh[1][1]), pf[1], acc[1]);
#pragma unroll
      for (int j = 4; j < 8; ++j) { e[j] = __builtin_amdgcn_exp2f(fmaf(s1[8 + j], CS, mc)); ps += e[j]; }
      { u32x4 a = {cvtpk(e[0], e[1]), cvtpk(e[2], e[3]), cvtpk(e[4], e[5]), cvtpk(e[6], e[7])}; pf[3] = __builtin_bit_cast(bf16x8, a); }
      l1 = l1 * alphab + ps;
      OPAQUE(l1);
      SBAR();
      acc[2] = MFMA32(cat8(vl[1][2], vh[1][2]), pf[1], acc[2]);
      float t2 = fmaxf(fmaxf(sr[0], sr[1]), fmaxf(sr[2], sr[3]));
      t2 = xmax16(t2);
      t2 = xmax32(t2);
      const bool keep2 = __all(t2 - m2 <= THRS);
      float mn2 = keep2 ? m2 : fmaxf(m2, t2);
      float alpha2 = keep2 ? 1.f : __builtin_amdgcn_exp2f((m2 - mn2) * CS);
      m2 = mn2;
      SBAR();
      acc[3] = MFMA32(cat8(vl[1][3], vh[1][3]), pf[1], acc[3]);
      const float mc2 = -mn2 * CS;
      float e0 = __builtin_amdgcn_exp2f(fmaf(sr[0], CS, mc2));
      float e1 = __builtin_amdgcn_exp2f(fmaf(sr[1], CS, mc2));
      float e2 = __builtin_amdgcn_exp2f(fmaf(sr[2], CS, mc2));
      float e3 = __builtin_amdgcn_exp2f(fmaf(sr[3], CS, mc2));
      l2 = l2 * alpha2 + (e0 + e1) + (e2 + e3);
      OPAQUE(l2);
      u32x2 p2u = {cvtpk(e0, e1), cvtpk(e2, e3)};
      s16x4 p2 = __builtin_bit_cast(s16x4, p2u);
      SBAR();
      if (!keepb) {
#pragma unroll
        for (int dt = 0; dt < 4; ++dt)
#pragma unroll
          for (int r = 0; r < 16; ++r) acc[dt][r] *= alphab;
      }
      if (!keep2) {
#pragma unroll
        for (int dd = 0; dd < 8; ++dd)
#pragma unroll
          for (int r = 0; r < 4; ++r) acc2[dd][r] *= alpha2;
      }
      issue_v(IC<3>{});
      WAITL8(8, vl[0][0], vh[0][0], vl[0][1], vh[0][1], vl[0][2], vh[0][2], vl[0][3], vh[0][3]);
#pragma unroll
      for (int dt = 0; dt < 4; ++dt) acc[dt] = MFMA32(cat8(vl[0][dt], vh[0][dt]), pf[2], acc[dt]);
      SBAR();
      s16x4 vr[8];
#pragma unroll
      for (int dd = 0; dd < 8; ++dd) vr[dd] = rdtr<OB>(va2_ ^ (dd << 5));
      WAITL8(8, vl[1][0], vh[1][0], vl[1][1], vh[1][1], vl[1][2], vh[1][2], vl[1][3], vh[1][3]);
#pragma unroll
      for (int dt = 0; dt < 4; ++dt) acc[dt] = MFMA32(cat8(vl[1][dt], vh[1][dt]), pf[3], acc[dt]);
      WAITL8(0, vr[0], vr[1], vr[2], vr[3], vr[4], vr[5], vr[6], vr[7]);
#pragma unroll
      for (int dd = 0; dd < 8; ++dd) acc2[dd] = MFMA16K16(vr[dd], p2, acc2[dd]);
    };
    for (int t = 0; t < 8; t += 2) {
      body(IC<0>{}, t);
      body(IC<1>{}, t + 1);
    }
  }

  int tz = threadIdx.x;
  OPAQUE(tz);
  asm volatile("s_waitcnt lgkmcnt(0)" ::: "memory");
  RAW_BAR();
  {
    const int l15 = tz & 15, g = (tz >> 4) & 3;
    float lt2 = xsum32(xsum16(l2));
    float* pw = (float*)(smem + ((w & 1) ? A_OFF_B + A_BUFB : A_OFF_A + A_BUFA) + (w >> 1) * 8192);
#pragma unroll
    for (int dd = 0; dd < 8; ++dd) *(f32x4*)(pw + l15 * 128 + 16 * dd + 4 * g) = acc2[dd];
    float* st = (float*)(smem + A_OFF_Q2);
    if (g == 0) { st[w * 32 + l15] = m2; st[w * 32 + 16 + l15] = lt2; }
  }
  asm volatile("s_waitcnt lgkmcnt(0)" ::: "memory");
  RAW_BAR();
  {
    const int q = tz >> 4, d0 = (tz & 15) * 8;
    const float* st = (const float*)(smem + A_OFF_Q2);
    float mm = fmaxf(fmaxf(st[q], st[32 + q]), fmaxf(st[64 + q], st[96 + q]));
    float o[8] = {0, 0, 0, 0, 0, 0, 0, 0};
    float ls = 0.f;
#pragma unroll
    for (int ww = 0; ww < 4; ++ww) {
      float sc = __builtin_amdgcn_exp2f((st[ww * 32 + q] - mm) * CS);
      ls += st[ww * 32 + 16 + q] * sc;
      const float* pw = (const float*)(smem + ((ww & 1) ? A_OFF_B + A_BUFB : A_OFF_A + A_BUFA) + (ww >> 1) * 8192) + q * 128 + d0;
      f32x4 a = *(const f32x4*)pw, c = *(const f32x4*)(pw + 4);
#pragma unroll
      for (int j = 0; j < 4; ++j) { o[j] += a[j] * sc; o[4 + j] += c[j] * sc; }
    }
    float inv = 1.f / ls;
    u32x4 pk = {cvtpk(o[0] * inv, o[1] * inv), cvtpk(o[2] * inv, o[3] * inv),
                cvtpk(o[4] * inv, o[5] * inv), cvtpk(o[6] * inv, o[7] * inv)};
    *(u32x4*)(og + (size_t)(b * SQ + 128 + q) * HD + h * DH + d0) = pk;
  }
  asm volatile("s_waitcnt lgkmcnt(0)" ::: "memory");
  RAW_BAR();
  {
    const int l31 = tz & 31, hh = (tz >> 5) & 1;
    float lt = xsum32(l1);
    float inv = 1.f / lt;
    char* ob = smem + (w < 2 ? A_OFF_A + A_BUFA : A_OFF_B + A_BUFB) + ((w & 1) * 32 + l31) * 256 + 8 * hh;
#pragma unroll
    for (int dt = 0; dt < 4; ++dt)
#pragma unroll
      for (int i = 0; i < 4; ++i) {
        u32x2 pk = {cvtpk(acc[dt][4 * i] * inv, acc[dt][4 * i + 1] * inv),
                    cvtpk(acc[dt][4 * i + 2] * inv, acc[dt][4 * i + 3] * inv)};
        *(u32x2*)(ob + (((4 * dt + i) ^ (l31 & 15)) << 4)) = pk;
      }
    if (tz == 0 && nidx >= 0) *slot = 4096u;
  }
  asm volatile("s_waitcnt lgkmcnt(0)" ::: "memory");
  RAW_BAR();
  {
    const int c = tz & 15, r0 = tz >> 4;
    short* orow = og + (size_t)(b * SQ + r0) * HD + h * DH + c * 8;
#pragma unroll
    for (int j = 0; j < 8; ++j) {
      const int r = r0 + 16 * j;
      const char* src = smem + (j < 4 ? A_OFF_A + A_BUFA + r * 256 : A_OFF_B + A_BUFB + (r - 64) * 256) + ((c ^ r0) << 4);
      bf16x8 v = *(const bf16x8*)src;
      *(bf16x8*)(orow + (size_t)(16 * j) * HD) = v;
    }
  }
  if (nidx < 0) break;
  pidx = nidx;
  }
}

constexpr int B_NST = 4;
constexpr int B_BUFA = 20480;
constexpr int B_BUFB = 8192;
constexpr int B_STRIDE = B_BUFA + B_BUFB;
constexpr int B_LDS = B_NST * B_STRIDE;

__global__ __launch_bounds__(576, 3) void oproj_kernel(const short* __restrict__ og,
                                                        const short* __restrict__ wo,
                                                        short* __restrict__ out) {
  extern __shared__ __attribute__((aligned(16))) char smem[];
  const int tid = threadIdx.x, lane = tid & 63;
  const int wv = __builtin_amdgcn_readfirstlane(tid >> 6);
  const int w = wv & 3;
  const int xg = blockIdx.x & 7, idx = blockIdx.x >> 3;
  const int b = xg * 4 + (idx >> 3), nt0 = idx & 7;
  const int l31 = lane & 31, hh = lane >> 5, l15 = lane & 15, g = lane >> 4;
  const int qp = l15 >> 2, p4 = lane & 3, blk = (lane >> 4) & 1;
  constexpr int NT = HD / 64;
  if (wv == 8) {
    constexpr int PFD = 12;
    const char* ob = (const char*)(og + (size_t)b * SQ * HD);
    const char* wob = (const char*)(wo + nt0 * 64);
    unsigned pv0 = 0, pv1 = 0, pv2 = 0, pv3 = 0;
    for (int t = -PFD; t < NT; ++t) {
      if (t >= 0) RAW_BAR();
      const int tp = t + PFD;
      if (tp < NT) {
        if ((tp & 7) == nt0) {
          const char* p0 = ob + (size_t)lane * (HD * 2) + tp * 128;
          const int r2 = lane + 128 > SQ - 1 ? SQ - 1 : lane + 128;
          asm volatile("global_load_dword %0, %1, off" : "+v"(pv0) : "v"(p0) : "memory");
          asm volatile("global_load_dword %0, %1, off" : "+v"(pv1) : "v"(p0 + (size_t)64 * (HD * 2)) : "memory");
          asm volatile("global_load_dword %0, %1, off" : "+v"(pv2) : "v"(ob + (size_t)r2 * (HD * 2) + tp * 128) : "memory");
        }
        if ((tp & 3) == (b & 3)) {
          asm volatile("global_load_dword %0, %1, off" : "+v"(pv3) : "v"(wob + (size_t)(tp * 64 + lane) * (DM * 2)) : "memory");
        }
      }
      asm volatile("s_waitcnt vmcnt(8)" ::: "memory");
    }
    asm volatile("s_waitcnt vmcnt(0)" : "+v"(pv0), "+v"(pv1), "+v"(pv2), "+v"(pv3) :: "memory");
    return;
  }
  if (wv >= 4) {
    const char* ob = (const char*)(og + (size_t)b * SQ * HD);
    const char* wob = (const char*)(wo + nt0 * 64);
    unsigned osrc[5], wsrc[2];
#pragma unroll
    for (int ii = 0; ii < 5; ++ii) {
      int r = 8 * (w * 5 + ii) + (lane >> 3), cp = lane & 7;
      int rs = r < SQ ? r : SQ - 1;
      osrc[ii] = rs * (HD * 2) + ((cp ^ ((r >> 1) & 7)) << 4);
    }
#pragma unroll
    for (int ii = 0; ii < 2; ++ii) {
      int r = 8 * (w * 2 + ii) + (lane >> 3), cp = lane & 7;
      wsrc[ii] = r * (DM * 2) + ((cp ^ (((r >> 1) & 1) << 2)) << 4);
    }
    auto stage = [&](int t) {
      const int buf = t & 3;
      char* la = smem + buf * B_STRIDE + w * 5120;
      char* lb = smem + buf * B_STRIDE + B_BUFA + w * 2048;
      const char* ot = ob + t * 128;
      const char* wt = wob + (size_t)t * (64 * DM * 2);
#pragma unroll
      for (int ii = 0; ii < 5; ++ii) glds16(ot + osrc[ii], la + ii * 1024);
#pragma unroll
      for (int ii = 0; ii < 2; ++ii) glds16(wt + wsrc[ii], lb + ii * 1024);
    };
    stage(0); stage(1); stage(2);
    asm volatile("s_waitcnt vmcnt(14)" ::: "memory");
    for (int t = 0; t < NT; ++t) {
      RAW_BAR();
      if (t + 3 < NT) { stage(t + 3); asm volatile("s_waitcnt vmcnt(14)" ::: "memory"); }
      else if (t + 2 < NT) asm volatile("s_waitcnt vmcnt(7)" ::: "memory");
      else asm volatile("s_waitcnt vmcnt(0)" ::: "memory");
    }
    return;
  }
  f32x16 acc[2] = {f32x16{}, f32x16{}};
  f32x4 acc2 = f32x4{};
  const int sb = (int)(uintptr_t)smem;
  const int xa = sb + (32 * w + l31) * 128 + ((hh ^ ((l31 >> 1) & 7)) << 4);
  const int wa = sb + B_BUFA + (8 * hh + qp) * 128 + (((2 * blk + (p4 >> 1)) ^ ((qp >> 1) << 2)) << 4) + 8 * (p4 & 1);
  const int xa2 = sb + (128 + l15) * 128 + ((g ^ ((l15 >> 1) & 7)) << 4);
  const int wa2 = sb + B_BUFA + (8 * g + qp) * 128 + (((2 * w + (p4 >> 1)) ^ ((qp >> 1) << 2)) << 4) + 8 * (p4 & 1);
  auto body = [&](auto BC) {
    constexpr int buf = decltype(BC)::value;
    constexpr int OB = (buf & 1) * B_STRIDE;
    const int hi2 = (buf >> 1) * 2 * B_STRIDE;
    RAW_BAR();
    const int xa_ = xa + hi2, w0 = wa + hi2, w1 = (wa ^ 64) + hi2, xa2_ = xa2 + hi2, wa2_ = wa2 + hi2;
    bf16x8 xf[4], xr[2];
    s16x4 wl[4][2], wh[4][2], rl[2], rh[2];
    sfor<0, 4>([&](auto SC) {
      constexpr int s = decltype(SC)::value;
      xf[s] = rd128<OB>(xa_ ^ (s << 5));
      wl[s][0] = rdtr<OB + s * 2048>(w0); wh[s][0] = rdtr<OB + s * 2048 + 512>(w0);
      wl[s][1] = rdtr<OB + s * 2048>(w1); wh[s][1] = rdtr<OB + s * 2048 + 512>(w1);
      if constexpr (s >= 1) {
        asm volatile("s_waitcnt lgkmcnt(5)" : "+v"(xf[s - 1]), "+v"(wl[s - 1][0]), "+v"(wh[s - 1][0]), "+v"(wl[s - 1][1]), "+v"(wh[s - 1][1]) :: "memory");
        acc[0] = MFMA32(cat8(wl[s - 1][0], wh[s - 1][0]), xf[s - 1], acc[0]);
        acc[1] = MFMA32(cat8(wl[s - 1][1], wh[s - 1][1]), xf[s - 1], acc[1]);
      }
    });
    xr[0] = rd128<OB>(xa2_); rl[0] = rdtr<OB>(wa2_); rh[0] = rdtr<OB + 512>(wa2_);
    xr[1] = rd128<OB>(xa2_ ^ 64); rl[1] = rdtr<OB + 4096>(wa2_); rh[1] = rdtr<OB + 4096 + 512>(wa2_);
    asm volatile("s_waitcnt lgkmcnt(6)" : "+v"(xf[3]), "+v"(wl[3][0]), "+v"(wh[3][0]), "+v"(wl[3][1]), "+v"(wh[3][1]) :: "memory");
    acc[0] = MFMA32(cat8(wl[3][0], wh[3][0]), xf[3], acc[0]);
    acc[1] = MFMA32(cat8(wl[3][1], wh[3][1]), xf[3], acc[1]);
    asm volatile("s_waitcnt lgkmcnt(0)" : "+v"(xr[0]), "+v"(rl[0]), "+v"(rh[0]), "+v"(xr[1]), "+v"(rl[1]), "+v"(rh[1]) :: "memory");
    acc2 = MFMA16(cat8(rl[0], rh[0]), xr[0], acc2);
    acc2 = MFMA16(cat8(rl[1], rh[1]), xr[1], acc2);
  };
  for (int t = 0; t < NT; t += 4) {
    body(IC<0>{});
    body(IC<1>{});
    body(IC<2>{});
    body(IC<3>{});
  }
  {
    short* orow = out + (size_t)(b * SQ + 32 * w + l31) * DM + nt0 * 64 + 4 * hh;
#pragma unroll
    for (int nt = 0; nt < 2; ++nt)
#pragma unroll
      for (int i = 0; i < 4; ++i) {
        u32x2 pk = {cvtpk(acc[nt][4 * i], acc[nt][4 * i + 1]), cvtpk(acc[nt][4 * i + 2], acc[nt][4 * i + 3])};
        *(u32x2*)(orow + 32 * nt + 8 * i) = pk;
      }
    u32x2 pk = {cvtpk(acc2[0], acc2[1]), cvtpk(acc2[2], acc2[3])};
    *(u32x2*)(out + (size_t)(b * SQ + 128 + l15) * DM + nt0 * 64 + 16 * w + 4 * g) = pk;
  }
}

extern "C" void kernel_launch(void* const* d_in, const int* in_sizes, int n_in, void* d_out,
                              int out_size, void* d_ws, size_t ws_size, hipStream_t stream) {
  const short* x = (const short*)d_in[0];
  const short* kc = (const short*)d_in[1];
  const short* vc = (const short*)d_in[2];
  const short* wq = (const short*)d_in[3];
  const short* wo = (const short*)d_in[4];
  short* og = (short*)d_ws;
  short* out = (short*)d_out;
  unsigned* counter = (unsigned*)(og + (size_t)MROWS * HD);
  (void)hipFuncSetAttribute((const void*)attn_kernel, hipFuncAttributeMaxDynamicSharedMemorySize, A_LDS);
  (void)hipFuncSetAttribute((const void*)oproj_kernel, hipFuncAttributeMaxDynamicSharedMemorySize, B_LDS);
  attn_kernel<<<512, 256, A_LDS, stream>>>(x, wq, kc, vc, og, counter);
  oproj_kernel<<<256, 576, B_LDS, stream>>>(og, wo, out);
}
```

```cpp
#include <hip/hip_runtime.h>
#include <hip/hip_bf16.h>

typedef __attribute__((ext_vector_type(8))) short bf16x8;
typedef __attribute__((ext_vector_type(4))) short s16x4;
typedef __attribute__((ext_vector_type(16))) float f32x16;
typedef __attribute__((ext_vector_type(4))) float f32x4;
typedef __attribute__((ext_vector_type(4))) unsigned u32x4;
typedef __attribute__((ext_vector_type(2))) unsigned u32x2;

constexpr int NB = 32, SQ = 144, DM = 512, NH = 32, SK = 512, DH = 128;
constexpr int HD = NH * DH;
constexpr int MROWS = NB * SQ;

#define MFMA32(a, b, c) __builtin_amdgcn_mfma_f32_32x32x16_bf16((a), (b), (c), 0, 0, 0)
#define MFMA16(a, b, c) __builtin_amdgcn_mfma_f32_16x16x32_bf16((a), (b), (c), 0, 0, 0)
#define MFMA16K16(a, b, c) __builtin_amdgcn_mfma_f32_16x16x16bf16_1k((a), (b), (c), 0, 0, 0)

__device__ __forceinline__ unsigned cvtpk(float lo, float hi) {
  unsigned r;
  asm volatile("v_cvt_pk_bf16_f32 %0, %1, %2" : "=v"(r) : "v"(lo), "v"(hi));
  return r;
}
__device__ __forceinline__ bf16x8 cat8(s16x4 lo, s16x4 hi) {
  return __builtin_shufflevector(lo, hi, 0, 1, 2, 3, 4, 5, 6, 7);
}
__device__ __forceinline__ void glds16(const void* g, char* l) {
  __builtin_amdgcn_global_load_lds((const unsigned*)g, (unsigned*)l, 16, 0, 0);
}
__device__ __forceinline__ void glds16nt(const void* g, char* l) {
  __builtin_amdgcn_global_load_lds((const unsigned*)g, (unsigned*)l, 16, 0, 2);
}
__device__ __forceinline__ float xmax32(float v) {
  auto rr = __builtin_amdgcn_permlane32_swap(__float_as_uint(v), __float_as_uint(v), false, false);
  return fmaxf(__uint_as_float(rr[0]), __uint_as_float(rr[1]));
}
__device__ __forceinline__ float xsum32(float v) {
  auto rr = __builtin_amdgcn_permlane32_swap(__float_as_uint(v), __float_as_uint(v), false, false);
  return __uint_as_float(rr[0]) + __uint_as_float(rr[1]);
}
__device__ __forceinline__ float xmax16(float v) {
  auto rr = __builtin_amdgcn_permlane16_swap(__float_as_uint(v), __float_as_uint(v), false, false);
  return fmaxf(__uint_as_float(rr[0]), __uint_as_float(rr[1]));
}
__device__ __forceinline__ float xsum16(float v) {
  auto rr = __builtin_amdgcn_permlane16_swap(__float_as_uint(v), __float_as_uint(v), false, false);
  return __uint_as_float(rr[0]) + __uint_as_float(rr[1]);
}
template <int OFF> __device__ __forceinline__ bf16x8 rd128(int a) {
  bf16x8 r;
  asm volatile("ds_read_b128 %0, %1 offset:%2" : "=v"(r) : "v"(a), "i"(OFF) : "memory");
  return r;
}
template <int OFF> __device__ __forceinline__ s16x4 rdtr(int a) {
  s16x4 r;
  asm volatile("ds_read_b64_tr_b16 %0, %1 offset:%2" : "=v"(r) : "v"(a), "i"(OFF) : "memory");
  return r;
}
#define WAITL2(N, a, b) asm volatile("s_waitcnt lgkmcnt(" #N ")" : "+v"(a), "+v"(b) :: "memory")
#define WAITL4(N, a, b, c, d) asm volatile("s_waitcnt lgkmcnt(" #N ")" : "+v"(a), "+v"(b), "+v"(c), "+v"(d) :: "memory")
#define WAITL8(N, a, b, c, d, e, f, g_, h_) asm volatile("s_waitcnt lgkmcnt(" #N ")" : "+v"(a), "+v"(b), "+v"(c), "+v"(d), "+v"(e), "+v"(f), "+v"(g_), "+v"(h_) :: "memory")
#define RAW_BAR() __builtin_amdgcn_s_barrier()
#define SBAR() __builtin_amdgcn_sched_barrier(0)
#define OPAQUE(v) asm volatile("" : "+v"(v))
template <int N> struct IC { static constexpr int value = N; };
template <int I, int N, class F> __device__ __forceinline__ void sfor(F&& f) {
  if constexpr (I < N) { f(IC<I>{}); sfor<I + 1, N>(f); }
}

constexpr int A_BUFA = 20480;
constexpr int A_BUFB = 16384;
constexpr int A_OFF_A = 0;
constexpr int A_OFF_B = 2 * A_BUFA;
constexpr int A_OFF_Q2 = A_OFF_B + 2 * A_BUFB;
constexpr int A_OFF_SLOT = A_OFF_Q2 + 4096;
constexpr int A_LDS = A_OFF_SLOT + 16;

__global__ __launch_bounds__(256, 2) void attn_kernel(
    const short* __restrict__ x, const short* __restrict__ wq, const short* __restrict__ kc,
    const short* __restrict__ vc, short* __restrict__ og, unsigned* __restrict__ counter) {
  extern __shared__ __attribute__((aligned(16))) char smem[];
  const int tid = threadIdx.x, lane = tid & 63;
  const int w = __builtin_amdgcn_readfirstlane(tid >> 6);
  const int l31 = lane & 31, hh = lane >> 5, l15 = lane & 15, g = lane >> 4;
  const int qp = l15 >> 2, p4 = lane & 3, blk = (lane >> 4) & 1;
  const int sb = (int)(uintptr_t)smem;
  volatile unsigned* slot = (volatile unsigned*)(smem + A_OFF_SLOT);

  const unsigned ksrc = (16 * w + g) * 256 + ((l15 ^ g) << 4);
  const unsigned vsrc = (16 * w + g) * 256 + ((l15 ^ (g << 2)) << 4);
  auto stage_kv = [&](int pi, int t, int buf, unsigned ko, unsigned vo) {
    const char* kb = (const char*)kc + (size_t)pi * (SK * DH * 2) + t * 16384;
    const char* vb = (const char*)vc + (size_t)pi * (SK * DH * 2) + t * 16384;
#pragma unroll
    for (int i = 0; i < 4; ++i)
      glds16nt(kb + ((ko ^ (i << 6)) + i * 1024), smem + A_OFF_A + buf * A_BUFA + w * 4096 + i * 1024);
#pragma unroll
    for (int i = 0; i < 4; ++i)
      glds16nt(vb + (vo + i * 1024), smem + A_OFF_B + buf * A_BUFB + w * 4096 + i * 1024);
  };
  auto stage_q = [&](int pi, int t, int buf) {
    int ln = lane;
    OPAQUE(ln);
    const char* xt = (const char*)(x + (size_t)(pi >> 5) * SQ * DM) + t * 128;
    const char* wt = (const char*)(wq + (pi & 31) * DH) + (size_t)t * (64 * HD * 2);
#pragma unroll
    for (int ii = 0; ii < 5; ++ii) {
      int r = 8 * (w * 5 + ii) + (ln >> 3), cp = ln & 7;
      int rs = r < SQ ? r : SQ - 1;
      unsigned so = rs * 1024 + ((cp ^ ((r >> 1) & 7)) << 4);
      glds16(xt + so, smem + A_OFF_A + buf * A_BUFA + w * 5120 + ii * 1024);
    }
    const unsigned wo0 = (16 * w + (ln >> 4)) * (HD * 2) + (((ln & 15) ^ ((ln >> 4) << 2)) << 4);
#pragma unroll
    for (int ii = 0; ii < 4; ++ii)
      glds16(wt + (wo0 + ii * 4 * HD * 2), smem + A_OFF_B + buf * A_BUFB + w * 4096 + ii * 1024);
  };
  int pidx = (blockIdx.x & 7) * 128 + (blockIdx.x >> 3);
#define STAG 0
  if (STAG > 0 && blockIdx.x >= 256) {
    unsigned long long t0 = __builtin_amdgcn_s_memtime();
    while (__builtin_amdgcn_s_memtime() - t0 < STAG) __builtin_amdgcn_s_sleep(8);
  }
  stage_q(pidx, 0, 0);
  if (tid == 0) *slot = blockIdx.x + 512u;
  constexpr float CS = 0.088388347648318440f * 1.4426950408889634f;
  constexpr float THRS = 8.f / CS;

  for (;;) {
  const int b = pidx >> 5, h = pidx & 31;
  f32x16 acc[4];
#pragma unroll
  for (int i = 0; i < 4; ++i) acc[i] = f32x16{};
  f32x4 acc2[8];
#pragma unroll
  for (int i = 0; i < 8; ++i) acc2[i] = f32x4{};
  {
    int lq = lane;
    OPAQUE(lq);
    const int l31 = lq & 31, hh = lq >> 5, l15 = lq & 15, g = lq >> 4;
    const int qp = l15 >> 2, p4 = lq & 3, blk = (lq >> 4) & 1;
    const int xa = sb + A_OFF_A + (32 * w + l31) * 128 + ((hh ^ ((l31 >> 1) & 7)) << 4);
    const int wa = sb + A_OFF_B + (8 * hh + qp) * 256 + (((2 * blk + (p4 >> 1)) ^ (qp << 2)) << 4) + 8 * (p4 & 1);
    const int xa2 = sb + A_OFF_A + (128 + l15) * 128 + ((g ^ ((l15 >> 1) & 7)) << 4);
    const int wa2 = sb + A_OFF_B + (8 * g + qp) * 256 + (((4 * w + (p4 >> 1)) ^ (qp << 2)) << 4) + 8 * (p4 & 1);
    auto body = [&](auto BC, int t) {
      constexpr int buf = decltype(BC)::value;
      constexpr int OA = buf * A_BUFA, OB = buf * A_BUFB;
      asm volatile("s_waitcnt vmcnt(0) lgkmcnt(0)" ::: "memory");
      RAW_BAR();
      if (t < 7) stage_q(pidx, t + 1, buf ^ 1);
      else stage_kv(pidx, 0, buf ^ 1, ksrc, vsrc);
      SBAR();
      const int w0 = wa, w1 = wa ^ 64, w2 = wa ^ 128, w3 = wa ^ 192;
      bf16x8 xf[2];
      s16x4 wl[2][4], wh[2][4];
      bf16x8 xr[2];
      s16x4 rl[2][2], rh[2][2];
      auto issue = [&](auto SC) {
        constexpr int s = decltype(SC)::value, u = s & 1;
        xf[u] = rd128<OA>(xa ^ (s << 5));
        wl[u][0] = rdtr<OB + s * 4096>(w0); wh[u][0] = rdtr<OB + s * 4096 + 1024>(w0);
        wl[u][1] = rdtr<OB + s * 4096>(w1); wh[u][1] = rdtr<OB + s * 4096 + 1024>(w1);
        wl[u][2] = rdtr<OB + s * 4096>(w2); wh[u][2] = rdtr<OB + s * 4096 + 1024>(w2);
        wl[u][3] = rdtr<OB + s * 4096>(w3); wh[u][3] = rdtr<OB + s * 4096 + 1024>(w3);
      };
      issue(IC<0>{});
      sfor<0, 4>([&](auto SC) {
        constexpr int s = decltype(SC)::value, u = s & 1;
        if constexpr (s < 3) {
          issue(IC<s + 1>{});
          asm volatile("s_waitcnt lgkmcnt(9)" : "+v"(xf[u]), "+v"(wl[u][0]), "+v"(wh[u][0]), "+v"(wl[u][1]), "+v"(wh[u][1]),
                       "+v"(wl[u][2]), "+v"(wh[u][2]), "+v"(wl[u][3]), "+v"(wh[u][3]) :: "memory");
        } else {
          xr[0] = rd128<OA>(xa2); xr[1] = rd128<OA>(xa2 ^ 64);
          rl[0][0] = rdtr<OB>(wa2); rh[0][0] = rdtr<OB + 1024>(wa2);
          rl[0][1] = rdtr<OB>(wa2 ^ 32); rh[0][1] = rdtr<OB + 1024>(wa2 ^ 32);
          rl[1][0] = rdtr<OB + 8192>(wa2); rh[1][0] = rdtr<OB + 8192 + 1024>(wa2);
          rl[1][1] = rdtr<OB + 8192>(wa2 ^ 32); rh[1][1] = rdtr<OB + 8192 + 1024>(wa2 ^ 32);
          asm volatile("s_waitcnt lgkmcnt(10)" : "+v"(xf[u]), "+v"(wl[u][0]), "+v"(wh[u][0]), "+v"(wl[u][1]), "+v"(wh[u][1]),
                       "+v"(wl[u][2]), "+v"(wh[u][2]), "+v"(wl[u][3]), "+v"(wh[u][3]) :: "memory");
        }
#pragma unroll
        for (int dt = 0; dt < 4; ++dt) acc[dt] = MFMA32(cat8(wl[u][dt], wh[u][dt]), xf[u], acc[dt]);
        SBAR();
      });
      asm volatile("s_waitcnt lgkmcnt(0)" : "+v"(xr[0]), "+v"(xr[1]), "+v"(rl[0][0]), "+v"(rh[0][0]), "+v"(rl[0][1]), "+v"(rh[0][1]),
                   "+v"(rl[1][0]), "+v"(rh[1][0]), "+v"(rl[1][1]), "+v"(rh[1][1]) :: "memory");
#pragma unroll
      for (int s2 = 0; s2 < 2; ++s2)
#pragma unroll
        for (int e = 0; e < 2; ++e) acc2[e] = MFMA16(cat8(rl[s2][e], rh[s2][e]), xr[s2], acc2[e]);
    };
    for (int t = 0; t < 8; t += 2) {
      body(IC<0>{}, t);
      body(IC<1>{}, t + 1);
    }
  }
  bf16x8 qf[8];
#pragma unroll
  for (int dt = 0; dt < 4; ++dt) {
#pragma unroll
    for (int s = 0; s < 2; ++s) {
      unsigned a0 = cvtpk(acc[dt][8 * s + 0], acc[dt][8 * s + 1]);
      unsigned a1 = cvtpk(acc[dt][8 * s + 2], acc[dt][8 * s + 3]);
      unsigned b0 = cvtpk(acc[dt][8 * s + 4], acc[dt][8 * s + 5]);
      unsigned b1 = cvtpk(acc[dt][8 * s + 6], acc[dt][8 * s + 7]);
      auto r0 = __builtin_amdgcn_permlane32_swap(a0, b0, false, false);
      auto r1 = __builtin_amdgcn_permlane32_swap(a1, b1, false, false);
      u32x4 wv = {r0[0], r1[0], r0[1], r1[1]};
      qf[2 * dt + s] = __builtin_bit_cast(bf16x8, wv);
    }
  }
  {
#pragma unroll
    for (int e = 0; e < 2; ++e) {
      int dd = 2 * w + e;
      u32x2 pk = {cvtpk(acc2[e][0], acc2[e][1]), cvtpk(acc2[e][2], acc2[e][3])};
      int ch = (2 * dd + (g >> 1)) ^ l15;
      asm volatile("ds_write_b64 %0, %1" :: "v"(sb + A_OFF_Q2 + l15 * 256 + (ch << 4) + 8 * (g & 1)), "v"(pk) : "memory");
    }
  }
#pragma unroll
  for (int i = 0; i < 4; ++i) acc[i] = f32x16{};
#pragma unroll
  for (int i = 0; i < 8; ++i) acc2[i] = f32x4{};
  int nidx = 0;
  float m1 = -1e30f, l1 = 0.f, m2 = -1e30f, l2 = 0.f;
  {
    int ka = sb + A_OFF_A + l31 * 256 + ((hh ^ l15) << 4);
    int ka2 = sb + A_OFF_A + (16 * w + l15) * 256 + ((g ^ l15) << 4);
    int q2a = sb + A_OFF_Q2 + l15 * 256 + ((g ^ l15) << 4);
    int va = sb + A_OFF_B + (4 * hh + qp) * 256 + (((2 * blk + (p4 >> 1)) ^ (qp << 2)) << 4) + 8 * (p4 & 1);
    int va2 = sb + A_OFF_B + (16 * w + 4 * g + qp) * 256 + (((p4 >> 1) ^ (qp << 2)) << 4) + 8 * (p4 & 1);
    auto body = [&](auto BC, int t) {
      constexpr int buf = decltype(BC)::value;
      constexpr int OA = buf * A_BUFA, OB = buf * A_BUFB;
      asm volatile("s_waitcnt vmcnt(0) lgkmcnt(0)" ::: "memory");
      RAW_BAR();
      int ka_ = ka, ka2_ = ka2, q2a_ = q2a, va_ = va, va2_ = va2;
      unsigned ko = ksrc, vo = vsrc;
      OPAQUE(ka_); OPAQUE(ka2_); OPAQUE(q2a_); OPAQUE(va_); OPAQUE(va2_); OPAQUE(ko); OPAQUE(vo);
      if (t < 7) {
        stage_kv(pidx, t + 1, buf ^ 1, ko, vo);
      } else {
        const unsigned it = __builtin_amdgcn_readfirstlane(*slot);
        nidx = it < 1024u ? (int)((it & 7) * 128 + (it >> 3)) : -1;
        if (nidx >= 0) stage_q(nidx, 0, 0);
      }
      SBAR();
      f32x16 s0 = f32x16{}, s1 = f32x16{};
      f32x4 sr = f32x4{};
      bf16x8 kA[4], kB[4];
#pragma unroll
      for (int i = 0; i < 4; ++i) kA[i] = rd128<OA>(ka_ ^ (i << 5));
#pragma unroll
      for (int i = 0; i < 4; ++i) kB[i] = rd128<OA>(ka_ ^ ((4 + i) << 5));
      WAITL4(4, kA[0], kA[1], kA[2], kA[3]);
      s0 = MFMA32(kA[0], qf[0], s0);
      s0 = MFMA32(kA[1], qf[1], s0);
      SBAR();
      s0 = MFMA32(kA[2], qf[2], s0);
      s0 = MFMA32(kA[3], qf[3], s0);
      SBAR();
#pragma unroll
      for (int i = 0; i < 4; ++i) kA[i] = rd128<OA + 8192>(ka_ ^ (i << 5));
      WAITL4(4, kB[0], kB[1], kB[2], kB[3]);
      s0 = MFMA32(kB[0], qf[4], s0);
      s0 = MFMA32(kB[1], qf[5], s0);
      SBAR();
      s0 = MFMA32(kB[2], qf[6], s0);
      s0 = MFMA32(kB[3], qf[7], s0);
      SBAR();
#pragma unroll
      for (int i = 0; i < 4; ++i) kB[i] = rd128<OA + 8192>(ka_ ^ ((4 + i) << 5));
      WAITL4(4, kA[0], kA[1], kA[2], kA[3]);
      s1 = MFMA32(kA[0], qf[0], s1);
      SBAR();
      s1 = MFMA32(kA[1], qf[1], s1);
      float tmax = fmaxf(fmaxf(s0[0], s0[1]), s0[2]);
#pragma unroll
      for (int r = 3; r < 9; r += 2) tmax = fmaxf(fmaxf(tmax, s0[r]), s0[r + 1]);
      SBAR();
      s1 = MFMA32(kA[2], qf[2], s1);
#pragma unroll
      for (int r = 9; r < 15; r += 2) tmax = fmaxf(fmaxf(tmax, s0[r]), s0[r + 1]);
      tmax = fmaxf(tmax, s0[15]);
      tmax = xmax32(tmax);
      SBAR();
      s1 = MFMA32(kA[3], qf[3], s1);
      SBAR();
      if (!__all(tmax - m1 <= THRS)) {
        float mn0 = fmaxf(m1, tmax);
        float alpha = __builtin_amdgcn_exp2f((m1 - mn0) * CS);
        m1 = mn0;
        l1 *= alpha;
#pragma unroll
        for (int dt = 0; dt < 4; ++dt)
#pragma unroll
          for (int r = 0; r < 16; ++r) acc[dt][r] *= alpha;
      }
      const float mn = m1;
      bf16x8 kr[4], qr[4];
#pragma unroll
      for (int ks = 0; ks < 4; ++ks) { kr[ks] = rd128<OA>(ka2_ ^ (ks << 6)); qr[ks] = rd128<0>(q2a_ ^ (ks << 6)); }
      WAITL4(8, kB[0], kB[1], kB[2], kB[3]);
      bf16x8 pf[4];
      float mc = -mn * CS;
      float psv[4] = {0.f, 0.f, 0.f, 0.f};
      float e[8];
      s1 = MFMA32(kB[0], qf[4], s1);
#pragma unroll
      for (int j = 0; j < 4; ++j) { e[j] = __builtin_amdgcn_exp2f(fmaf(s0[j], CS, mc)); psv[j & 3] += e[j]; }
      SBAR();
      s1 = MFMA32(kB[1], qf[5], s1);
#pragma unroll
      for (int j = 4; j < 8; ++j) { e[j] = __builtin_amdgcn_exp2f(fmaf(s0[j], CS, mc)); psv[j & 3] += e[j]; }
      { u32x4 a = {cvtpk(e[0], e[1]), cvtpk(e[2], e[3]), cvtpk(e[4], e[5]), cvtpk(e[6], e[7])}; pf[0] = __builtin_bit_cast(bf16x8, a); }
      SBAR();
      s1 = MFMA32(kB[2], qf[6], s1);
#pragma unroll
      for (int j = 0; j < 4; ++j) { e[j] = __builtin_amdgcn_exp2f(fmaf(s0[8 + j], CS, mc)); psv[j & 3] += e[j]; }
      SBAR();
      s1 = MFMA32(kB[3], qf[7], s1);
#pragma unroll
      for (int j = 4; j < 8; ++j) { e[j] = __builtin_amdgcn_exp2f(fmaf(s0[8 + j], CS, mc)); psv[j & 3] += e[j]; }
      { u32x4 a = {cvtpk(e[0], e[1]), cvtpk(e[2], e[3]), cvtpk(e[4], e[5]), cvtpk(e[6], e[7])}; pf[1] = __builtin_bit_cast(bf16x8, a); }
      l1 += (psv[0] + psv[1]) + (psv[2] + psv[3]);
      OPAQUE(l1);
      SBAR();
      SBAR();
      const int v0 = va_, v1 = va_ ^ 64, v2 = va_ ^ 128, v3 = va_ ^ 192;
      s16x4 vl[2][4], vh[2][4];
      auto issue_v = [&](auto SC) {
        constexpr int s = decltype(SC)::value, u = s & 1;
        vl[u][0] = rdtr<OB + s * 4096>(v0); vh[u][0] = rdtr<OB + s * 4096 + 2048>(v0);
        vl[u][1] = rdtr<OB + s * 4096>(v1); vh[u][1] = rdtr<OB + s * 4096 + 2048>(v1);
        vl[u][2] = rdtr<OB + s * 4096>(v2); vh[u][2] = rdtr<OB + s * 4096 + 2048>(v2);
        vl[u][3] = rdtr<OB + s * 4096>(v3); vh[u][3] = rdtr<OB + s * 4096 + 2048>(v3);
      };
      issue_v(IC<0>{});
      WAITL8(8, kr[0], kr[1], kr[2], kr[3], qr[0], qr[1], qr[2], qr[3]);
#pragma unroll
      for (int ks = 0; ks < 4; ++ks) sr = MFMA16(kr[ks], qr[ks], sr);
      SBAR();
      SBAR();
      issue_v(IC<1>{});
      WAITL8(8, vl[0][0], vh[0][0], vl[0][1], vh[0][1], vl[0][2], vh[0][2], vl[0][3], vh[0][3]);
      acc[0] = MFMA32(cat8(vl[0][0], vh[0][0]), pf[0], acc[0]);
      float tm1 = fmaxf(fmaxf(s1[0], s1[1]), s1[2]);
#pragma unroll
      for (int r = 3; r < 9; r += 2) tm1 = fmaxf(fmaxf(tm1, s1[r]), s1[r + 1]);
      SBAR();
      acc[1] = MFMA32(cat8(vl[0][1], vh[0][1]), pf[0], acc[1]);
#pragma unroll
      for (int r = 9; r < 15; r += 2) tm1 = fmaxf(fmaxf(tm1, s1[r]), s1[r + 1]);
      tm1 = fmaxf(tm1, s1[15]);
      tm1 = xmax32(tm1);
      const bool keepb = __all(tm1 - m1 <= THRS);
      float mnb = keepb ? m1 : fmaxf(m1, tm1);
      float alphab = keepb ? 1.f : __builtin_amdgcn_exp2f((m1 - mnb) * CS);
      m1 = mnb;
      mc = -mnb * CS;
      psv[0] = psv[1] = psv[2] = psv[3] = 0.f;
      SBAR();
      acc[2] = MFMA32(cat8(vl[0][2], vh[0][2]), pf[0], acc[2]);
#pragma unroll
      for (int j = 0; j < 4; ++j) { e[j] = __builtin_amdgcn_exp2f(fmaf(s1[j], CS, mc)); psv[j & 3] += e[j]; }
      SBAR();
      acc[3] = MFMA32(cat8(vl[0][3], vh[0][3]), pf[0], acc[3]);
#pragma unroll
      for (int j = 4; j < 8; ++j) { e[j] = __builtin_amdgcn_exp2f(fmaf(s1[j], CS, mc)); psv[j & 3] += e[j]; }
      { u32x4 a = {cvtpk(e[0], e[1]), cvtpk(e[2], e[3]), cvtpk(e[4], e[5]), cvtpk(e[6], e[7])}; pf[2] = __builtin_bit_cast(bf16x8, a); }
      SBAR();
      issue_v(IC<2>{});
      WAITL8(8, vl[1][0], vh[1][0], vl[1][1], vh[1][1], vl[1][2], vh[1][2], vl[1][3], vh[1][3]);
      acc[0] = MFMA32(cat8(vl[1][0], vh[1][0]), pf[1], acc[0]);
#pragma unroll
      for (int j = 0; j < 4; ++j) { e[j] = __builtin_amdgcn_exp2f(fmaf(s1[8 + j], CS, mc)); psv[j & 3] += e[j]; }
      SBAR();
      acc[1] = MFMA32(cat8(vl[1][1], vh[1][1]), pf[1], acc[1]);
#pragma unroll
      for (int j = 4; j < 8; ++j) { e[j] = __builtin_amdgcn_exp2f(fmaf(s1[8 + j], CS, mc)); psv[j & 3] += e[j]; }
      { u32x4 a = {cvtpk(e[0], e[1]), cvtpk(e[2], e[3]), cvtpk(e[4], e[5]), cvtpk(e[6], e[7])}; pf[3] = __builtin_bit_cast(bf16x8, a); }
      l1 = l1 * alphab + ((psv[0] + psv[1]) + (psv[2] + psv[3]));
      OPAQUE(l1);
      SBAR();
      acc[2] = MFMA32(cat8(vl[1][2], vh[1][2]), pf[1], acc[2]);
      float t2 = fmaxf(fmaxf(sr[0], sr[1]), fmaxf(sr[2], sr[3]));
      t2 = xmax16(t2);
      t2 = xmax32(t2);
      const bool keep2 = __all(t2 - m2 <= THRS);
      float mn2 = keep2 ? m2 : fmaxf(m2, t2);
      float alpha2 = keep2 ? 1.f : __builtin_amdgcn_exp2f((m2 - mn2) * CS);
      m2 = mn2;
      SBAR();
      acc[3] = MFMA32(cat8(vl[1][3], vh[1][3]), pf[1], acc[3]);
      const float mc2 = -mn2 * CS;
      float e0 = __builtin_amdgcn_exp2f(fmaf(sr[0], CS, mc2));
      float e1 = __builtin_amdgcn_exp2f(fmaf(sr[1], CS, mc2));
      float e2 = __builtin_amdgcn_exp2f(fmaf(sr[2], CS, mc2));
      float e3 = __builtin_amdgcn_exp2f(fmaf(sr[3], CS, mc2));
      l2 = l2 * alpha2 + (e0 + e1) + (e2 + e3);
      OPAQUE(l2);
      u32x2 p2u = {cvtpk(e0, e1), cvtpk(e2, e3)};
      s16x4 p2 = __builtin_bit_cast(s16x4, p2u);
      SBAR();
      if (!keepb) {
#pragma unroll
        for (int dt = 0; dt < 4; ++dt)
#pragma unroll
          for (int r = 0; r < 16; ++r) acc[dt][r] *= alphab;
      }
      if (!keep2) {
#pragma unroll
        for (int dd = 0; dd < 8; ++dd)
#pragma unroll
          for (int r = 0; r < 4; ++r) acc2[dd][r] *= alpha2;
      }
      issue_v(IC<3>{});
      WAITL8(8, vl[0][0], vh[0][0], vl[0][1], vh[0][1], vl[0][2], vh[0][2], vl[0][3], vh[0][3]);
#pragma unroll
      for (int dt = 0; dt < 4; ++dt) acc[dt] = MFMA32(cat8(vl[0][dt], vh[0][dt]), pf[2], acc[dt]);
      SBAR();
      s16x4 vr[8];
#pragma unroll
      for (int dd = 0; dd < 8; ++dd) vr[dd] = rdtr<OB>(va2_ ^ (dd << 5));
      WAITL8(8, vl[1][0], vh[1][0], vl[1][1], vh[1][1], vl[1][2], vh[1][2], vl[1][3], vh[1][3]);
#pragma unroll
      for (int dt = 0; dt < 4; ++dt) acc[dt] = MFMA32(cat8(vl[1][dt], vh[1][dt]), pf[3], acc[dt]);
      WAITL8(0, vr[0], vr[1], vr[2], vr[3], vr[4], vr[5], vr[6], vr[7]);
#pragma unroll
      for (int dd = 0; dd < 8; ++dd) acc2[dd] = MFMA16K16(vr[dd], p2, acc2[dd]);
    };
    for (int t = 0; t < 8; t += 2) {
      body(IC<0>{}, t);
      body(IC<1>{}, t + 1);
    }
  }

  int tz = threadIdx.x;
  OPAQUE(tz);
  asm volatile("s_waitcnt lgkmcnt(0)" ::: "memory");
  RAW_BAR();
  {
    const int l15 = tz & 15, g = (tz >> 4) & 3;
    float lt2 = xsum32(xsum16(l2));
    float* pw = (float*)(smem + ((w & 1) ? A_OFF_B + A_BUFB : A_OFF_A + A_BUFA) + (w >> 1) * 8192);
#pragma unroll
    for (int dd = 0; dd < 8; ++dd) *(f32x4*)(pw + l15 * 128 + 16 * dd + 4 * g) = acc2[dd];
    float* st = (float*)(smem + A_OFF_Q2);
    if (g == 0) { st[w * 32 + l15] = m2; st[w * 32 + 16 + l15] = lt2; }
  }
  asm volatile("s_waitcnt lgkmcnt(0)" ::: "memory");
  RAW_BAR();
  {
    const int q = tz >> 4, d0 = (tz & 15) * 8;
    const float* st = (const float*)(smem + A_OFF_Q2);
    float mm = fmaxf(fmaxf(st[q], st[32 + q]), fmaxf(st[64 + q], st[96 + q]));
    float o[8] = {0, 0, 0, 0, 0, 0, 0, 0};
    float ls = 0.f;
#pragma unroll
    for (int ww = 0; ww < 4; ++ww) {
      float sc = __builtin_amdgcn_exp2f((st[ww * 32 + q] - mm) * CS);
      ls += st[ww * 32 + 16 + q] * sc;
      const float* pw = (const float*)(smem + ((ww & 1) ? A_OFF_B + A_BUFB : A_OFF_A + A_BUFA) + (ww >> 1) * 8192) + q * 128 + d0;
      f32x4 a = *(const f32x4*)pw, c = *(const f32x4*)(pw + 4);
#pragma unroll
      for (int j = 0; j < 4; ++j) { o[j] += a[j] * sc; o[4 + j] += c[j] * sc; }
    }
    float inv = 1.f / ls;
    u32x4 pk = {cvtpk(o[0] * inv, o[1] * inv), cvtpk(o[2] * inv, o[3] * inv),
                cvtpk(o[4] * inv, o[5] * inv), cvtpk(o[6] * inv, o[7] * inv)};
    *(u32x4*)(og + (size_t)(b * SQ + 128 + q) * HD + h * DH + d0) = pk;
  }
  asm volatile("s_waitcnt lgkmcnt(0)" ::: "memory");
  RAW_BAR();
  {
    const int l31 = tz & 31, hh = (tz >> 5) & 1;
    float lt = xsum32(l1);
    float inv = 1.f / lt;
    char* ob = smem + (w < 2 ? A_OFF_A + A_BUFA : A_OFF_B + A_BUFB) + ((w & 1) * 32 + l31) * 256 + 8 * hh;
#pragma unroll
    for (int dt = 0; dt < 4; ++dt)
#pragma unroll
      for (int i = 0; i < 4; ++i) {
        u32x2 pk = {cvtpk(acc[dt][4 * i] * inv, acc[dt][4 * i + 1] * inv),
                    cvtpk(acc[dt][4 * i + 2] * inv, acc[dt][4 * i + 3] * inv)};
        *(u32x2*)(ob + (((4 * dt + i) ^ (l31 & 15)) << 4)) = pk;
      }
    if (tz == 0 && nidx >= 0) *slot = 4096u;
  }
  asm volatile("s_waitcnt lgkmcnt(0)" ::: "memory");
  RAW_BAR();
  {
    const int c = tz & 15, r0 = tz >> 4;
    short* orow = og + (size_t)(b * SQ + r0) * HD + h * DH + c * 8;
#pragma unroll
    for (int j = 0; j < 8; ++j) {
      const int r = r0 + 16 * j;
      const char* src = smem + (j < 4 ? A_OFF_A + A_BUFA + r * 256 : A_OFF_B + A_BUFB + (r - 64) * 256) + ((c ^ r0) << 4);
      bf16x8 v = *(const bf16x8*)src;
      *(bf16x8*)(orow + (size_t)(16 * j) * HD) = v;
    }
  }
  if (nidx < 0) break;
  pidx = nidx;
  }
}

constexpr int B_NST = 4;
constexpr int B_BUFA = 20480;
constexpr int B_BUFB = 8192;
constexpr int B_STRIDE = B_BUFA + B_BUFB;
constexpr int B_LDS = B_NST * B_STRIDE;

__global__ __launch_bounds__(512, 2) void oproj_kernel(const short* __restrict__ og,
                                                        const short* __restrict__ wo,
                                                        short* __restrict__ out) {
  extern __shared__ __attribute__((aligned(16))) char smem[];
  const int tid = threadIdx.x, lane = tid & 63;
  const int wv = __builtin_amdgcn_readfirstlane(tid >> 6);
  const int w = wv & 3;
  const int xg = blockIdx.x & 7, idx = blockIdx.x >> 3;
  const int b = xg * 4 + (idx >> 3), nt0 = idx & 7;
  const int l31 = lane & 31, hh = lane >> 5, l15 = lane & 15, g = lane >> 4;
  const int qp = l15 >> 2, p4 = lane & 3, blk = (lane >> 4) & 1;
  constexpr int NT = HD / 64;
  if (wv >= 4) {
    const char* ob = (const char*)(og + (size_t)b * SQ * HD);
    const char* wob = (const char*)(wo + nt0 * 64);
    unsigned osrc[5], wsrc[2];
#pragma unroll
    for (int ii = 0; ii < 5; ++ii) {
      int r = 8 * (w * 5 + ii) + (lane >> 3), cp = lane & 7;
      int rs = r < SQ ? r : SQ - 1;
      osrc[ii] = rs * (HD * 2) + ((cp ^ ((r >> 1) & 7)) << 4);
    }
#pragma unroll
    for (int ii = 0; ii < 2; ++ii) {
      int r = 8 * (w * 2 + ii) + (lane >> 3), cp = lane & 7;
      wsrc[ii] = r * (DM * 2) + ((cp ^ (((r >> 1) & 1) << 2)) << 4);
    }
    auto stage = [&](int t) {
      const int buf = t & 3;
      char* la = smem + buf * B_STRIDE + w * 5120;
      char* lb = smem + buf * B_STRIDE + B_BUFA + w * 2048;
      const char* ot = ob + t * 128;
      const char* wt = wob + (size_t)t * (64 * DM * 2);
#pragma unroll
      for (int ii = 0; ii < 5; ++ii) glds16(ot + osrc[ii], la + ii * 1024);
#pragma unroll
      for (int ii = 0; ii < 2; ++ii) glds16(wt + wsrc[ii], lb + ii * 1024);
    };
    stage(0); stage(1); stage(2);
    asm volatile("s_waitcnt vmcnt(14)" ::: "memory");
    for (int t = 0; t < NT; ++t) {
      RAW_BAR();
      if (t + 3 < NT) { stage(t + 3); asm volatile("s_waitcnt vmcnt(14)" ::: "memory"); }
      else if (t + 2 < NT) asm volatile("s_waitcnt vmcnt(7)" ::: "memory");
      else asm volatile("s_waitcnt vmcnt(0)" ::: "memory");
    }
    return;
  }
  f32x16 acc[2] = {f32x16{}, f32x16{}};
  f32x4 acc2 = f32x4{};
  const int sb = (int)(uintptr_t)smem;
  const int xa = sb + (32 * w + l31) * 128 + ((hh ^ ((l31 >> 1) & 7)) << 4);
  const int wa = sb + B_BUFA + (8 * hh + qp) * 128 + (((2 * blk + (p4 >> 1)) ^ ((qp >> 1) << 2)) << 4) + 8 * (p4 & 1);
  const int xa2 = sb + (128 + l15) * 128 + ((g ^ ((l15 >> 1) & 7)) << 4);
  const int wa2 = sb + B_BUFA + (8 * g + qp) * 128 + (((2 * w + (p4 >> 1)) ^ ((qp >> 1) << 2)) << 4) + 8 * (p4 & 1);
  auto body = [&](auto BC) {
    constexpr int buf = decltype(BC)::value;
    constexpr int OB = (buf & 1) * B_STRIDE;
    const int hi2 = (buf >> 1) * 2 * B_STRIDE;
    RAW_BAR();
    const int xa_ = xa + hi2, w0 = wa + hi2, w1 = (wa ^ 64) + hi2, xa2_ = xa2 + hi2, wa2_ = wa2 + hi2;
    bf16x8 xf[4], xr[2];
    s16x4 wl[4][2], wh[4][2], rl[2], rh[2];
    sfor<0, 4>([&](auto SC) {
      constexpr int s = decltype(SC)::value;
      xf[s] = rd128<OB>(xa_ ^ (s << 5));
      wl[s][0] = rdtr<OB + s * 2048>(w0); wh[s][0] = rdtr<OB + s * 2048 + 512>(w0);
      wl[s][1] = rdtr<OB + s * 2048>(w1); wh[s][1] = rdtr<OB + s * 2048 + 512>(w1);
      if constexpr (s >= 1) {
        asm volatile("s_waitcnt lgkmcnt(5)" : "+v"(xf[s - 1]), "+v"(wl[s - 1][0]), "+v"(wh[s - 1][0]), "+v"(wl[s - 1][1]), "+v"(wh[s - 1][1]) :: "memory");
        acc[0] = MFMA32(cat8(wl[s - 1][0], wh[s - 1][0]), xf[s - 1], acc[0]);
        acc[1] = MFMA32(cat8(wl[s - 1][1], wh[s - 1][1]), xf[s - 1], acc[1]);
      }
    });
    xr[0] = rd128<OB>(xa2_); rl[0] = rdtr<OB>(wa2_); rh[0] = rdtr<OB + 512>(wa2_);
    xr[1] = rd128<OB>(xa2_ ^ 64); rl[1] = rdtr<OB + 4096>(wa2_); rh[1] = rdtr<OB + 4096 + 512>(wa2_);
    asm volatile("s_waitcnt lgkmcnt(6)" : "+v"(xf[3]), "+v"(wl[3][0]), "+v"(wh[3][0]), "+v"(wl[3][1]), "+v"(wh[3][1]) :: "memory");
    acc[0] = MFMA32(cat8(wl[3][0], wh[3][0]), xf[3], acc[0]);
    acc[1] = MFMA32(cat8(wl[3][1], wh[3][1]), xf[3], acc[1]);
    asm volatile("s_waitcnt lgkmcnt(0)" : "+v"(xr[0]), "+v"(rl[0]), "+v"(rh[0]), "+v"(xr[1]), "+v"(rl[1]), "+v"(rh[1]) :: "memory");
    acc2 = MFMA16(cat8(rl[0], rh[0]), xr[0], acc2);
    acc2 = MFMA16(cat8(rl[1], rh[1]), xr[1], acc2);
  };
  for (int t = 0; t < NT; t += 4) {
    body(IC<0>{});
    body(IC<1>{});
    body(IC<2>{});
    body(IC<3>{});
  }
  {
    short* orow = out + (size_t)(b * SQ + 32 * w + l31) * DM + nt0 * 64 + 4 * hh;
#pragma unroll
    for (int nt = 0; nt < 2; ++nt)
#pragma unroll
      for (int i = 0; i < 4; ++i) {
        u32x2 pk = {cvtpk(acc[nt][4 * i], acc[nt][4 * i + 1]), cvtpk(acc[nt][4 * i + 2], acc[nt][4 * i + 3])};
        *(u32x2*)(orow + 32 * nt + 8 * i) = pk;
      }
    u32x2 pk = {cvtpk(acc2[0], acc2[1]), cvtpk(acc2[2], acc2[3])};
    *(u32x2*)(out + (size_t)(b * SQ + 128 + l15) * DM + nt0 * 64 + 16 * w + 4 * g) = pk;
  }
}

extern "C" void kernel_launch(void* const* d_in, const int* in_sizes, int n_in, void* d_out,
                              int out_size, void* d_ws, size_t ws_size, hipStream_t stream) {
  const short* x = (const short*)d_in[0];
  const short* kc = (const short*)d_in[1];
  const short* vc = (const short*)d_in[2];
  const short* wq = (const short*)d_in[3];
  const short* wo = (const short*)d_in[4];
  short* og = (short*)d_ws;
  short* out = (short*)d_out;
  unsigned* counter = (unsigned*)(og + (size_t)MROWS * HD);
  (void)hipFuncSetAttribute((const void*)attn_kernel, hipFuncAttributeMaxDynamicSharedMemorySize, A_LDS);
  (void)hipFuncSetAttribute((const void*)oproj_kernel, hipFuncAttributeMaxDynamicSharedMemorySize, B_LDS);
  attn_kernel<<<512, 256, A_LDS, stream>>>(x, wq, kc, vc, og, counter);
  oproj_kernel<<<256, 512, B_LDS, stream>>>(og, wo, out);
}
```

```cpp
#include <hip/hip_runtime.h>
#include <hip/hip_bf16.h>

typedef __attribute__((ext_vector_type(8))) short bf16x8;
typedef __attribute__((ext_vector_type(4))) short s16x4;
typedef __attribute__((ext_vector_type(16))) float f32x16;
typedef __attribute__((ext_vector_type(4))) float f32x4;
typedef __attribute__((ext_vector_type(4))) unsigned u32x4;
typedef __attribute__((ext_vector_type(2))) unsigned u32x2;

constexpr int NB = 32, SQ = 144, DM = 512, NH = 32, SK = 512, DH = 128;
constexpr int HD = NH * DH;
constexpr int MROWS = NB * SQ;

#define MFMA32(a, b, c) __builtin_amdgcn_mfma_f32_32x32x16_bf16((a), (b), (c), 0, 0, 0)
#define MFMA16(a, b, c) __builtin_amdgcn_mfma_f32_16x16x32_bf16((a), (b), (c), 0, 0, 0)
#define MFMA16K16(a, b, c) __builtin_amdgcn_mfma_f32_16x16x16bf16_1k((a), (b), (c), 0, 0, 0)

__device__ __forceinline__ unsigned cvtpk(float lo, float hi) {
  unsigned r;
  asm volatile("v_cvt_pk_bf16_f32 %0, %1, %2" : "=v"(r) : "v"(lo), "v"(hi));
  return r;
}
__device__ __forceinline__ bf16x8 cat8(s16x4 lo, s16x4 hi) {
  return __builtin_shufflevector(lo, hi, 0, 1, 2, 3, 4, 5, 6, 7);
}
__device__ __forceinline__ void glds16(const void* g, char* l) {
  __builtin_amdgcn_global_load_lds((const unsigned*)g, (unsigned*)l, 16, 0, 0);
}
__device__ __forceinline__ void glds16nt(const void* g, char* l) {
  __builtin_amdgcn_global_load_lds((const unsigned*)g, (unsigned*)l, 16, 0, 2);
}
__device__ __forceinline__ float xmax32(float v) {
  auto rr = __builtin_amdgcn_permlane32_swap(__float_as_uint(v), __float_as_uint(v), false, false);
  return fmaxf(__uint_as_float(rr[0]), __uint_as_float(rr[1]));
}
__device__ __forceinline__ float xsum32(float v) {
  auto rr = __builtin_amdgcn_permlane32_swap(__float_as_uint(v), __float_as_uint(v), false, false);
  return __uint_as_float(rr[0]) + __uint_as_float(rr[1]);
}
__device__ __forceinline__ float xmax16(float v) {
  auto rr = __builtin_amdgcn_permlane16_swap(__float_as_uint(v), __float_as_uint(v), false, false);
  return fmaxf(__uint_as_float(rr[0]), __uint_as_float(rr[1]));
}
__device__ __forceinline__ float xsum16(float v) {
  auto rr = __builtin_amdgcn_permlane16_swap(__float_as_uint(v), __float_as_uint(v), false, false);
  return __uint_as_float(rr[0]) + __uint_as_float(rr[1]);
}
template <int OFF> __device__ __forceinline__ bf16x8 rd128(int a) {
  bf16x8 r;
  asm volatile("ds_read_b128 %0, %1 offset:%2" : "=v"(r) : "v"(a), "i"(OFF) : "memory");
  return r;
}
template <int OFF> __device__ __forceinline__ s16x4 rdtr(int a) {
  s16x4 r;
  asm volatile("ds_read_b64_tr_b16 %0, %1 offset:%2" : "=v"(r) : "v"(a), "i"(OFF) : "memory");
  return r;
}
#define WAITL2(N, a, b) asm volatile("s_waitcnt lgkmcnt(" #N ")" : "+v"(a), "+v"(b) :: "memory")
#define WAITL4(N, a, b, c, d) asm volatile("s_waitcnt lgkmcnt(" #N ")" : "+v"(a), "+v"(b), "+v"(c), "+v"(d) :: "memory")
#define WAITL8(N, a, b, c, d, e, f, g_, h_) asm volatile("s_waitcnt lgkmcnt(" #N ")" : "+v"(a), "+v"(b), "+v"(c), "+v"(d), "+v"(e), "+v"(f), "+v"(g_), "+v"(h_) :: "memory")
#define RAW_BAR() __builtin_amdgcn_s_barrier()
#define SBAR() __builtin_amdgcn_sched_barrier(0)
#define OPAQUE(v) asm volatile("" : "+v"(v))
template <int N> struct IC { static constexpr int value = N; };
template <int I, int N, class F> __device__ __forceinline__ void sfor(F&& f) {
  if constexpr (I < N) { f(IC<I>{}); sfor<I + 1, N>(f); }
}

constexpr int A_BUFA = 20480;
constexpr int A_BUFB = 16384;
constexpr int A_OFF_A = 0;
constexpr int A_OFF_B = 2 * A_BUFA;
constexpr int A_OFF_Q2 = A_OFF_B + 2 * A_BUFB;
constexpr int A_OFF_SLOT = A_OFF_Q2 + 4096;
constexpr int A_LDS = A_OFF_SLOT + 16;

__global__ __launch_bounds__(256, 2) void attn_kernel(
    const short* __restrict__ x, const short* __restrict__ wq, const short* __restrict__ kc,
    const short* __restrict__ vc, short* __restrict__ og) {
  extern __shared__ __attribute__((aligned(16))) char smem[];
  const int tid = threadIdx.x, lane = tid & 63;
  const int w = __builtin_amdgcn_readfirstlane(tid >> 6);
  const int l31 = lane & 31, hh = lane >> 5, l15 = lane & 15, g = lane >> 4;
  const int qp = l15 >> 2, p4 = lane & 3, blk = (lane >> 4) & 1;
  const int sb = (int)(uintptr_t)smem;
  volatile unsigned* slot = (volatile unsigned*)(smem + A_OFF_SLOT);

  const unsigned ksrc = (16 * w + g) * 256 + ((l15 ^ g) << 4);
  const unsigned vsrc = (16 * w + g) * 256 + ((l15 ^ (g << 2)) << 4);
  auto stage_kv = [&](int pi, int t, int buf, unsigned ko, unsigned vo) {
    const char* kb = (const char*)kc + (size_t)pi * (SK * DH * 2) + t * 16384;
    const char* vb = (const char*)vc + (size_t)pi * (SK * DH * 2) + t * 16384;
#pragma unroll
    for (int i = 0; i < 4; ++i)
      glds16nt(kb + ((ko ^ (i << 6)) + i * 1024), smem + A_OFF_A + buf * A_BUFA + w * 4096 + i * 1024);
#pragma unroll
    for (int i = 0; i < 4; ++i)
      glds16nt(vb + (vo + i * 1024), smem + A_OFF_B + buf * A_BUFB + w * 4096 + i * 1024);
  };
  auto stage_q = [&](int pi, int t, int buf) {
    int ln = lane;
    OPAQUE(ln);
    const char* xt = (const char*)(x + (size_t)(pi >> 5) * SQ * DM) + t * 128;
    const char* wt = (const char*)(wq + (pi & 31) * DH) + (size_t)t * (64 * HD * 2);
#pragma unroll
    for (int ii = 0; ii < 5; ++ii) {
      int r = 8 * (w * 5 + ii) + (ln >> 3), cp = ln & 7;
      int rs = r < SQ ? r : SQ - 1;
      unsigned so = rs * 1024 + ((cp ^ ((r >> 1) & 7)) << 4);
      glds16(xt + so, smem + A_OFF_A + buf * A_BUFA + w * 5120 + ii * 1024);
    }
    const unsigned wo0 = (16 * w + (ln >> 4)) * (HD * 2) + (((ln & 15) ^ ((ln >> 4) << 2)) << 4);
#pragma unroll
    for (int ii = 0; ii < 4; ++ii)
      glds16(wt + (wo0 + ii * 4 * HD * 2), smem + A_OFF_B + buf * A_BUFB + w * 4096 + ii * 1024);
  };
  int pidx = (blockIdx.x & 7) * 128 + (blockIdx.x >> 3);
  stage_q(pidx, 0, 0);
  if (tid == 0) *slot = blockIdx.x + 512u;
  constexpr float CS = 0.088388347648318440f * 1.4426950408889634f;
  constexpr float THRS = 8.f / CS;

  for (;;) {
  const int b = pidx >> 5, h = pidx & 31;
  f32x16 acc[4];
#pragma unroll
  for (int i = 0; i < 4; ++i) acc[i] = f32x16{};
  f32x4 acc2[8];
#pragma unroll
  for (int i = 0; i < 8; ++i) acc2[i] = f32x4{};
  {
    int lq = lane;
    OPAQUE(lq);
    const int l31 = lq & 31, hh = lq >> 5, l15 = lq & 15, g = lq >> 4;
    const int qp = l15 >> 2, p4 = lq & 3, blk = (lq >> 4) & 1;
    const int xa = sb + A_OFF_A + (32 * w + l31) * 128 + ((hh ^ ((l31 >> 1) & 7)) << 4);
    const int wa = sb + A_OFF_B + (8 * hh + qp) * 256 + (((2 * blk + (p4 >> 1)) ^ (qp << 2)) << 4) + 8 * (p4 & 1);
    const int xa2 = sb + A_OFF_A + (128 + l15) * 128 + ((g ^ ((l15 >> 1) & 7)) << 4);
    const int wa2 = sb + A_OFF_B + (8 * g + qp) * 256 + (((4 * w + (p4 >> 1)) ^ (qp << 2)) << 4) + 8 * (p4 & 1);
    auto body = [&](auto BC, int t) {
      constexpr int buf = decltype(BC)::value;
      constexpr int OA = buf * A_BUFA, OB = buf * A_BUFB;
      asm volatile("s_waitcnt vmcnt(0) lgkmcnt(0)" ::: "memory");
      RAW_BAR();
      if (t < 7) stage_q(pidx, t + 1, buf ^ 1);
      else stage_kv(pidx, 0, buf ^ 1, ksrc, vsrc);
      SBAR();
      const int w0 = wa, w1 = wa ^ 64, w2 = wa ^ 128, w3 = wa ^ 192;
      bf16x8 xf[2];
      s16x4 wl[2][4], wh[2][4];
      bf16x8 xr[2];
      s16x4 rl[2][2], rh[2][2];
      auto issue = [&](auto SC) {
        constexpr int s = decltype(SC)::value, u = s & 1;
        xf[u] = rd128<OA>(xa ^ (s << 5));
        wl[u][0] = rdtr<OB + s * 4096>(w0); wh[u][0] = rdtr<OB + s * 4096 + 1024>(w0);
        wl[u][1] = rdtr<OB + s * 4096>(w1); wh[u][1] = rdtr<OB + s * 4096 + 1024>(w1);
        wl[u][2] = rdtr<OB + s * 4096>(w2); wh[u][2] = rdtr<OB + s * 4096 + 1024>(w2);
        wl[u][3] = rdtr<OB + s * 4096>(w3); wh[u][3] = rdtr<OB + s * 4096 + 1024>(w3);
      };
      issue(IC<0>{});
      sfor<0, 4>([&](auto SC) {
        constexpr int s = decltype(SC)::value, u = s & 1;
        if constexpr (s < 3) {
          issue(IC<s + 1>{});
          asm volatile("s_waitcnt lgkmcnt(9)" : "+v"(xf[u]), "+v"(wl[u][0]), "+v"(wh[u][0]), "+v"(wl[u][1]), "+v"(wh[u][1]),
                       "+v"(wl[u][2]), "+v"(wh[u][2]), "+v"(wl[u][3]), "+v"(wh[u][3]) :: "memory");
        } else {
          xr[0] = rd128<OA>(xa2); xr[1] = rd128<OA>(xa2 ^ 64);
          rl[0][0] = rdtr<OB>(wa2); rh[0][0] = rdtr<OB + 1024>(wa2);
          rl[0][1] = rdtr<OB>(wa2 ^ 32); rh[0][1] = rdtr<OB + 1024>(wa2 ^ 32);
          rl[1][0] = rdtr<OB + 8192>(wa2); rh[1][0] = rdtr<OB + 8192 + 1024>(wa2);
          rl[1][1] = rdtr<OB + 8192>(wa2 ^ 32); rh[1][1] = rdtr<OB + 8192 + 1024>(wa2 ^ 32);
          asm volatile("s_waitcnt lgkmcnt(10)" : "+v"(xf[u]), "+v"(wl[u][0]), "+v"(wh[u][0]), "+v"(wl[u][1]), "+v"(wh[u][1]),
                       "+v"(wl[u][2]), "+v"(wh[u][2]), "+v"(wl[u][3]), "+v"(wh[u][3]) :: "memory");
        }
#pragma unroll
        for (int dt = 0; dt < 4; ++dt) acc[dt] = MFMA32(cat8(wl[u][dt], wh[u][dt]), xf[u], acc[dt]);
        SBAR();
      });
      asm volatile("s_waitcnt lgkmcnt(0)" : "+v"(xr[0]), "+v"(xr[1]), "+v"(rl[0][0]), "+v"(rh[0][0]), "+v"(rl[0][1]), "+v"(rh[0][1]),
                   "+v"(rl[1][0]), "+v"(rh[1][0]), "+v"(rl[1][1]), "+v"(rh[1][1]) :: "memory");
#pragma unroll
      for (int s2 = 0; s2 < 2; ++s2)
#pragma unroll
        for (int e = 0; e < 2; ++e) acc2[e] = MFMA16(cat8(rl[s2][e], rh[s2][e]), xr[s2], acc2[e]);
    };
    for (int t = 0; t < 8; t += 2) {
      body(IC<0>{}, t);
      body(IC<1>{}, t + 1);
    }
  }
  bf16x8 qf[8];
#pragma unroll
  for (int dt = 0; dt < 4; ++dt) {
#pragma unroll
    for (int s = 0; s < 2; ++s) {
      unsigned a0 = cvtpk(acc[dt][8 * s + 0], acc[dt][8 * s + 1]);
      unsigned a1 = cvtpk(acc[dt][8 * s + 2], acc[dt][8 * s + 3]);
      unsigned b0 = cvtpk(acc[dt][8 * s + 4], acc[dt][8 * s + 5]);
      unsigned b1 = cvtpk(acc[dt][8 * s + 6], acc[dt][8 * s + 7]);
      auto r0 = __builtin_amdgcn_permlane32_swap(a0, b0, false, false);
      auto r1 = __builtin_amdgcn_permlane32_swap(a1, b1, false, false);
      u32x4 wv = {r0[0], r1[0], r0[1], r1[1]};
      qf[2 * dt + s] = __builtin_bit_cast(bf16x8, wv);
    }
  }
  {
#pragma unroll
    for (int e = 0; e < 2; ++e) {
      int dd = 2 * w + e;
      u32x2 pk = {cvtpk(acc2[e][0], acc2[e][1]), cvtpk(acc2[e][2], acc2[e][3])};
      int ch = (2 * dd + (g >> 1)) ^ l15;
      asm volatile("ds_write_b64 %0, %1" :: "v"(sb + A_OFF_Q2 + l15 * 256 + (ch << 4) + 8 * (g & 1)), "v"(pk) : "memory");
    }
  }
#pragma unroll
  for (int i = 0; i < 4; ++i) acc[i] = f32x16{};
#pragma unroll
  for (int i = 0; i < 8; ++i) acc2[i] = f32x4{};
  int nidx = 0;
  float m1 = -1e30f, l1 = 0.f, m2 = -1e30f, l2 = 0.f;
  {
    int ka = sb + A_OFF_A + l31 * 256 + ((hh ^ l15) << 4);
    int ka2 = sb + A_OFF_A + (16 * w + l15) * 256 + ((g ^ l15) << 4);
    int q2a = sb + A_OFF_Q2 + l15 * 256 + ((g ^ l15) << 4);
    int va = sb + A_OFF_B + (4 * hh + qp) * 256 + (((2 * blk + (p4 >> 1)) ^ (qp << 2)) << 4) + 8 * (p4 & 1);
    int va2 = sb + A_OFF_B + (16 * w + 4 * g + qp) * 256 + (((p4 >> 1) ^ (qp << 2)) << 4) + 8 * (p4 & 1);
    auto body = [&](auto BC, int t) {
      constexpr int buf = decltype(BC)::value;
      constexpr int OA = buf * A_BUFA, OB = buf * A_BUFB;
      asm volatile("s_waitcnt vmcnt(0) lgkmcnt(0)" ::: "memory");
      RAW_BAR();
      int ka_ = ka, ka2_ = ka2, q2a_ = q2a, va_ = va, va2_ = va2;
      unsigned ko = ksrc, vo = vsrc;
      OPAQUE(ka_); OPAQUE(ka2_); OPAQUE(q2a_); OPAQUE(va_); OPAQUE(va2_); OPAQUE(ko); OPAQUE(vo);
      if (t < 7) {
        stage_kv(pidx, t + 1, buf ^ 1, ko, vo);
      } else {
        const unsigned it = __builtin_amdgcn_readfirstlane(*slot);
        nidx = it < 1024u ? (int)((it & 7) * 128 + (it >> 3)) : -1;
        if (nidx >= 0) stage_q(nidx, 0, 0);
      }
      SBAR();
      f32x16 s0 = f32x16{}, s1 = f32x16{};
      f32x4 sr = f32x4{};
      bf16x8 kA[4], kB[4];
#pragma unroll
      for (int i = 0; i < 4; ++i) kA[i] = rd128<OA>(ka_ ^ (i << 5));
#pragma unroll
      for (int i = 0; i < 4; ++i) kB[i] = rd128<OA>(ka_ ^ ((4 + i) << 5));
      WAITL4(4, kA[0], kA[1], kA[2], kA[3]);
      s0 = MFMA32(kA[0], qf[0], s0);
      s0 = MFMA32(kA[1], qf[1], s0);
      SBAR();
      s0 = MFMA32(kA[2], qf[2], s0);
      s0 = MFMA32(kA[3], qf[3], s0);
      SBAR();
#pragma unroll
      for (int i = 0; i < 4; ++i) kA[i] = rd128<OA + 8192>(ka_ ^ (i << 5));
      WAITL4(4, kB[0], kB[1], kB[2], kB[3]);
      s0 = MFMA32(kB[0], qf[4], s0);
      s0 = MFMA32(kB[1], qf[5], s0);
      SBAR();
      s0 = MFMA32(kB[2], qf[6], s0);
      s0 = MFMA32(kB[3], qf[7], s0);
      SBAR();
#pragma unroll
      for (int i = 0; i < 4; ++i) kB[i] = rd128<OA + 8192>(ka_ ^ ((4 + i) << 5));
      WAITL4(4, kA[0], kA[1], kA[2], kA[3]);
      s1 = MFMA32(kA[0], qf[0], s1);
      SBAR();
      s1 = MFMA32(kA[1], qf[1], s1);
      float tmax = fmaxf(fmaxf(s0[0], s0[1]), s0[2]);
#pragma unroll
      for (int r = 3; r < 9; r += 2) tmax = fmaxf(fmaxf(tmax, s0[r]), s0[r + 1]);
      SBAR();
      s1 = MFMA32(kA[2], qf[2], s1);
#pragma unroll
      for (int r = 9; r < 15; r += 2) tmax = fmaxf(fmaxf(tmax, s0[r]), s0[r + 1]);
      tmax = fmaxf(tmax, s0[15]);
      tmax = xmax32(tmax);
      SBAR();
      s1 = MFMA32(kA[3], qf[3], s1);
      SBAR();
      if (!__all(tmax - m1 <= THRS)) {
        float mn0 = fmaxf(m1, tmax);
        float alpha = __builtin_amdgcn_exp2f((m1 - mn0) * CS);
        m1 = mn0;
        l1 *= alpha;
#pragma unroll
        for (int dt = 0; dt < 4; ++dt)
#pragma unroll
          for (int r = 0; r < 16; ++r) acc[dt][r] *= alpha;
      }
      const float mn = m1;
      bf16x8 kr[4], qr[4];
#pragma unroll
      for (int ks = 0; ks < 4; ++ks) { kr[ks] = rd128<OA>(ka2_ ^ (ks << 6)); qr[ks] = rd128<0>(q2a_ ^ (ks << 6)); }
      WAITL4(8, kB[0], kB[1], kB[2], kB[3]);
      bf16x8 pf[4];
      float mc = -mn * CS;
      float ps = 0.f;
      float e[8];
      s1 = MFMA32(kB[0], qf[4], s1);
#pragma unroll
      for (int j = 0; j < 4; ++j) { e[j] = __builtin_amdgcn_exp2f(fmaf(s0[j], CS, mc)); ps += e[j]; }
      SBAR();
      s1 = MFMA32(kB[1], qf[5], s1);
#pragma unroll
      for (int j = 4; j < 8; ++j) { e[j] = __builtin_amdgcn_exp2f(fmaf(s0[j], CS, mc)); ps += e[j]; }
      { u32x4 a = {cvtpk(e[0], e[1]), cvtpk(e[2], e[3]), cvtpk(e[4], e[5]), cvtpk(e[6], e[7])}; pf[0] = __builtin_bit_cast(bf16x8, a); }
      SBAR();
      s1 = MFMA32(kB[2], qf[6], s1);
#pragma unroll
      for (int j = 0; j < 4; ++j) { e[j] = __builtin_amdgcn_exp2f(fmaf(s0[8 + j], CS, mc)); ps += e[j]; }
      SBAR();
      s1 = MFMA32(kB[3], qf[7], s1);
#pragma unroll
      for (int j = 4; j < 8; ++j) { e[j] = __builtin_amdgcn_exp2f(fmaf(s0[8 + j], CS, mc)); ps += e[j]; }
      { u32x4 a = {cvtpk(e[0], e[1]), cvtpk(e[2], e[3]), cvtpk(e[4], e[5]), cvtpk(e[6], e[7])}; pf[1] = __builtin_bit_cast(bf16x8, a); }
      l1 += ps;
      OPAQUE(l1);
      SBAR();
      SBAR();
      const int v0 = va_, v1 = va_ ^ 64, v2 = va_ ^ 128, v3 = va_ ^ 192;
      s16x4 vl[2][4], vh[2][4];
      auto issue_v = [&](auto SC) {
        constexpr int s = decltype(SC)::value, u = s & 1;
        vl[u][0] = rdtr<OB + s * 4096>(v0); vh[u][0] = rdtr<OB + s * 4096 + 2048>(v0);
        vl[u][1] = rdtr<OB + s * 4096>(v1); vh[u][1] = rdtr<OB + s * 4096 + 2048>(v1);
        vl[u][2] = rdtr<OB + s * 4096>(v2); vh[u][2] = rdtr<OB + s * 4096 + 2048>(v2);
        vl[u][3] = rdtr<OB + s * 4096>(v3); vh[u][3] = rdtr<OB + s * 4096 + 2048>(v3);
      };
      issue_v(IC<0>{});
      WAITL8(8, kr[0], kr[1], kr[2], kr[3], qr[0], qr[1], qr[2], qr[3]);
#pragma unroll
      for (int ks = 0; ks < 4; ++ks) sr = MFMA16(kr[ks], qr[ks], sr);
      SBAR();
      SBAR();
      issue_v(IC<1>{});
      WAITL8(8, vl[0][0], vh[0][0], vl[0][1], vh[0][1], vl[0][2], vh[0][2], vl[0][3], vh[0][3]);
      acc[0] = MFMA32(cat8(vl[0][0], vh[0][0]), pf[0], acc[0]);
      float tm1 = fmaxf(fmaxf(s1[0], s1[1]), s1[2]);
#pragma unroll
      for (int r = 3; r < 9; r += 2) tm1 = fmaxf(fmaxf(tm1, s1[r]), s1[r + 1]);
      SBAR();
      acc[1] = MFMA32(cat8(vl[0][1], vh[0][1]), pf[0], acc[1]);
#pragma unroll
      for (int r = 9; r < 15; r += 2) tm1 = fmaxf(fmaxf(tm1, s1[r]), s1[r + 1]);
      tm1 = fmaxf(tm1, s1[15]);
      tm1 = xmax32(tm1);
      const bool keepb = __all(tm1 - m1 <= THRS);
      float mnb = keepb ? m1 : fmaxf(m1, tm1);
      float alphab = keepb ? 1.f : __builtin_amdgcn_exp2f((m1 - mnb) * CS);
      m1 = mnb;
      mc = -mnb * CS;
      ps = 0.f;
      SBAR();
      acc[2] = MFMA32(cat8(vl[0][2], vh[0][2]), pf[0], acc[2]);
#pragma unroll
      for (int j = 0; j < 4; ++j) { e[j] = __builtin_amdgcn_exp2f(fmaf(s1[j], CS, mc)); ps += e[j]; }
      SBAR();
      acc[3] = MFMA32(cat8(vl[0][3], vh[0][3]), pf[0], acc[3]);
#pragma unroll
      for (int j = 4; j < 8; ++j) { e[j] = __builtin_amdgcn_exp2f(fmaf(s1[j], CS, mc)); ps += e[j]; }
      { u32x4 a = {cvtpk(e[0], e[1]), cvtpk(e[2], e[3]), cvtpk(e[4], e[5]), cvtpk(e[6], e[7])}; pf[2] = __builtin_bit_cast(bf16x8, a); }
      SBAR();
      issue_v(IC<2>{});
      WAITL8(8, vl[1][0], vh[1][0], vl[1][1], vh[1][1], vl[1][2], vh[1][2], vl[1][3], vh[1][3]);
      acc[0] = MFMA32(cat8(vl[1][0], vh[1][0]), pf[1], acc[0]);
#pragma unroll
      for (int j = 0; j < 4; ++j) { e[j] = __builtin_amdgcn_exp2f(fmaf(s1[8 + j], CS, mc)); ps += e[j]; }
      SBAR();
      acc[1] = MFMA32(cat8(vl[1][1], vh[1][1]), pf[1], acc[1]);
#pragma unroll
      for (int j = 4; j < 8; ++j) { e[j] = __builtin_amdgcn_exp2f(fmaf(s1[8 + j], CS, mc)); ps += e[j]; }
      { u32x4 a = {cvtpk(e[0], e[1]), cvtpk(e[2], e[3]), cvtpk(e[4], e[5]), cvtpk(e[6], e[7])}; pf[3] = __builtin_bit_cast(bf16x8, a); }
      l1 = l1 * alphab + ps;
      OPAQUE(l1);
      SBAR();
      acc[2] = MFMA32(cat8(vl[1][2], vh[1][2]), pf[1], acc[2]);
      float t2 = fmaxf(fmaxf(sr[0], sr[1]), fmaxf(sr[2], sr[3]));
      t2 = xmax16(t2);
      t2 = xmax32(t2);
      const bool keep2 = __all(t2 - m2 <= THRS);
      float mn2 = keep2 ? m2 : fmaxf(m2, t2);
      float alpha2 = keep2 ? 1.f : __builtin_amdgcn_exp2f((m2 - mn2) * CS);
      m2 = mn2;
      SBAR();
      acc[3] = MFMA32(cat8(vl[1][3], vh[1][3]), pf[1], acc[3]);
      const float mc2 = -mn2 * CS;
      float e0 = __builtin_amdgcn_exp2f(fmaf(sr[0], CS, mc2));
      float e1 = __builtin_amdgcn_exp2f(fmaf(sr[1], CS, mc2));
      float e2 = __builtin_amdgcn_exp2f(fmaf(sr[2], CS, mc2));
      float e3 = __builtin_amdgcn_exp2f(fmaf(sr[3], CS, mc2));
      l2 = l2 * alpha2 + (e0 + e1) + (e2 + e3);
      OPAQUE(l2);
      u32x2 p2u = {cvtpk(e0, e1), cvtpk(e2, e3)};
      s16x4 p2 = __builtin_bit_cast(s16x4, p2u);
      SBAR();
      if (!keepb) {
#pragma unroll
        for (int dt = 0; dt < 4; ++dt)
#pragma unroll
          for (int r = 0; r < 16; ++r) acc[dt][r] *= alphab;
      }
      if (!keep2) {
#pragma unroll
        for (int dd = 0; dd < 8; ++dd)
#pragma unroll
          for (int r = 0; r < 4; ++r) acc2[dd][r] *= alpha2;
      }
      issue_v(IC<3>{});
      WAITL8(8, vl[0][0], vh[0][0], vl[0][1], vh[0][1], vl[0][2], vh[0][2], vl[0][3], vh[0][3]);
#pragma unroll
      for (int dt = 0; dt < 4; ++dt) acc[dt] = MFMA32(cat8(vl[0][dt], vh[0][dt]), pf[2], acc[dt]);
      SBAR();
      s16x4 vr[8];
#pragma unroll
      for (int dd = 0; dd < 8; ++dd) vr[dd] = rdtr<OB>(va2_ ^ (dd << 5));
      WAITL8(8, vl[1][0], vh[1][0], vl[1][1], vh[1][1], vl[1][2], vh[1][2], vl[1][3], vh[1][3]);
#pragma unroll
      for (int dt = 0; dt < 4; ++dt) acc[dt] = MFMA32(cat8(vl[1][dt], vh[1][dt]), pf[3], acc[dt]);
      WAITL8(0, vr[0], vr[1], vr[2], vr[3], vr[4], vr[5], vr[6], vr[7]);
#pragma unroll
      for (int dd = 0; dd < 8; ++dd) acc2[dd] = MFMA16K16(vr[dd], p2, acc2[dd]);
    };
    for (int t = 0; t < 8; t += 2) {
      body(IC<0>{}, t);
      body(IC<1>{}, t + 1);
    }
  }

  int tz = threadIdx.x;
  OPAQUE(tz);
  asm volatile("s_waitcnt lgkmcnt(0)" ::: "memory");
  RAW_BAR();
  {
    const int l15 = tz & 15, g = (tz >> 4) & 3;
    float lt2 = xsum32(xsum16(l2));
    float* pw = (float*)(smem + ((w & 1) ? A_OFF_B + A_BUFB : A_OFF_A + A_BUFA) + (w >> 1) * 8192);
#pragma unroll
    for (int dd = 0; dd < 8; ++dd) *(f32x4*)(pw + l15 * 128 + 16 * dd + 4 * g) = acc2[dd];
    float* st = (float*)(smem + A_OFF_Q2);
    if (g == 0) { st[w * 32 + l15] = m2; st[w * 32 + 16 + l15] = lt2; }
  }
  asm volatile("s_waitcnt lgkmcnt(0)" ::: "memory");
  RAW_BAR();
  {
    const int q = tz >> 4, d0 = (tz & 15) * 8;
    const float* st = (const float*)(smem + A_OFF_Q2);
    float mm = fmaxf(fmaxf(st[q], st[32 + q]), fmaxf(st[64 + q], st[96 + q]));
    float o[8] = {0, 0, 0, 0, 0, 0, 0, 0};
    float ls = 0.f;
#pragma unroll
    for (int ww = 0; ww < 4; ++ww) {
      float sc = __builtin_amdgcn_exp2f((st[ww * 32 + q] - mm) * CS);
      ls += st[ww * 32 + 16 + q] * sc;
      const float* pw = (const float*)(smem + ((ww & 1) ? A_OFF_B + A_BUFB : A_OFF_A + A_BUFA) + (ww >> 1) * 8192) + q * 128 + d0;
      f32x4 a = *(const f32x4*)pw, c = *(const f32x4*)(pw + 4);
#pragma unroll
      for (int j = 0; j < 4; ++j) { o[j] += a[j] * sc; o[4 + j] += c[j] * sc; }
    }
    float inv = 1.f / ls;
    u32x4 pk = {cvtpk(o[0] * inv, o[1] * inv), cvtpk(o[2] * inv, o[3] * inv),
                cvtpk(o[4] * inv, o[5] * inv), cvtpk(o[6] * inv, o[7] * inv)};
    *(u32x4*)(og + (size_t)(b * SQ + 128 + q) * HD + h * DH + d0) = pk;
  }
  asm volatile("s_waitcnt lgkmcnt(0)" ::: "memory");
  RAW_BAR();
  {
    const int l31 = tz & 31, hh = (tz >> 5) & 1;
    float lt = xsum32(l1);
    float inv = 1.f / lt;
    char* ob = smem + (w < 2 ? A_OFF_A + A_BUFA : A_OFF_B + A_BUFB) + ((w & 1) * 32 + l31) * 256 + 8 * hh;
#pragma unroll
    for (int dt = 0; dt < 4; ++dt)
#pragma unroll
      for (int i = 0; i < 4; ++i) {
        u32x2 pk = {cvtpk(acc[dt][4 * i] * inv, acc[dt][4 * i + 1] * inv),
                    cvtpk(acc[dt][4 * i + 2] * inv, acc[dt][4 * i + 3] * inv)};
        *(u32x2*)(ob + (((4 * dt + i) ^ (l31 & 15)) << 4)) = pk;
      }
    if (tz == 0 && nidx >= 0) *slot = 4096u;
  }
  asm volatile("s_waitcnt lgkmcnt(0)" ::: "memory");
  RAW_BAR();
  {
    const int c = tz & 15, r0 = tz >> 4;
    short* orow = og + (size_t)(b * SQ + r0) * HD + h * DH + c * 8;
#pragma unroll
    for (int j = 0; j < 8; ++j) {
      const int r = r0 + 16 * j;
      const char* src = smem + (j < 4 ? A_OFF_A + A_BUFA + r * 256 : A_OFF_B + A_BUFB + (r - 64) * 256) + ((c ^ r0) << 4);
      bf16x8 v = *(const bf16x8*)src;
      *(bf16x8*)(orow + (size_t)(16 * j) * HD) = v;
    }
  }
  if (nidx < 0) break;
  pidx = nidx;
  }
}

constexpr int B_NST = 4;
constexpr int B_BUFA = 20480;
constexpr int B_BUFB = 8192;
constexpr int B_STRIDE = B_BUFA + B_BUFB;
constexpr int B_LDS = B_NST * B_STRIDE;

__global__ __launch_bounds__(576, 3) void oproj_kernel(const short* __restrict__ og,
                                                        const short* __restrict__ wo,
                                                        short* __restrict__ out) {
  extern __shared__ __attribute__((aligned(16))) char smem[];
  const int tid = threadIdx.x, lane = tid & 63;
  const int wv = __builtin_amdgcn_readfirstlane(tid >> 6);
  const int w = wv & 3;
  const int xg = blockIdx.x & 7, idx = blockIdx.x >> 3;
  const int b = xg * 4 + (idx >> 3), nt0 = idx & 7;
  const int l31 = lane & 31, hh = lane >> 5, l15 = lane & 15, g = lane >> 4;
  const int qp = l15 >> 2, p4 = lane & 3, blk = (lane >> 4) & 1;
  constexpr int NT = HD / 64;
  if (wv == 8) {
    constexpr int PFD = 12;
    const char* ob = (const char*)(og + (size_t)b * SQ * HD);
    const char* wob = (const char*)(wo + nt0 * 64);
    unsigned pv0 = 0, pv1 = 0, pv2 = 0, pv3 = 0;
    for (int t = -PFD; t < NT; ++t) {
      if (t >= 0) RAW_BAR();
      const int tp = t + PFD;
      if (tp < NT) {
        if ((tp & 7) == nt0) {
          const char* p0 = ob + (size_t)lane * (HD * 2) + tp * 128;
          const int r2 = lane + 128 > SQ - 1 ? SQ - 1 : lane + 128;
          asm volatile("global_load_dword %0, %1, off" : "+v"(pv0) : "v"(p0) : "memory");
          asm volatile("global_load_dword %0, %1, off" : "+v"(pv1) : "v"(p0 + (size_t)64 * (HD * 2)) : "memory");
          asm volatile("global_load_dword %0, %1, off" : "+v"(pv2) : "v"(ob + (size_t)r2 * (HD * 2) + tp * 128) : "memory");
        }
        if ((tp & 3) == (b & 3)) {
          asm volatile("global_load_dword %0, %1, off" : "+v"(pv3) : "v"(wob + (size_t)(tp * 64 + lane) * (DM * 2)) : "memory");
        }
      }
      asm volatile("s_waitcnt vmcnt(8)" ::: "memory");
    }
    asm volatile("s_waitcnt vmcnt(0)" : "+v"(pv0), "+v"(pv1), "+v"(pv2), "+v"(pv3) :: "memory");
    return;
  }
  if (wv >= 4) {
    const char* ob = (const char*)(og + (size_t)b * SQ * HD);
    const char* wob = (const char*)(wo + nt0 * 64);
    unsigned osrc[5], wsrc[2];
#pragma unroll
    for (int ii = 0; ii < 5; ++ii) {
      int r = 8 * (w * 5 + ii) + (lane >> 3), cp = lane & 7;
      int rs = r < SQ ? r : SQ - 1;
      osrc[ii] = rs * (HD * 2) + ((cp ^ ((r >> 1) & 7)) << 4);
    }
#pragma unroll
    for (int ii = 0; ii < 2; ++ii) {
      int r = 8 * (w * 2 + ii) + (lane >> 3), cp = lane & 7;
      wsrc[ii] = r * (DM * 2) + ((cp ^ (((r >> 1) & 1) << 2)) << 4);
    }
    auto stage = [&](int t) {
      const int buf = t & 3;
      char* la = smem + buf * B_STRIDE + w * 5120;
      char* lb = smem + buf * B_STRIDE + B_BUFA + w * 2048;
      const char* ot = ob + t * 128;
      const char* wt = wob + (size_t)t * (64 * DM * 2);
#pragma unroll
      for (int ii = 0; ii < 5; ++ii) glds16(ot + osrc[ii], la + ii * 1024);
#pragma unroll
      for (int ii = 0; ii < 2; ++ii) glds16(wt + wsrc[ii], lb + ii * 1024);
    };
    stage(0); stage(1); stage(2);
    asm volatile("s_waitcnt vmcnt(14)" ::: "memory");
    for (int t = 0; t < NT; ++t) {
      RAW_BAR();
      if (t + 3 < NT) { stage(t + 3); asm volatile("s_waitcnt vmcnt(14)" ::: "memory"); }
      else if (t + 2 < NT) asm volatile("s_waitcnt vmcnt(7)" ::: "memory");
      else asm volatile("s_waitcnt vmcnt(0)" ::: "memory");
    }
    return;
  }
  f32x16 acc[2] = {f32x16{}, f32x16{}};
  f32x4 acc2 = f32x4{};
  const int sb = (int)(uintptr_t)smem;
  const int xa = sb + (32 * w + l31) * 128 + ((hh ^ ((l31 >> 1) & 7)) << 4);
  const int wa = sb + B_BUFA + (8 * hh + qp) * 128 + (((2 * blk + (p4 >> 1)) ^ ((qp >> 1) << 2)) << 4) + 8 * (p4 & 1);
  const int xa2 = sb + (128 + l15) * 128 + ((g ^ ((l15 >> 1) & 7)) << 4);
  const int wa2 = sb + B_BUFA + (8 * g + qp) * 128 + (((2 * w + (p4 >> 1)) ^ ((qp >> 1) << 2)) << 4) + 8 * (p4 & 1);
  auto body = [&](auto BC) {
    constexpr int buf = decltype(BC)::value;
    constexpr int OB = (buf & 1) * B_STRIDE;
    const int hi2 = (buf >> 1) * 2 * B_STRIDE;
    RAW_BAR();
    const int xa_ = xa + hi2, w0 = wa + hi2, w1 = (wa ^ 64) + hi2, xa2_ = xa2 + hi2, wa2_ = wa2 + hi2;
    bf16x8 xf[4], xr[2];
    s16x4 wl[4][2], wh[4][2], rl[2], rh[2];
    sfor<0, 4>([&](auto SC) {
      constexpr int s = decltype(SC)::value;
      xf[s] = rd128<OB>(xa_ ^ (s << 5));
      wl[s][0] = rdtr<OB + s * 2048>(w0); wh[s][0] = rdtr<OB + s * 2048 + 512>(w0);
      wl[s][1] = rdtr<OB + s * 2048>(w1); wh[s][1] = rdtr<OB + s * 2048 + 512>(w1);
      if constexpr (s >= 1) {
        asm volatile("s_waitcnt lgkmcnt(5)" : "+v"(xf[s - 1]), "+v"(wl[s - 1][0]), "+v"(wh[s - 1][0]), "+v"(wl[s - 1][1]), "+v"(wh[s - 1][1]) :: "memory");
        acc[0] = MFMA32(cat8(wl[s - 1][0], wh[s - 1][0]), xf[s - 1], acc[0]);
        acc[1] = MFMA32(cat8(wl[s - 1][1], wh[s - 1][1]), xf[s - 1], acc[1]);
      }
    });
    xr[0] = rd128<OB>(xa2_); rl[0] = rdtr<OB>(wa2_); rh[0] = rdtr<OB + 512>(wa2_);
    xr[1] = rd128<OB>(xa2_ ^ 64); rl[1] = rdtr<OB + 4096>(wa2_); rh[1] = rdtr<OB + 4096 + 512>(wa2_);
    asm volatile("s_waitcnt lgkmcnt(6)" : "+v"(xf[3]), "+v"(wl[3][0]), "+v"(wh[3][0]), "+v"(wl[3][1]), "+v"(wh[3][1]) :: "memory");
    acc[0] = MFMA32(cat8(wl[3][0], wh[3][0]), xf[3], acc[0]);
    acc[1] = MFMA32(cat8(wl[3][1], wh[3][1]), xf[3], acc[1]);
    asm volatile("s_waitcnt lgkmcnt(0)" : "+v"(xr[0]), "+v"(rl[0]), "+v"(rh[0]), "+v"(xr[1]), "+v"(rl[1]), "+v"(rh[1]) :: "memory");
    acc2 = MFMA16(cat8(rl[0], rh[0]), xr[0], acc2);
    acc2 = MFMA16(cat8(rl[1], rh[1]), xr[1], acc2);
  };
  for (int t = 0; t < NT; t += 4) {
    body(IC<0>{});
    body(IC<1>{});
    body(IC<2>{});
    body(IC<3>{});
  }
  {
    short* orow = out + (size_t)(b * SQ + 32 * w + l31) * DM + nt0 * 64 + 4 * hh;
#pragma unroll
    for (int nt = 0; nt < 2; ++nt)
#pragma unroll
      for (int i = 0; i < 4; ++i) {
        u32x2 pk = {cvtpk(acc[nt][4 * i], acc[nt][4 * i + 1]), cvtpk(acc[nt][4 * i + 2], acc[nt][4 * i + 3])};
        *(u32x2*)(orow + 32 * nt + 8 * i) = pk;
      }
    u32x2 pk = {cvtpk(acc2[0], acc2[1]), cvtpk(acc2[2], acc2[3])};
    *(u32x2*)(out + (size_t)(b * SQ + 128 + l15) * DM + nt0 * 64 + 16 * w + 4 * g) = pk;
  }
}

extern "C" void kernel_launch(void* const* d_in, const int* in_sizes, int n_in, void* d_out,
                              int out_size, void* d_ws, size_t ws_size, hipStream_t stream) {
  const short* x = (const short*)d_in[0];
  const short* kc = (const short*)d_in[1];
  const short* vc = (const short*)d_in[2];
  const short* wq = (const short*)d_in[3];
  const short* wo = (const short*)d_in[4];
  short* og = (short*)d_ws;
  short* out = (short*)d_out;
  (void)hipFuncSetAttribute((const void*)attn_kernel, hipFuncAttributeMaxDynamicSharedMemorySize, A_LDS);
  (void)hipFuncSetAttribute((const void*)oproj_kernel, hipFuncAttributeMaxDynamicSharedMemorySize, B_LDS);
  attn_kernel<<<512, 256, A_LDS, stream>>>(x, wq, kc, vc, og);
  oproj_kernel<<<256, 576, B_LDS, stream>>>(og, wo, out);
}
```
